```python
import torch

def _softmax(x, axis=-1):
    return torch.softmax(x, dim=axis)
def _gelu(x):
    return torch.nn.functional.gelu(x, approximate="tanh")
def _silu(x):
    return torch.nn.functional.silu(x)
def _sigmoid(x):
    return torch.sigmoid(x)
def _layernorm(x, axis=-1, eps=1e-5):
    m = x.mean(dim=axis, keepdim=True)
    v = x.var(dim=axis, keepdim=True, unbiased=False)
    return (x - m) / torch.sqrt(v + eps)
def _scatter_add(tgt, idx, src, axis=0):
    return tgt.clone().index_add_(axis, idx, src)
def _segment_sum(src, seg, n):
    out = torch.zeros((n,) + src.shape[1:], dtype=src.dtype)
    return out.index_add_(0, seg, src)
def _pad(x, pw):
    flat = tuple(v for pair in reversed(pw) for v in pair)
    return torch.nn.functional.pad(x, flat)

kernel_name = 'standalone__gqa_block_cached__near_ridge'

def setup_inputs():

    _w = (lambda a: (lambda t: t.to(torch.bfloat16) if t.is_floating_point() else t)(torch.as_tensor(a)))
    B, Sq, D = 8, 240, 640
    Hq, Hkv, Sk, Dh = 32, 32, 1024, 192
    assert Hq % Hkv == 0, (Hq, Hkv)
    return {
        "x": _w((lambda *s: torch.randn(*s, dtype=torch.bfloat16) * 0.8)(B, Sq, D)),
        "k_cache": _w((lambda *s: torch.randn(*s, dtype=torch.bfloat16) * 0.8)(B, Hkv, Sk, Dh)),
        "v_cache": _w((lambda *s: torch.randn(*s, dtype=torch.bfloat16) * 0.05)(B, Hkv, Sk, Dh)),
        "wq": _w((lambda *s: torch.randn(*s, dtype=torch.bfloat16) * 0.8)(D, Hq * Dh) * (D ** -0.5)),
        "wo": _w((lambda *s: torch.randn(*s, dtype=torch.bfloat16) * 0.05)(Hq * Dh, D) * ((Hq * Dh) ** -0.5)),
    }

def reference(x, k_cache, v_cache, wq, wo):
    B, Sq, D = x.shape
    Hq, Dh = 32, 192
    Hkv = k_cache.shape[1]
    rep = Hq // Hkv
    q = (lambda x, *d: x.permute(*d))((x @ wq).reshape(B, Sq, Hq, Dh), 0, 2, 1, 3)
    k = (lambda x, n, axis: torch.repeat_interleave(x, n, dim=axis))(k_cache, rep, axis=1)
    v = (lambda x, n, axis: torch.repeat_interleave(x, n, dim=axis))(v_cache, rep, axis=1)
    scores = torch.einsum("bhqd,bhkd->bhqk", q, k) * (Dh ** -0.5)
    p = _softmax(scores, axis=-1)
    o = torch.einsum("bhqk,bhkd->bhqd", p, v)
    return (lambda x, *d: x.permute(*d))(o, 0, 2, 1, 3).reshape(B, Sq, Hq * Dh) @ wo
```

```cpp
#include <hip/hip_runtime.h>
#include <stdint.h>

typedef unsigned short u16;

__global__ __launch_bounds__(256, 1) void k_fused(const u16* x, const u16* k, const u16* v, const u16* wq, const u16* wo, u16* o, u16* out) {
  extern __shared__ __attribute__((aligned(16))) char lds[];
  asm volatile(
"s_mov_b32 s91, 0\n"
"v_mov_b32 v247, 59768832\n"
"global_load_dwordx2 v[248:249], v247, %[o] sc1\n"
"v_lshrrev_b32 v5, 6, %[tid]\n"
"v_and_b32 v4, 63, %[tid]\n"
"s_and_b32 s61, %[blk], 7\n"
"v_readfirstlane_b32 s40, v5\n"
"s_lshr_b32 s62, %[blk], 3\n"
"s_lshr_b32 s67, s61, 2\n"
"s_lshl_b32 s67, s67, 2\n"
"s_and_b32 s63, s62, 3\n"
"s_add_u32 s67, s67, s63\n"
"s_and_b32 s66, s61, 3\n"
"s_lshl_b32 s66, s66, 3\n"
"s_lshr_b32 s63, s62, 2\n"
"s_add_u32 s66, s66, s63\n"
"s_mul_i32 s62, s67, 307200\n"
"s_mov_b64 s[28:29], %[x]\n"
"s_add_u32 s28, s28, s62\n"
"s_addc_u32 s29, s29, 0\n"
"s_and_b32 s29, s29, 0xffff\n"
"s_mov_b32 s30, 307200\n"
"s_mov_b32 s31, 0x20000\n"
"v_lshrrev_b32 v5, 3, v4\n"
"v_and_b32 v11, 7, v4\n"
"v_add_u32 v6, 0, v5\n"
"v_bfe_u32 v8, v6, 2, 1\n"
"v_bfe_u32 v9, v6, 3, 1\n"
"v_xor_b32 v8, v8, v9\n"
"v_lshl_or_b32 v9, v8, 2, v9\n"
"v_bfe_u32 v8, v6, 1, 1\n"
"v_lshl_or_b32 v7, v8, 1, v9\n"
"v_xor_b32 v7, v7, v11\n"
"v_lshlrev_b32 v7, 4, v7\n"
"v_mul_u32_u24 v8, 1280, v5\n"
"v_add_u32 v7, v7, v8\n"
"v_add_u32 v216, 0, v7\n"
"v_add_u32 v218, 18432, v7\n"
"v_add_u32 v220, 40960, v7\n"
"v_add_u32 v222, 59392, v7\n"
"v_add_u32 v6, 8, v5\n"
"v_bfe_u32 v8, v6, 2, 1\n"
"v_bfe_u32 v9, v6, 3, 1\n"
"v_xor_b32 v8, v8, v9\n"
"v_lshl_or_b32 v9, v8, 2, v9\n"
"v_bfe_u32 v8, v6, 1, 1\n"
"v_lshl_or_b32 v7, v8, 1, v9\n"
"v_xor_b32 v7, v7, v11\n"
"v_lshlrev_b32 v7, 4, v7\n"
"v_mul_u32_u24 v8, 1280, v5\n"
"v_add_u32 v7, v7, v8\n"
"v_add_u32 v217, 9216, v7\n"
"v_add_u32 v219, 27648, v7\n"
"v_add_u32 v221, 50176, v7\n"
"v_add_u32 v223, 68608, v7\n"
"s_mul_i32 s46, s40, 81920\n"
"s_mul_i32 s47, s40, 196608\n"
"s_and_b32 s62, s66, 7\n"
"s_and_b32 s63, s67, 3\n"
"s_xor_b32 s59, s62, s63\n"
"s_and_b32 s59, s59, 1\n"
"s_mul_i32 s59, s59, 5\n"
"s_lshl_b32 s50, s59, 7\n"
"s_add_u32 s50, s50, s46\n"
"s_mul_i32 s51, s59, 786432\n"
"s_add_u32 s51, s51, s47\n"
"s_lshl_b32 s61, s40, 13\n"
"s_add_u32 s52, s61, 122880\n"
"s_add_u32 s53, s61, 98304\n"
"s_cmp_eq_u32 s40, 3\n"
"s_cselect_b32 s53, 155648, s53\n"
"s_mov_b32 m0, s52\n"
"s_nop 0\n"
"buffer_load_dwordx4 v216, s[28:31], s50 offen offset:0 lds\n"
"buffer_load_dwordx4 v217, s[28:31], s50 offen offset:1024 lds\n"
"buffer_load_dwordx4 v218, s[28:31], s50 offen offset:2048 lds\n"
"buffer_load_dwordx4 v219, s[28:31], s50 offen offset:3072 lds\n"
"s_add_u32 m0, s52, 4096\n"
"s_nop 0\n"
"buffer_load_dwordx4 v220, s[28:31], s50 offen offset:0 lds\n"
"buffer_load_dwordx4 v221, s[28:31], s50 offen offset:1024 lds\n"
"buffer_load_dwordx4 v222, s[28:31], s50 offen offset:2048 lds\n"
"buffer_load_dwordx4 v223, s[28:31], s50 offen offset:3072 lds\n"
"s_mul_i32 s62, s66, 384\n"
"s_sub_u32 s63, 7864320, s62\n"
"s_mov_b64 s[32:33], %[wq]\n"
"s_add_u32 s32, s32, s62\n"
"s_addc_u32 s33, s33, 0\n"
"s_and_b32 s33, s33, 0xffff\n"
"s_mov_b32 s34, s63\n"
"s_mov_b32 s35, 0x20000\n"
"s_mov_b32 s64, 0xAAAAAAB\n"
"v_add_u32 v5, 0, v4\n"
"v_mul_hi_u32 v6, v5, s64\n"
"v_lshlrev_b32 v9, 4, v5\n"
"v_bfe_u32 v8, v6, 3, 1\n"
"v_bfe_u32 v7, v6, 1, 1\n"
"v_lshl_or_b32 v7, v8, 2, v7\n"
"v_mul_u32_u24 v10, 11904, v6\n"
"v_lshlrev_b32 v7, 4, v7\n"
"v_xor_b32 v9, v9, v7\n"
"v_add_u32 v9, v9, v10\n"
"v_subrev_u32 v224, 0, v9\n"
"v_add_u32 v5, 64, v4\n"
"v_mul_hi_u32 v6, v5, s64\n"
"v_lshlrev_b32 v9, 4, v5\n"
"v_bfe_u32 v8, v6, 3, 1\n"
"v_bfe_u32 v7, v6, 1, 1\n"
"v_lshl_or_b32 v7, v8, 2, v7\n"
"v_mul_u32_u24 v10, 11904, v6\n"
"v_lshlrev_b32 v7, 4, v7\n"
"v_xor_b32 v9, v9, v7\n"
"v_add_u32 v9, v9, v10\n"
"v_subrev_u32 v225, 1024, v9\n"
"v_add_u32 v5, 128, v4\n"
"v_mul_hi_u32 v6, v5, s64\n"
"v_lshlrev_b32 v9, 4, v5\n"
"v_bfe_u32 v8, v6, 3, 1\n"
"v_bfe_u32 v7, v6, 1, 1\n"
"v_lshl_or_b32 v7, v8, 2, v7\n"
"v_mul_u32_u24 v10, 11904, v6\n"
"v_lshlrev_b32 v7, 4, v7\n"
"v_xor_b32 v9, v9, v7\n"
"v_add_u32 v9, v9, v10\n"
"v_subrev_u32 v226, 2048, v9\n"
"v_add_u32 v5, 192, v4\n"
"v_mul_hi_u32 v6, v5, s64\n"
"v_lshlrev_b32 v9, 4, v5\n"
"v_bfe_u32 v8, v6, 3, 1\n"
"v_bfe_u32 v7, v6, 1, 1\n"
"v_lshl_or_b32 v7, v8, 2, v7\n"
"v_mul_u32_u24 v10, 11904, v6\n"
"v_lshlrev_b32 v7, 4, v7\n"
"v_xor_b32 v9, v9, v7\n"
"v_add_u32 v9, v9, v10\n"
"v_subrev_u32 v227, 3072, v9\n"
"v_add_u32 v5, 256, v4\n"
"v_mul_hi_u32 v6, v5, s64\n"
"v_lshlrev_b32 v9, 4, v5\n"
"v_bfe_u32 v8, v6, 3, 1\n"
"v_bfe_u32 v7, v6, 1, 1\n"
"v_lshl_or_b32 v7, v8, 2, v7\n"
"v_mul_u32_u24 v10, 11904, v6\n"
"v_lshlrev_b32 v7, 4, v7\n"
"v_xor_b32 v9, v9, v7\n"
"v_add_u32 v9, v9, v10\n"
"v_subrev_u32 v228, 0, v9\n"
"v_add_u32 v5, 320, v4\n"
"v_mul_hi_u32 v6, v5, s64\n"
"v_lshlrev_b32 v9, 4, v5\n"
"v_bfe_u32 v8, v6, 3, 1\n"
"v_bfe_u32 v7, v6, 1, 1\n"
"v_lshl_or_b32 v7, v8, 2, v7\n"
"v_mul_u32_u24 v10, 11904, v6\n"
"v_lshlrev_b32 v7, 4, v7\n"
"v_xor_b32 v9, v9, v7\n"
"v_add_u32 v9, v9, v10\n"
"v_subrev_u32 v229, 1024, v9\n"
"s_mul_i32 s89, s40, 6144\n"
"s_add_u32 s54, s89, 49152\n"
"s_add_u32 s55, s89, 24576\n"
"s_mov_b32 m0, s54\n"
"s_nop 0\n"
"buffer_load_dwordx4 v224, s[32:35], s51 offen offset:0 lds\n"
"buffer_load_dwordx4 v225, s[32:35], s51 offen offset:1024 lds\n"
"buffer_load_dwordx4 v226, s[32:35], s51 offen offset:2048 lds\n"
"buffer_load_dwordx4 v227, s[32:35], s51 offen offset:3072 lds\n"
"s_add_u32 m0, s54, 4096\n"
"s_nop 0\n"
"buffer_load_dwordx4 v228, s[32:35], s51 offen offset:0 lds\n"
"buffer_load_dwordx4 v229, s[32:35], s51 offen offset:1024 lds\n"
"s_lshl_b32 s60, s67, 5\n"
"s_add_u32 s60, s60, s66\n"
"s_mul_i32 s61, s60, 393216\n"
"s_mov_b64 s[20:21], %[k]\n"
"s_add_u32 s20, s20, s61\n"
"s_addc_u32 s21, s21, 0\n"
"s_and_b32 s21, s21, 0xffff\n"
"s_mov_b32 s22, 393216\n"
"s_mov_b32 s23, 0x20000\n"
"s_mov_b64 s[24:25], %[v]\n"
"s_add_u32 s24, s24, s61\n"
"s_addc_u32 s25, s25, 0\n"
"s_and_b32 s25, s25, 0xffff\n"
"s_mov_b32 s26, 393216\n"
"s_mov_b32 s27, 0x20000\n"
"s_mul_i32 s62, s67, 2949120\n"
"s_mov_b64 s[36:37], %[o]\n"
"s_add_u32 s36, s36, s62\n"
"s_addc_u32 s37, s37, 0\n"
"s_and_b32 s37, s37, 0xffff\n"
"s_mov_b32 s38, 2949120\n"
"s_mov_b32 s39, 0x20000\n"
"s_mov_b32 s64, 0xAAAAAAB\n"
"v_add_u32 v5, 0, v4\n"
"v_mul_hi_u32 v6, v5, s64\n"
"v_bfe_u32 v8, v6, 2, 1\n"
"v_bfe_u32 v9, v6, 3, 1\n"
"v_xor_b32 v8, v8, v9\n"
"v_lshl_or_b32 v9, v8, 2, v9\n"
"v_bfe_u32 v8, v6, 1, 1\n"
"v_lshl_or_b32 v7, v8, 1, v9\n"
"v_lshlrev_b32 v8, 4, v7\n"
"v_lshlrev_b32 v9, 4, v5\n"
"v_xor_b32 v9, v9, v8\n"
"v_subrev_u32 v202, 0, v9\n"
"v_add_u32 v5, 64, v4\n"
"v_mul_hi_u32 v6, v5, s64\n"
"v_bfe_u32 v8, v6, 2, 1\n"
"v_bfe_u32 v9, v6, 3, 1\n"
"v_xor_b32 v8, v8, v9\n"
"v_lshl_or_b32 v9, v8, 2, v9\n"
"v_bfe_u32 v8, v6, 1, 1\n"
"v_lshl_or_b32 v7, v8, 1, v9\n"
"v_lshlrev_b32 v8, 4, v7\n"
"v_lshlrev_b32 v9, 4, v5\n"
"v_xor_b32 v9, v9, v8\n"
"v_subrev_u32 v203, 1024, v9\n"
"v_add_u32 v5, 128, v4\n"
"v_mul_hi_u32 v6, v5, s64\n"
"v_bfe_u32 v8, v6, 2, 1\n"
"v_bfe_u32 v9, v6, 3, 1\n"
"v_xor_b32 v8, v8, v9\n"
"v_lshl_or_b32 v9, v8, 2, v9\n"
"v_bfe_u32 v8, v6, 1, 1\n"
"v_lshl_or_b32 v7, v8, 1, v9\n"
"v_lshlrev_b32 v8, 4, v7\n"
"v_lshlrev_b32 v9, 4, v5\n"
"v_xor_b32 v9, v9, v8\n"
"v_subrev_u32 v204, 2048, v9\n"
"v_add_u32 v5, 192, v4\n"
"v_mul_hi_u32 v6, v5, s64\n"
"v_bfe_u32 v8, v6, 2, 1\n"
"v_bfe_u32 v9, v6, 3, 1\n"
"v_xor_b32 v8, v8, v9\n"
"v_lshl_or_b32 v9, v8, 2, v9\n"
"v_bfe_u32 v8, v6, 1, 1\n"
"v_lshl_or_b32 v7, v8, 1, v9\n"
"v_lshlrev_b32 v8, 4, v7\n"
"v_lshlrev_b32 v9, 4, v5\n"
"v_xor_b32 v9, v9, v8\n"
"v_subrev_u32 v205, 3072, v9\n"
"v_add_u32 v5, 256, v4\n"
"v_mul_hi_u32 v6, v5, s64\n"
"v_bfe_u32 v8, v6, 2, 1\n"
"v_bfe_u32 v9, v6, 3, 1\n"
"v_xor_b32 v8, v8, v9\n"
"v_lshl_or_b32 v9, v8, 2, v9\n"
"v_bfe_u32 v8, v6, 1, 1\n"
"v_lshl_or_b32 v7, v8, 1, v9\n"
"v_lshlrev_b32 v8, 4, v7\n"
"v_lshlrev_b32 v9, 4, v5\n"
"v_xor_b32 v9, v9, v8\n"
"v_subrev_u32 v206, 0, v9\n"
"v_add_u32 v5, 320, v4\n"
"v_mul_hi_u32 v6, v5, s64\n"
"v_bfe_u32 v8, v6, 2, 1\n"
"v_bfe_u32 v9, v6, 3, 1\n"
"v_xor_b32 v8, v8, v9\n"
"v_lshl_or_b32 v9, v8, 2, v9\n"
"v_bfe_u32 v8, v6, 1, 1\n"
"v_lshl_or_b32 v7, v8, 1, v9\n"
"v_lshlrev_b32 v8, 4, v7\n"
"v_lshlrev_b32 v9, 4, v5\n"
"v_xor_b32 v9, v9, v8\n"
"v_subrev_u32 v207, 1024, v9\n"
"s_lshr_b32 s88, %[blk], 3\n"
"s_and_b32 s88, s88, 0\n"
"v_and_b32 v5, 15, v4\n"
"v_lshrrev_b32 v6, 4, v4\n"
"v_bfe_u32 v8, v5, 2, 1\n"
"v_bfe_u32 v9, v5, 3, 1\n"
"v_xor_b32 v8, v8, v9\n"
"v_lshl_or_b32 v9, v8, 2, v9\n"
"v_bfe_u32 v8, v5, 1, 1\n"
"v_lshl_or_b32 v7, v8, 1, v9\n"
"v_mul_u32_u24 v10, 384, v5\n"
"v_lshlrev_b32 v11, 7, v5\n"
"v_add_u32 v8, 0, v6\n"
"v_xor_b32 v8, v8, v7\n"
"v_lshl_add_u32 v196, v8, 4, v10\n"
"v_lshl_add_u32 v12, v8, 4, v11\n"
"v_add_u32 v212, s52, v12\n"
"v_add_u32 v214, s53, v12\n"
"v_add_u32 v8, 4, v6\n"
"v_xor_b32 v8, v8, v7\n"
"v_lshl_add_u32 v197, v8, 4, v10\n"
"v_lshl_add_u32 v12, v8, 4, v11\n"
"v_add_u32 v213, s52, v12\n"
"v_add_u32 v215, s53, v12\n"
"v_bfe_u32 v8, v4, 2, 2\n"
"v_and_b32 v9, 3, v4\n"
"v_lshl_add_u32 v10, v6, 2, v8\n"
"v_bfe_u32 v11, v10, 2, 1\n"
"v_bfe_u32 v12, v10, 3, 1\n"
"v_xor_b32 v11, v11, v12\n"
"v_lshl_or_b32 v12, v11, 2, v12\n"
"v_bfe_u32 v11, v10, 1, 1\n"
"v_lshl_or_b32 v7, v11, 1, v12\n"
"v_mul_u32_u24 v10, 384, v10\n"
"v_and_b32 v11, 1, v9\n"
"v_lshl_add_u32 v10, v11, 3, v10\n"
"v_lshrrev_b32 v11, 1, v9\n"
"v_add_u32 v12, 0, v11\n"
"v_xor_b32 v12, v12, v7\n"
"v_lshl_add_u32 v198, v12, 4, v10\n"
"v_add_u32 v12, 2, v11\n"
"v_xor_b32 v12, v12, v7\n"
"v_lshl_add_u32 v199, v12, 4, v10\n"
"v_add_u32 v12, 4, v11\n"
"v_xor_b32 v12, v12, v7\n"
"v_lshl_add_u32 v200, v12, 4, v10\n"
"v_add_u32 v12, 6, v11\n"
"v_xor_b32 v12, v12, v7\n"
"v_lshl_add_u32 v201, v12, 4, v10\n"
"v_lshl_add_u32 v10, v6, 3, v8\n"
"v_mul_u32_u24 v10, 384, v10\n"
"v_and_b32 v11, 1, v6\n"
"v_lshlrev_b32 v11, 2, v11\n"
"v_lshrrev_b32 v12, 1, v8\n"
"v_or_b32 v7, v11, v12\n"
"v_and_b32 v11, 1, v9\n"
"v_add_u32 v12, 0, v9\n"
"v_xor_b32 v12, v12, v7\n"
"v_lshl_add_u32 v12, v12, 4, v10\n"
"v_xor_b32 v13, 0, v11\n"
"v_lshl_add_u32 v208, v13, 3, v12\n"
"v_xor_b32 v13, 1, v11\n"
"v_lshl_add_u32 v209, v13, 3, v12\n"
"v_add_u32 v12, 4, v9\n"
"v_xor_b32 v12, v12, v7\n"
"v_lshl_add_u32 v12, v12, 4, v10\n"
"v_xor_b32 v13, 0, v11\n"
"v_lshl_add_u32 v210, v13, 3, v12\n"
"v_xor_b32 v13, 1, v11\n"
"v_lshl_add_u32 v211, v13, 3, v12\n"
"v_mov_b32 v5, 0\n"
"v_accvgpr_write_b32 a0, v5\n"
"v_accvgpr_write_b32 a1, v5\n"
"v_accvgpr_write_b32 a2, v5\n"
"v_accvgpr_write_b32 a3, v5\n"
"v_accvgpr_write_b32 a4, v5\n"
"v_accvgpr_write_b32 a5, v5\n"
"v_accvgpr_write_b32 a6, v5\n"
"v_accvgpr_write_b32 a7, v5\n"
"v_accvgpr_write_b32 a8, v5\n"
"v_accvgpr_write_b32 a9, v5\n"
"v_accvgpr_write_b32 a10, v5\n"
"v_accvgpr_write_b32 a11, v5\n"
"v_accvgpr_write_b32 a12, v5\n"
"v_accvgpr_write_b32 a13, v5\n"
"v_accvgpr_write_b32 a14, v5\n"
"v_accvgpr_write_b32 a15, v5\n"
"v_accvgpr_write_b32 a16, v5\n"
"v_accvgpr_write_b32 a17, v5\n"
"v_accvgpr_write_b32 a18, v5\n"
"v_accvgpr_write_b32 a19, v5\n"
"v_accvgpr_write_b32 a20, v5\n"
"v_accvgpr_write_b32 a21, v5\n"
"v_accvgpr_write_b32 a22, v5\n"
"v_accvgpr_write_b32 a23, v5\n"
"v_accvgpr_write_b32 a24, v5\n"
"v_accvgpr_write_b32 a25, v5\n"
"v_accvgpr_write_b32 a26, v5\n"
"v_accvgpr_write_b32 a27, v5\n"
"v_accvgpr_write_b32 a28, v5\n"
"v_accvgpr_write_b32 a29, v5\n"
"v_accvgpr_write_b32 a30, v5\n"
"v_accvgpr_write_b32 a31, v5\n"
"v_accvgpr_write_b32 a32, v5\n"
"v_accvgpr_write_b32 a33, v5\n"
"v_accvgpr_write_b32 a34, v5\n"
"v_accvgpr_write_b32 a35, v5\n"
"v_accvgpr_write_b32 a36, v5\n"
"v_accvgpr_write_b32 a37, v5\n"
"v_accvgpr_write_b32 a38, v5\n"
"v_accvgpr_write_b32 a39, v5\n"
"v_accvgpr_write_b32 a40, v5\n"
"v_accvgpr_write_b32 a41, v5\n"
"v_accvgpr_write_b32 a42, v5\n"
"v_accvgpr_write_b32 a43, v5\n"
"v_accvgpr_write_b32 a44, v5\n"
"v_accvgpr_write_b32 a45, v5\n"
"v_accvgpr_write_b32 a46, v5\n"
"v_accvgpr_write_b32 a47, v5\n"
"v_accvgpr_write_b32 a48, v5\n"
"v_accvgpr_write_b32 a49, v5\n"
"v_accvgpr_write_b32 a50, v5\n"
"v_accvgpr_write_b32 a51, v5\n"
"v_accvgpr_write_b32 a52, v5\n"
"v_accvgpr_write_b32 a53, v5\n"
"v_accvgpr_write_b32 a54, v5\n"
"v_accvgpr_write_b32 a55, v5\n"
"v_accvgpr_write_b32 a56, v5\n"
"v_accvgpr_write_b32 a57, v5\n"
"v_accvgpr_write_b32 a58, v5\n"
"v_accvgpr_write_b32 a59, v5\n"
"v_accvgpr_write_b32 a60, v5\n"
"v_accvgpr_write_b32 a61, v5\n"
"v_accvgpr_write_b32 a62, v5\n"
"v_accvgpr_write_b32 a63, v5\n"
"v_accvgpr_write_b32 a64, v5\n"
"v_accvgpr_write_b32 a65, v5\n"
"v_accvgpr_write_b32 a66, v5\n"
"v_accvgpr_write_b32 a67, v5\n"
"v_accvgpr_write_b32 a68, v5\n"
"v_accvgpr_write_b32 a69, v5\n"
"v_accvgpr_write_b32 a70, v5\n"
"v_accvgpr_write_b32 a71, v5\n"
"v_accvgpr_write_b32 a72, v5\n"
"v_accvgpr_write_b32 a73, v5\n"
"v_accvgpr_write_b32 a74, v5\n"
"v_accvgpr_write_b32 a75, v5\n"
"v_accvgpr_write_b32 a76, v5\n"
"v_accvgpr_write_b32 a77, v5\n"
"v_accvgpr_write_b32 a78, v5\n"
"v_accvgpr_write_b32 a79, v5\n"
"v_accvgpr_write_b32 a80, v5\n"
"v_accvgpr_write_b32 a81, v5\n"
"v_accvgpr_write_b32 a82, v5\n"
"v_accvgpr_write_b32 a83, v5\n"
"v_accvgpr_write_b32 a84, v5\n"
"v_accvgpr_write_b32 a85, v5\n"
"v_accvgpr_write_b32 a86, v5\n"
"v_accvgpr_write_b32 a87, v5\n"
"v_accvgpr_write_b32 a88, v5\n"
"v_accvgpr_write_b32 a89, v5\n"
"v_accvgpr_write_b32 a90, v5\n"
"v_accvgpr_write_b32 a91, v5\n"
"v_accvgpr_write_b32 a92, v5\n"
"v_accvgpr_write_b32 a93, v5\n"
"v_accvgpr_write_b32 a94, v5\n"
"v_accvgpr_write_b32 a95, v5\n"
"v_accvgpr_write_b32 a96, v5\n"
"v_accvgpr_write_b32 a97, v5\n"
"v_accvgpr_write_b32 a98, v5\n"
"v_accvgpr_write_b32 a99, v5\n"
"v_accvgpr_write_b32 a100, v5\n"
"v_accvgpr_write_b32 a101, v5\n"
"v_accvgpr_write_b32 a102, v5\n"
"v_accvgpr_write_b32 a103, v5\n"
"v_accvgpr_write_b32 a104, v5\n"
"v_accvgpr_write_b32 a105, v5\n"
"v_accvgpr_write_b32 a106, v5\n"
"v_accvgpr_write_b32 a107, v5\n"
"v_accvgpr_write_b32 a108, v5\n"
"v_accvgpr_write_b32 a109, v5\n"
"v_accvgpr_write_b32 a110, v5\n"
"v_accvgpr_write_b32 a111, v5\n"
"v_accvgpr_write_b32 a112, v5\n"
"v_accvgpr_write_b32 a113, v5\n"
"v_accvgpr_write_b32 a114, v5\n"
"v_accvgpr_write_b32 a115, v5\n"
"v_accvgpr_write_b32 a116, v5\n"
"v_accvgpr_write_b32 a117, v5\n"
"v_accvgpr_write_b32 a118, v5\n"
"v_accvgpr_write_b32 a119, v5\n"
"v_accvgpr_write_b32 a120, v5\n"
"v_accvgpr_write_b32 a121, v5\n"
"v_accvgpr_write_b32 a122, v5\n"
"v_accvgpr_write_b32 a123, v5\n"
"v_accvgpr_write_b32 a124, v5\n"
"v_accvgpr_write_b32 a125, v5\n"
"v_accvgpr_write_b32 a126, v5\n"
"v_accvgpr_write_b32 a127, v5\n"
"v_accvgpr_write_b32 a128, v5\n"
"v_accvgpr_write_b32 a129, v5\n"
"v_accvgpr_write_b32 a130, v5\n"
"v_accvgpr_write_b32 a131, v5\n"
"v_accvgpr_write_b32 a132, v5\n"
"v_accvgpr_write_b32 a133, v5\n"
"v_accvgpr_write_b32 a134, v5\n"
"v_accvgpr_write_b32 a135, v5\n"
"v_accvgpr_write_b32 a136, v5\n"
"v_accvgpr_write_b32 a137, v5\n"
"v_accvgpr_write_b32 a138, v5\n"
"v_accvgpr_write_b32 a139, v5\n"
"v_accvgpr_write_b32 a140, v5\n"
"v_accvgpr_write_b32 a141, v5\n"
"v_accvgpr_write_b32 a142, v5\n"
"v_accvgpr_write_b32 a143, v5\n"
"v_accvgpr_write_b32 a144, v5\n"
"v_accvgpr_write_b32 a145, v5\n"
"v_accvgpr_write_b32 a146, v5\n"
"v_accvgpr_write_b32 a147, v5\n"
"v_accvgpr_write_b32 a148, v5\n"
"v_accvgpr_write_b32 a149, v5\n"
"v_accvgpr_write_b32 a150, v5\n"
"v_accvgpr_write_b32 a151, v5\n"
"v_accvgpr_write_b32 a152, v5\n"
"v_accvgpr_write_b32 a153, v5\n"
"v_accvgpr_write_b32 a154, v5\n"
"v_accvgpr_write_b32 a155, v5\n"
"v_accvgpr_write_b32 a156, v5\n"
"v_accvgpr_write_b32 a157, v5\n"
"v_accvgpr_write_b32 a158, v5\n"
"v_accvgpr_write_b32 a159, v5\n"
"v_accvgpr_write_b32 a160, v5\n"
"v_accvgpr_write_b32 a161, v5\n"
"v_accvgpr_write_b32 a162, v5\n"
"v_accvgpr_write_b32 a163, v5\n"
"v_accvgpr_write_b32 a164, v5\n"
"v_accvgpr_write_b32 a165, v5\n"
"v_accvgpr_write_b32 a166, v5\n"
"v_accvgpr_write_b32 a167, v5\n"
"v_accvgpr_write_b32 a168, v5\n"
"v_accvgpr_write_b32 a169, v5\n"
"v_accvgpr_write_b32 a170, v5\n"
"v_accvgpr_write_b32 a171, v5\n"
"v_accvgpr_write_b32 a172, v5\n"
"v_accvgpr_write_b32 a173, v5\n"
"v_accvgpr_write_b32 a174, v5\n"
"v_accvgpr_write_b32 a175, v5\n"
"v_accvgpr_write_b32 a176, v5\n"
"v_accvgpr_write_b32 a177, v5\n"
"v_accvgpr_write_b32 a178, v5\n"
"v_accvgpr_write_b32 a179, v5\n"
"v_accvgpr_write_b32 a180, v5\n"
"v_accvgpr_write_b32 a181, v5\n"
"v_accvgpr_write_b32 a182, v5\n"
"v_accvgpr_write_b32 a183, v5\n"
"v_accvgpr_write_b32 a184, v5\n"
"v_accvgpr_write_b32 a185, v5\n"
"v_accvgpr_write_b32 a186, v5\n"
"v_accvgpr_write_b32 a187, v5\n"
"v_accvgpr_write_b32 a188, v5\n"
"v_accvgpr_write_b32 a189, v5\n"
"v_accvgpr_write_b32 a190, v5\n"
"v_accvgpr_write_b32 a191, v5\n"
"v_mov_b32 v112, 0x3f803f80\n"
"v_mov_b32 v113, 0x3f803f80\n"
"v_mov_b32 v114, 0x3f803f80\n"
"v_mov_b32 v115, 0x3f803f80\n"
"s_mov_b32 s56, 0x3dd53b95\n"
"s_mov_b32 s57, 0x3dd53b95\n"
"s_add_u32 s59, s59, 1\n"
"s_cmp_eq_u32 s59, 10\n"
"s_cselect_b32 s59, 0, s59\n"
"s_lshl_b32 s50, s59, 7\n"
"s_add_u32 s50, s50, s46\n"
"s_mul_i32 s51, s59, 786432\n"
"s_add_u32 s51, s51, s47\n"
"s_mov_b32 m0, s53\n"
"s_nop 0\n"
"buffer_load_dwordx4 v216, s[28:31], s50 offen offset:0 lds\n"
"buffer_load_dwordx4 v217, s[28:31], s50 offen offset:1024 lds\n"
"buffer_load_dwordx4 v218, s[28:31], s50 offen offset:2048 lds\n"
"buffer_load_dwordx4 v219, s[28:31], s50 offen offset:3072 lds\n"
"s_add_u32 m0, s53, 4096\n"
"s_nop 0\n"
"buffer_load_dwordx4 v220, s[28:31], s50 offen offset:0 lds\n"
"buffer_load_dwordx4 v221, s[28:31], s50 offen offset:1024 lds\n"
"buffer_load_dwordx4 v222, s[28:31], s50 offen offset:2048 lds\n"
"buffer_load_dwordx4 v223, s[28:31], s50 offen offset:3072 lds\n"
"s_mov_b32 m0, s55\n"
"s_nop 0\n"
"buffer_load_dwordx4 v224, s[32:35], s51 offen offset:0 lds\n"
"buffer_load_dwordx4 v225, s[32:35], s51 offen offset:1024 lds\n"
"buffer_load_dwordx4 v226, s[32:35], s51 offen offset:2048 lds\n"
"buffer_load_dwordx4 v227, s[32:35], s51 offen offset:3072 lds\n"
"s_add_u32 m0, s55, 4096\n"
"s_nop 0\n"
"buffer_load_dwordx4 v228, s[32:35], s51 offen offset:0 lds\n"
"buffer_load_dwordx4 v229, s[32:35], s51 offen offset:1024 lds\n"
"s_add_u32 s59, s59, 1\n"
"s_cmp_eq_u32 s59, 10\n"
"s_cselect_b32 s59, 0, s59\n"
"s_lshl_b32 s50, s59, 7\n"
"s_add_u32 s50, s50, s46\n"
"s_mul_i32 s51, s59, 786432\n"
"s_add_u32 s51, s51, s47\n"
"s_getpc_b64 s[62:63]\n"
"L_pc_L_end%=:\n"
"s_mov_b32 s64, L_end%=-L_pc_L_end%=\n"
"s_lshr_b32 s64, s64, 7\n"
"s_lshr_b32 s65, %[blk], 3\n"
"s_lshl_b32 s65, s65, 2\n"
"s_add_u32 s65, s65, s40\n"
"v_lshlrev_b32 v5, 7, v4\n"
"v_add_u32 v5, s65, v5\n"
"v_min_u32 v5, s64, v5\n"
"v_lshlrev_b32 v5, 7, v5\n"
"global_load_dword v254, v5, s[62:63]\n"
"s_waitcnt vmcnt(15)\n"
"s_barrier\n"
"ds_read_b64_tr_b16 v[148:149], v208 offset:49152\n"
"ds_read_b64_tr_b16 v[150:151], v208 offset:50688\n"
"ds_read_b64_tr_b16 v[152:153], v208 offset:61440\n"
"ds_read_b64_tr_b16 v[154:155], v208 offset:62976\n"
"ds_read_b128 v[16:19], v212 offset:0\n"
"ds_read_b128 v[32:35], v213 offset:0\n"
"ds_read_b128 v[20:23], v212 offset:2048\n"
"ds_read_b128 v[36:39], v213 offset:2048\n"
"ds_read_b128 v[24:27], v212 offset:4096\n"
"ds_read_b128 v[40:43], v213 offset:4096\n"
"ds_read_b128 v[28:31], v212 offset:6144\n"
"ds_read_b128 v[44:47], v213 offset:6144\n"
"ds_read_b64_tr_b16 v[156:157], v209 offset:49152\n"
"ds_read_b64_tr_b16 v[158:159], v209 offset:50688\n"
"ds_read_b64_tr_b16 v[160:161], v209 offset:61440\n"
"ds_read_b64_tr_b16 v[162:163], v209 offset:62976\n"
"s_mul_i32 s60, s40, 6144\n"
"s_add_u32 s61, s60, 73728\n"
"s_add_u32 s62, s88, 0\n"
"s_and_b32 s62, s62, 15\n"
"s_mul_i32 s62, s62, 24576\n"
"s_add_u32 s62, s62, s89\n"
"s_mov_b32 s86, s62\n"
"s_mov_b32 s87, s61\n"
"s_waitcnt lgkmcnt(4)\n"
"v_mfma_f32_16x16x32_bf16 a[0:3], v[148:151], v[16:19], a[0:3]\n"
"v_mfma_f32_16x16x32_bf16 a[0:3], v[152:155], v[32:35], a[0:3]\n"
"v_mfma_f32_16x16x32_bf16 a[4:7], v[148:151], v[20:23], a[4:7]\n"
"v_mfma_f32_16x16x32_bf16 a[4:7], v[152:155], v[36:39], a[4:7]\n"
"v_mfma_f32_16x16x32_bf16 a[8:11], v[148:151], v[24:27], a[8:11]\n"
"ds_read_b64_tr_b16 v[164:165], v210 offset:49152\n"
"ds_read_b64_tr_b16 v[166:167], v210 offset:50688\n"
"v_mfma_f32_16x16x32_bf16 a[8:11], v[152:155], v[40:43], a[8:11]\n"
"ds_read_b64_tr_b16 v[168:169], v210 offset:61440\n"
"ds_read_b64_tr_b16 v[170:171], v210 offset:62976\n"
"v_mfma_f32_16x16x32_bf16 a[12:15], v[148:151], v[28:31], a[12:15]\n"
"v_mfma_f32_16x16x32_bf16 a[12:15], v[152:155], v[44:47], a[12:15]\n"
"s_waitcnt lgkmcnt(4)\n"
"v_mfma_f32_16x16x32_bf16 a[16:19], v[156:159], v[16:19], a[16:19]\n"
"v_mfma_f32_16x16x32_bf16 a[16:19], v[160:163], v[32:35], a[16:19]\n"
"v_mfma_f32_16x16x32_bf16 a[20:23], v[156:159], v[20:23], a[20:23]\n"
"v_mfma_f32_16x16x32_bf16 a[20:23], v[160:163], v[36:39], a[20:23]\n"
"v_mfma_f32_16x16x32_bf16 a[24:27], v[156:159], v[24:27], a[24:27]\n"
"ds_read_b64_tr_b16 v[172:173], v211 offset:49152\n"
"ds_read_b64_tr_b16 v[174:175], v211 offset:50688\n"
"v_mfma_f32_16x16x32_bf16 a[24:27], v[160:163], v[40:43], a[24:27]\n"
"s_mov_b32 m0, s52\n"
"ds_read_b64_tr_b16 v[176:177], v211 offset:61440\n"
"ds_read_b64_tr_b16 v[178:179], v211 offset:62976\n"
"v_mfma_f32_16x16x32_bf16 a[28:31], v[156:159], v[28:31], a[28:31]\n"
"buffer_load_dwordx4 v216, s[28:31], s50 offen offset:0 lds\n"
"v_mfma_f32_16x16x32_bf16 a[28:31], v[160:163], v[44:47], a[28:31]\n"
"s_waitcnt lgkmcnt(4)\n"
"v_mfma_f32_16x16x32_bf16 a[32:35], v[164:167], v[16:19], a[32:35]\n"
"v_mfma_f32_16x16x32_bf16 a[32:35], v[168:171], v[32:35], a[32:35]\n"
"v_mfma_f32_16x16x32_bf16 a[36:39], v[164:167], v[20:23], a[36:39]\n"
"v_mfma_f32_16x16x32_bf16 a[36:39], v[168:171], v[36:39], a[36:39]\n"
"v_mfma_f32_16x16x32_bf16 a[40:43], v[164:167], v[24:27], a[40:43]\n"
"ds_read_b64_tr_b16 v[148:149], v208 offset:49280\n"
"ds_read_b64_tr_b16 v[150:151], v208 offset:50816\n"
"v_mfma_f32_16x16x32_bf16 a[40:43], v[168:171], v[40:43], a[40:43]\n"
"ds_read_b64_tr_b16 v[152:153], v208 offset:61568\n"
"ds_read_b64_tr_b16 v[154:155], v208 offset:63104\n"
"v_mfma_f32_16x16x32_bf16 a[44:47], v[164:167], v[28:31], a[44:47]\n"
"buffer_load_dwordx4 v217, s[28:31], s50 offen offset:1024 lds\n"
"v_mfma_f32_16x16x32_bf16 a[44:47], v[168:171], v[44:47], a[44:47]\n"
"s_waitcnt lgkmcnt(4)\n"
"v_mfma_f32_16x16x32_bf16 a[48:51], v[172:175], v[16:19], a[48:51]\n"
"v_mfma_f32_16x16x32_bf16 a[48:51], v[176:179], v[32:35], a[48:51]\n"
"v_mfma_f32_16x16x32_bf16 a[52:55], v[172:175], v[20:23], a[52:55]\n"
"v_mfma_f32_16x16x32_bf16 a[52:55], v[176:179], v[36:39], a[52:55]\n"
"v_mfma_f32_16x16x32_bf16 a[56:59], v[172:175], v[24:27], a[56:59]\n"
"ds_read_b64_tr_b16 v[156:157], v209 offset:49280\n"
"ds_read_b64_tr_b16 v[158:159], v209 offset:50816\n"
"v_mfma_f32_16x16x32_bf16 a[56:59], v[176:179], v[40:43], a[56:59]\n"
"ds_read_b64_tr_b16 v[160:161], v209 offset:61568\n"
"ds_read_b64_tr_b16 v[162:163], v209 offset:63104\n"
"v_mfma_f32_16x16x32_bf16 a[60:63], v[172:175], v[28:31], a[60:63]\n"
"buffer_load_dwordx4 v218, s[28:31], s50 offen offset:2048 lds\n"
"v_mfma_f32_16x16x32_bf16 a[60:63], v[176:179], v[44:47], a[60:63]\n"
"s_waitcnt lgkmcnt(4)\n"
"v_mfma_f32_16x16x32_bf16 a[64:67], v[148:151], v[16:19], a[64:67]\n"
"v_mfma_f32_16x16x32_bf16 a[64:67], v[152:155], v[32:35], a[64:67]\n"
"v_mfma_f32_16x16x32_bf16 a[68:71], v[148:151], v[20:23], a[68:71]\n"
"v_mfma_f32_16x16x32_bf16 a[68:71], v[152:155], v[36:39], a[68:71]\n"
"buffer_load_dwordx4 v219, s[28:31], s50 offen offset:3072 lds\n"
"v_mfma_f32_16x16x32_bf16 a[72:75], v[148:151], v[24:27], a[72:75]\n"
"ds_read_b64_tr_b16 v[164:165], v210 offset:49280\n"
"ds_read_b64_tr_b16 v[166:167], v210 offset:50816\n"
"v_mfma_f32_16x16x32_bf16 a[72:75], v[152:155], v[40:43], a[72:75]\n"
"ds_read_b64_tr_b16 v[168:169], v210 offset:61568\n"
"ds_read_b64_tr_b16 v[170:171], v210 offset:63104\n"
"v_mfma_f32_16x16x32_bf16 a[76:79], v[148:151], v[28:31], a[76:79]\n"
"v_mfma_f32_16x16x32_bf16 a[76:79], v[152:155], v[44:47], a[76:79]\n"
"s_waitcnt lgkmcnt(4)\n"
"v_mfma_f32_16x16x32_bf16 a[80:83], v[156:159], v[16:19], a[80:83]\n"
"v_mfma_f32_16x16x32_bf16 a[80:83], v[160:163], v[32:35], a[80:83]\n"
"s_add_u32 m0, s52, 4096\n"
"v_mfma_f32_16x16x32_bf16 a[84:87], v[156:159], v[20:23], a[84:87]\n"
"buffer_load_dwordx4 v220, s[28:31], s50 offen offset:0 lds\n"
"v_mfma_f32_16x16x32_bf16 a[84:87], v[160:163], v[36:39], a[84:87]\n"
"v_mfma_f32_16x16x32_bf16 a[88:91], v[156:159], v[24:27], a[88:91]\n"
"ds_read_b64_tr_b16 v[172:173], v211 offset:49280\n"
"ds_read_b64_tr_b16 v[174:175], v211 offset:50816\n"
"ds_read_b64_tr_b16 v[148:149], v208 offset:49408\n"
"ds_read_b64_tr_b16 v[150:151], v208 offset:50944\n"
"v_mfma_f32_16x16x32_bf16 a[88:91], v[160:163], v[40:43], a[88:91]\n"
"ds_read_b64_tr_b16 v[176:177], v211 offset:61568\n"
"ds_read_b64_tr_b16 v[178:179], v211 offset:63104\n"
"ds_read_b64_tr_b16 v[152:153], v208 offset:61696\n"
"ds_read_b64_tr_b16 v[154:155], v208 offset:63232\n"
"v_mfma_f32_16x16x32_bf16 a[92:95], v[156:159], v[28:31], a[92:95]\n"
"v_mfma_f32_16x16x32_bf16 a[92:95], v[160:163], v[44:47], a[92:95]\n"
"s_waitcnt lgkmcnt(8)\n"
"v_mfma_f32_16x16x32_bf16 a[96:99], v[164:167], v[16:19], a[96:99]\n"
"v_mfma_f32_16x16x32_bf16 a[96:99], v[168:171], v[32:35], a[96:99]\n"
"buffer_load_dwordx4 v221, s[28:31], s50 offen offset:1024 lds\n"
"v_mfma_f32_16x16x32_bf16 a[100:103], v[164:167], v[20:23], a[100:103]\n"
"ds_read_b64_tr_b16 v[156:157], v209 offset:49408\n"
"ds_read_b64_tr_b16 v[158:159], v209 offset:50944\n"
"v_mfma_f32_16x16x32_bf16 a[100:103], v[168:171], v[36:39], a[100:103]\n"
"ds_read_b64_tr_b16 v[160:161], v209 offset:61696\n"
"ds_read_b64_tr_b16 v[162:163], v209 offset:63232\n"
"v_mfma_f32_16x16x32_bf16 a[104:107], v[164:167], v[24:27], a[104:107]\n"
"v_mfma_f32_16x16x32_bf16 a[104:107], v[168:171], v[40:43], a[104:107]\n"
"v_mfma_f32_16x16x32_bf16 a[108:111], v[164:167], v[28:31], a[108:111]\n"
"v_mfma_f32_16x16x32_bf16 a[108:111], v[168:171], v[44:47], a[108:111]\n"
"s_waitcnt lgkmcnt(6)\n"
"v_mfma_f32_16x16x32_bf16 a[112:115], v[172:175], v[16:19], a[112:115]\n"
"buffer_load_dwordx4 v222, s[28:31], s50 offen offset:2048 lds\n"
"v_mfma_f32_16x16x32_bf16 a[112:115], v[176:179], v[32:35], a[112:115]\n"
"ds_read_b64_tr_b16 v[164:165], v210 offset:49408\n"
"ds_read_b64_tr_b16 v[166:167], v210 offset:50944\n"
"v_mfma_f32_16x16x32_bf16 a[116:119], v[172:175], v[20:23], a[116:119]\n"
"ds_read_b64_tr_b16 v[168:169], v210 offset:61696\n"
"ds_read_b64_tr_b16 v[170:171], v210 offset:63232\n"
"v_mfma_f32_16x16x32_bf16 a[116:119], v[176:179], v[36:39], a[116:119]\n"
"v_mfma_f32_16x16x32_bf16 a[120:123], v[172:175], v[24:27], a[120:123]\n"
"v_mfma_f32_16x16x32_bf16 a[120:123], v[176:179], v[40:43], a[120:123]\n"
"v_mfma_f32_16x16x32_bf16 a[124:127], v[172:175], v[28:31], a[124:127]\n"
"v_mfma_f32_16x16x32_bf16 a[124:127], v[176:179], v[44:47], a[124:127]\n"
"s_waitcnt lgkmcnt(8)\n"
"buffer_load_dwordx4 v223, s[28:31], s50 offen offset:3072 lds\n"
"v_mfma_f32_16x16x32_bf16 a[128:131], v[148:151], v[16:19], a[128:131]\n"
"ds_read_b64_tr_b16 v[172:173], v211 offset:49408\n"
"ds_read_b64_tr_b16 v[174:175], v211 offset:50944\n"
"v_mfma_f32_16x16x32_bf16 a[128:131], v[152:155], v[32:35], a[128:131]\n"
"ds_read_b64_tr_b16 v[176:177], v211 offset:61696\n"
"ds_read_b64_tr_b16 v[178:179], v211 offset:63232\n"
"v_mfma_f32_16x16x32_bf16 a[132:135], v[148:151], v[20:23], a[132:135]\n"
"v_mfma_f32_16x16x32_bf16 a[132:135], v[152:155], v[36:39], a[132:135]\n"
"v_mfma_f32_16x16x32_bf16 a[136:139], v[148:151], v[24:27], a[136:139]\n"
"v_mfma_f32_16x16x32_bf16 a[136:139], v[152:155], v[40:43], a[136:139]\n"
"v_mfma_f32_16x16x32_bf16 a[140:143], v[148:151], v[28:31], a[140:143]\n"
"s_mov_b32 s95, s51\n"
"v_mfma_f32_16x16x32_bf16 a[140:143], v[152:155], v[44:47], a[140:143]\n"
"s_waitcnt lgkmcnt(8)\n"
"v_mfma_f32_16x16x32_bf16 a[144:147], v[156:159], v[16:19], a[144:147]\n"
"v_mfma_f32_16x16x32_bf16 a[144:147], v[160:163], v[32:35], a[144:147]\n"
"s_waitcnt vmcnt(9) lgkmcnt(0)\n"
"s_barrier\n"
"v_mfma_f32_16x16x32_bf16 a[148:151], v[156:159], v[20:23], a[148:151]\n"
"ds_read_b64_tr_b16 v[148:149], v208 offset:24576\n"
"ds_read_b64_tr_b16 v[150:151], v208 offset:26112\n"
"v_mfma_f32_16x16x32_bf16 a[148:151], v[160:163], v[36:39], a[148:151]\n"
"ds_read_b64_tr_b16 v[152:153], v208 offset:36864\n"
"ds_read_b64_tr_b16 v[154:155], v208 offset:38400\n"
"v_mfma_f32_16x16x32_bf16 a[152:155], v[156:159], v[24:27], a[152:155]\n"
"ds_read_b128 v[48:51], v214 offset:0\n"
"ds_read_b128 v[64:67], v215 offset:0\n"
"v_mfma_f32_16x16x32_bf16 a[152:155], v[160:163], v[40:43], a[152:155]\n"
"ds_read_b128 v[52:55], v214 offset:2048\n"
"ds_read_b128 v[68:71], v215 offset:2048\n"
"v_mfma_f32_16x16x32_bf16 a[156:159], v[156:159], v[28:31], a[156:159]\n"
"ds_read_b128 v[56:59], v214 offset:4096\n"
"ds_read_b128 v[72:75], v215 offset:4096\n"
"v_mfma_f32_16x16x32_bf16 a[156:159], v[160:163], v[44:47], a[156:159]\n"
"ds_read_b128 v[60:63], v214 offset:6144\n"
"ds_read_b128 v[76:79], v215 offset:6144\n"
"v_mfma_f32_16x16x32_bf16 a[160:163], v[164:167], v[16:19], a[160:163]\n"
"s_mov_b32 m0, s54\n"
"v_mfma_f32_16x16x32_bf16 a[160:163], v[168:171], v[32:35], a[160:163]\n"
"buffer_load_dwordx4 v224, s[32:35], s95 offen offset:0 lds\n"
"v_mfma_f32_16x16x32_bf16 a[164:167], v[164:167], v[20:23], a[164:167]\n"
"buffer_load_dwordx4 v225, s[32:35], s95 offen offset:1024 lds\n"
"v_mfma_f32_16x16x32_bf16 a[164:167], v[168:171], v[36:39], a[164:167]\n"
"s_add_u32 s59, s59, 1\n"
"s_cmp_eq_u32 s59, 10\n"
"s_cselect_b32 s59, 0, s59\n"
"v_mfma_f32_16x16x32_bf16 a[168:171], v[164:167], v[24:27], a[168:171]\n"
"s_lshl_b32 s50, s59, 7\n"
"ds_read_b64_tr_b16 v[156:157], v209 offset:24576\n"
"ds_read_b64_tr_b16 v[158:159], v209 offset:26112\n"
"v_mfma_f32_16x16x32_bf16 a[168:171], v[168:171], v[40:43], a[168:171]\n"
"s_add_u32 s50, s50, s46\n"
"ds_read_b64_tr_b16 v[160:161], v209 offset:36864\n"
"ds_read_b64_tr_b16 v[162:163], v209 offset:38400\n"
"v_mfma_f32_16x16x32_bf16 a[172:175], v[164:167], v[28:31], a[172:175]\n"
"s_mul_i32 s51, s59, 786432\n"
"buffer_load_dwordx4 v226, s[32:35], s95 offen offset:2048 lds\n"
"v_mfma_f32_16x16x32_bf16 a[172:175], v[168:171], v[44:47], a[172:175]\n"
"s_add_u32 s51, s51, s47\n"
"v_mfma_f32_16x16x32_bf16 a[176:179], v[172:175], v[16:19], a[176:179]\n"
"v_mfma_f32_16x16x32_bf16 a[176:179], v[176:179], v[32:35], a[176:179]\n"
"v_mfma_f32_16x16x32_bf16 a[180:183], v[172:175], v[20:23], a[180:183]\n"
"v_mfma_f32_16x16x32_bf16 a[180:183], v[176:179], v[36:39], a[180:183]\n"
"buffer_load_dwordx4 v227, s[32:35], s95 offen offset:3072 lds\n"
"v_mfma_f32_16x16x32_bf16 a[184:187], v[172:175], v[24:27], a[184:187]\n"
"v_mfma_f32_16x16x32_bf16 a[184:187], v[176:179], v[40:43], a[184:187]\n"
"v_mfma_f32_16x16x32_bf16 a[188:191], v[172:175], v[28:31], a[188:191]\n"
"v_mfma_f32_16x16x32_bf16 a[188:191], v[176:179], v[44:47], a[188:191]\n"
"s_waitcnt lgkmcnt(4)\n"
"v_mfma_f32_16x16x32_bf16 a[0:3], v[148:151], v[48:51], a[0:3]\n"
"v_mfma_f32_16x16x32_bf16 a[0:3], v[152:155], v[64:67], a[0:3]\n"
"s_add_u32 m0, s54, 4096\n"
"v_mfma_f32_16x16x32_bf16 a[4:7], v[148:151], v[52:55], a[4:7]\n"
"buffer_load_dwordx4 v228, s[32:35], s95 offen offset:0 lds\n"
"v_mfma_f32_16x16x32_bf16 a[4:7], v[152:155], v[68:71], a[4:7]\n"
"v_mfma_f32_16x16x32_bf16 a[8:11], v[148:151], v[56:59], a[8:11]\n"
"ds_read_b64_tr_b16 v[164:165], v210 offset:24576\n"
"ds_read_b64_tr_b16 v[166:167], v210 offset:26112\n"
"v_mfma_f32_16x16x32_bf16 a[8:11], v[152:155], v[72:75], a[8:11]\n"
"ds_read_b64_tr_b16 v[168:169], v210 offset:36864\n"
"ds_read_b64_tr_b16 v[170:171], v210 offset:38400\n"
"v_mfma_f32_16x16x32_bf16 a[12:15], v[148:151], v[60:63], a[12:15]\n"
"v_mfma_f32_16x16x32_bf16 a[12:15], v[152:155], v[76:79], a[12:15]\n"
"s_waitcnt lgkmcnt(4)\n"
"v_mfma_f32_16x16x32_bf16 a[16:19], v[156:159], v[48:51], a[16:19]\n"
"buffer_load_dwordx4 v229, s[32:35], s95 offen offset:1024 lds\n"
"v_mfma_f32_16x16x32_bf16 a[16:19], v[160:163], v[64:67], a[16:19]\n"
"v_mfma_f32_16x16x32_bf16 a[20:23], v[156:159], v[52:55], a[20:23]\n"
"v_mfma_f32_16x16x32_bf16 a[20:23], v[160:163], v[68:71], a[20:23]\n"
"v_mfma_f32_16x16x32_bf16 a[24:27], v[156:159], v[56:59], a[24:27]\n"
"ds_read_b64_tr_b16 v[172:173], v211 offset:24576\n"
"ds_read_b64_tr_b16 v[174:175], v211 offset:26112\n"
"v_mfma_f32_16x16x32_bf16 a[24:27], v[160:163], v[72:75], a[24:27]\n"
"s_mov_b32 m0, s53\n"
"ds_read_b64_tr_b16 v[176:177], v211 offset:36864\n"
"ds_read_b64_tr_b16 v[178:179], v211 offset:38400\n"
"v_mfma_f32_16x16x32_bf16 a[28:31], v[156:159], v[60:63], a[28:31]\n"
"buffer_load_dwordx4 v216, s[28:31], s50 offen offset:0 lds\n"
"v_mfma_f32_16x16x32_bf16 a[28:31], v[160:163], v[76:79], a[28:31]\n"
"s_waitcnt lgkmcnt(4)\n"
"v_mfma_f32_16x16x32_bf16 a[32:35], v[164:167], v[48:51], a[32:35]\n"
"v_mfma_f32_16x16x32_bf16 a[32:35], v[168:171], v[64:67], a[32:35]\n"
"v_mfma_f32_16x16x32_bf16 a[36:39], v[164:167], v[52:55], a[36:39]\n"
"v_mfma_f32_16x16x32_bf16 a[36:39], v[168:171], v[68:71], a[36:39]\n"
"v_mfma_f32_16x16x32_bf16 a[40:43], v[164:167], v[56:59], a[40:43]\n"
"ds_read_b64_tr_b16 v[148:149], v208 offset:24704\n"
"ds_read_b64_tr_b16 v[150:151], v208 offset:26240\n"
"v_mfma_f32_16x16x32_bf16 a[40:43], v[168:171], v[72:75], a[40:43]\n"
"ds_read_b64_tr_b16 v[152:153], v208 offset:36992\n"
"ds_read_b64_tr_b16 v[154:155], v208 offset:38528\n"
"v_mfma_f32_16x16x32_bf16 a[44:47], v[164:167], v[60:63], a[44:47]\n"
"buffer_load_dwordx4 v217, s[28:31], s50 offen offset:1024 lds\n"
"v_mfma_f32_16x16x32_bf16 a[44:47], v[168:171], v[76:79], a[44:47]\n"
"s_waitcnt lgkmcnt(4)\n"
"v_mfma_f32_16x16x32_bf16 a[48:51], v[172:175], v[48:51], a[48:51]\n"
"v_mfma_f32_16x16x32_bf16 a[48:51], v[176:179], v[64:67], a[48:51]\n"
"v_mfma_f32_16x16x32_bf16 a[52:55], v[172:175], v[52:55], a[52:55]\n"
"v_mfma_f32_16x16x32_bf16 a[52:55], v[176:179], v[68:71], a[52:55]\n"
"v_mfma_f32_16x16x32_bf16 a[56:59], v[172:175], v[56:59], a[56:59]\n"
"ds_read_b64_tr_b16 v[156:157], v209 offset:24704\n"
"ds_read_b64_tr_b16 v[158:159], v209 offset:26240\n"
"v_mfma_f32_16x16x32_bf16 a[56:59], v[176:179], v[72:75], a[56:59]\n"
"ds_read_b64_tr_b16 v[160:161], v209 offset:36992\n"
"ds_read_b64_tr_b16 v[162:163], v209 offset:38528\n"
"v_mfma_f32_16x16x32_bf16 a[60:63], v[172:175], v[60:63], a[60:63]\n"
"buffer_load_dwordx4 v218, s[28:31], s50 offen offset:2048 lds\n"
"v_mfma_f32_16x16x32_bf16 a[60:63], v[176:179], v[76:79], a[60:63]\n"
"s_waitcnt lgkmcnt(4)\n"
"v_mfma_f32_16x16x32_bf16 a[64:67], v[148:151], v[48:51], a[64:67]\n"
"v_mfma_f32_16x16x32_bf16 a[64:67], v[152:155], v[64:67], a[64:67]\n"
"v_mfma_f32_16x16x32_bf16 a[68:71], v[148:151], v[52:55], a[68:71]\n"
"v_mfma_f32_16x16x32_bf16 a[68:71], v[152:155], v[68:71], a[68:71]\n"
"buffer_load_dwordx4 v219, s[28:31], s50 offen offset:3072 lds\n"
"v_mfma_f32_16x16x32_bf16 a[72:75], v[148:151], v[56:59], a[72:75]\n"
"ds_read_b64_tr_b16 v[164:165], v210 offset:24704\n"
"ds_read_b64_tr_b16 v[166:167], v210 offset:26240\n"
"v_mfma_f32_16x16x32_bf16 a[72:75], v[152:155], v[72:75], a[72:75]\n"
"ds_read_b64_tr_b16 v[168:169], v210 offset:36992\n"
"ds_read_b64_tr_b16 v[170:171], v210 offset:38528\n"
"v_mfma_f32_16x16x32_bf16 a[76:79], v[148:151], v[60:63], a[76:79]\n"
"v_mfma_f32_16x16x32_bf16 a[76:79], v[152:155], v[76:79], a[76:79]\n"
"s_waitcnt lgkmcnt(4)\n"
"v_mfma_f32_16x16x32_bf16 a[80:83], v[156:159], v[48:51], a[80:83]\n"
"v_mfma_f32_16x16x32_bf16 a[80:83], v[160:163], v[64:67], a[80:83]\n"
"s_add_u32 m0, s53, 4096\n"
"v_mfma_f32_16x16x32_bf16 a[84:87], v[156:159], v[52:55], a[84:87]\n"
"buffer_load_dwordx4 v220, s[28:31], s50 offen offset:0 lds\n"
"v_mfma_f32_16x16x32_bf16 a[84:87], v[160:163], v[68:71], a[84:87]\n"
"v_mfma_f32_16x16x32_bf16 a[88:91], v[156:159], v[56:59], a[88:91]\n"
"ds_read_b64_tr_b16 v[172:173], v211 offset:24704\n"
"ds_read_b64_tr_b16 v[174:175], v211 offset:26240\n"
"ds_read_b64_tr_b16 v[148:149], v208 offset:24832\n"
"ds_read_b64_tr_b16 v[150:151], v208 offset:26368\n"
"v_mfma_f32_16x16x32_bf16 a[88:91], v[160:163], v[72:75], a[88:91]\n"
"ds_read_b64_tr_b16 v[176:177], v211 offset:36992\n"
"ds_read_b64_tr_b16 v[178:179], v211 offset:38528\n"
"ds_read_b64_tr_b16 v[152:153], v208 offset:37120\n"
"ds_read_b64_tr_b16 v[154:155], v208 offset:38656\n"
"v_mfma_f32_16x16x32_bf16 a[92:95], v[156:159], v[60:63], a[92:95]\n"
"v_mfma_f32_16x16x32_bf16 a[92:95], v[160:163], v[76:79], a[92:95]\n"
"s_waitcnt lgkmcnt(8)\n"
"v_mfma_f32_16x16x32_bf16 a[96:99], v[164:167], v[48:51], a[96:99]\n"
"v_mfma_f32_16x16x32_bf16 a[96:99], v[168:171], v[64:67], a[96:99]\n"
"buffer_load_dwordx4 v221, s[28:31], s50 offen offset:1024 lds\n"
"v_mfma_f32_16x16x32_bf16 a[100:103], v[164:167], v[52:55], a[100:103]\n"
"ds_read_b64_tr_b16 v[156:157], v209 offset:24832\n"
"ds_read_b64_tr_b16 v[158:159], v209 offset:26368\n"
"v_mfma_f32_16x16x32_bf16 a[100:103], v[168:171], v[68:71], a[100:103]\n"
"ds_read_b64_tr_b16 v[160:161], v209 offset:37120\n"
"ds_read_b64_tr_b16 v[162:163], v209 offset:38656\n"
"v_mfma_f32_16x16x32_bf16 a[104:107], v[164:167], v[56:59], a[104:107]\n"
"v_mfma_f32_16x16x32_bf16 a[104:107], v[168:171], v[72:75], a[104:107]\n"
"v_mfma_f32_16x16x32_bf16 a[108:111], v[164:167], v[60:63], a[108:111]\n"
"v_mfma_f32_16x16x32_bf16 a[108:111], v[168:171], v[76:79], a[108:111]\n"
"s_waitcnt lgkmcnt(6)\n"
"v_mfma_f32_16x16x32_bf16 a[112:115], v[172:175], v[48:51], a[112:115]\n"
"buffer_load_dwordx4 v222, s[28:31], s50 offen offset:2048 lds\n"
"v_mfma_f32_16x16x32_bf16 a[112:115], v[176:179], v[64:67], a[112:115]\n"
"ds_read_b64_tr_b16 v[164:165], v210 offset:24832\n"
"ds_read_b64_tr_b16 v[166:167], v210 offset:26368\n"
"v_mfma_f32_16x16x32_bf16 a[116:119], v[172:175], v[52:55], a[116:119]\n"
"ds_read_b64_tr_b16 v[168:169], v210 offset:37120\n"
"ds_read_b64_tr_b16 v[170:171], v210 offset:38656\n"
"v_mfma_f32_16x16x32_bf16 a[116:119], v[176:179], v[68:71], a[116:119]\n"
"v_mfma_f32_16x16x32_bf16 a[120:123], v[172:175], v[56:59], a[120:123]\n"
"v_mfma_f32_16x16x32_bf16 a[120:123], v[176:179], v[72:75], a[120:123]\n"
"v_mfma_f32_16x16x32_bf16 a[124:127], v[172:175], v[60:63], a[124:127]\n"
"v_mfma_f32_16x16x32_bf16 a[124:127], v[176:179], v[76:79], a[124:127]\n"
"s_waitcnt lgkmcnt(8)\n"
"buffer_load_dwordx4 v223, s[28:31], s50 offen offset:3072 lds\n"
"v_mfma_f32_16x16x32_bf16 a[128:131], v[148:151], v[48:51], a[128:131]\n"
"ds_read_b64_tr_b16 v[172:173], v211 offset:24832\n"
"ds_read_b64_tr_b16 v[174:175], v211 offset:26368\n"
"v_mfma_f32_16x16x32_bf16 a[128:131], v[152:155], v[64:67], a[128:131]\n"
"ds_read_b64_tr_b16 v[176:177], v211 offset:37120\n"
"ds_read_b64_tr_b16 v[178:179], v211 offset:38656\n"
"v_mfma_f32_16x16x32_bf16 a[132:135], v[148:151], v[52:55], a[132:135]\n"
"v_mfma_f32_16x16x32_bf16 a[132:135], v[152:155], v[68:71], a[132:135]\n"
"v_mfma_f32_16x16x32_bf16 a[136:139], v[148:151], v[56:59], a[136:139]\n"
"v_mfma_f32_16x16x32_bf16 a[136:139], v[152:155], v[72:75], a[136:139]\n"
"v_mfma_f32_16x16x32_bf16 a[140:143], v[148:151], v[60:63], a[140:143]\n"
"s_mov_b32 s95, s51\n"
"v_mfma_f32_16x16x32_bf16 a[140:143], v[152:155], v[76:79], a[140:143]\n"
"s_waitcnt lgkmcnt(8)\n"
"v_mfma_f32_16x16x32_bf16 a[144:147], v[156:159], v[48:51], a[144:147]\n"
"v_mfma_f32_16x16x32_bf16 a[144:147], v[160:163], v[64:67], a[144:147]\n"
"s_waitcnt vmcnt(8) lgkmcnt(0)\n"
"s_barrier\n"
"v_mfma_f32_16x16x32_bf16 a[148:151], v[156:159], v[52:55], a[148:151]\n"
"ds_read_b64_tr_b16 v[148:149], v208 offset:49152\n"
"ds_read_b64_tr_b16 v[150:151], v208 offset:50688\n"
"v_mfma_f32_16x16x32_bf16 a[148:151], v[160:163], v[68:71], a[148:151]\n"
"ds_read_b64_tr_b16 v[152:153], v208 offset:61440\n"
"ds_read_b64_tr_b16 v[154:155], v208 offset:62976\n"
"v_mfma_f32_16x16x32_bf16 a[152:155], v[156:159], v[56:59], a[152:155]\n"
"ds_read_b128 v[16:19], v212 offset:0\n"
"ds_read_b128 v[32:35], v213 offset:0\n"
"v_mfma_f32_16x16x32_bf16 a[152:155], v[160:163], v[72:75], a[152:155]\n"
"ds_read_b128 v[20:23], v212 offset:2048\n"
"ds_read_b128 v[36:39], v213 offset:2048\n"
"v_mfma_f32_16x16x32_bf16 a[156:159], v[156:159], v[60:63], a[156:159]\n"
"ds_read_b128 v[24:27], v212 offset:4096\n"
"ds_read_b128 v[40:43], v213 offset:4096\n"
"v_mfma_f32_16x16x32_bf16 a[156:159], v[160:163], v[76:79], a[156:159]\n"
"ds_read_b128 v[28:31], v212 offset:6144\n"
"ds_read_b128 v[44:47], v213 offset:6144\n"
"v_mfma_f32_16x16x32_bf16 a[160:163], v[164:167], v[48:51], a[160:163]\n"
"s_mov_b32 m0, s55\n"
"v_mfma_f32_16x16x32_bf16 a[160:163], v[168:171], v[64:67], a[160:163]\n"
"buffer_load_dwordx4 v224, s[32:35], s95 offen offset:0 lds\n"
"v_mfma_f32_16x16x32_bf16 a[164:167], v[164:167], v[52:55], a[164:167]\n"
"buffer_load_dwordx4 v225, s[32:35], s95 offen offset:1024 lds\n"
"v_mfma_f32_16x16x32_bf16 a[164:167], v[168:171], v[68:71], a[164:167]\n"
"s_add_u32 s59, s59, 1\n"
"s_cmp_eq_u32 s59, 10\n"
"s_cselect_b32 s59, 0, s59\n"
"v_mfma_f32_16x16x32_bf16 a[168:171], v[164:167], v[56:59], a[168:171]\n"
"s_lshl_b32 s50, s59, 7\n"
"ds_read_b64_tr_b16 v[156:157], v209 offset:49152\n"
"ds_read_b64_tr_b16 v[158:159], v209 offset:50688\n"
"v_mfma_f32_16x16x32_bf16 a[168:171], v[168:171], v[72:75], a[168:171]\n"
"s_add_u32 s50, s50, s46\n"
"ds_read_b64_tr_b16 v[160:161], v209 offset:61440\n"
"ds_read_b64_tr_b16 v[162:163], v209 offset:62976\n"
"v_mfma_f32_16x16x32_bf16 a[172:175], v[164:167], v[60:63], a[172:175]\n"
"s_mul_i32 s51, s59, 786432\n"
"buffer_load_dwordx4 v226, s[32:35], s95 offen offset:2048 lds\n"
"v_mfma_f32_16x16x32_bf16 a[172:175], v[168:171], v[76:79], a[172:175]\n"
"s_add_u32 s51, s51, s47\n"
"v_mfma_f32_16x16x32_bf16 a[176:179], v[172:175], v[48:51], a[176:179]\n"
"v_mfma_f32_16x16x32_bf16 a[176:179], v[176:179], v[64:67], a[176:179]\n"
"v_mfma_f32_16x16x32_bf16 a[180:183], v[172:175], v[52:55], a[180:183]\n"
"v_mfma_f32_16x16x32_bf16 a[180:183], v[176:179], v[68:71], a[180:183]\n"
"buffer_load_dwordx4 v227, s[32:35], s95 offen offset:3072 lds\n"
"v_mfma_f32_16x16x32_bf16 a[184:187], v[172:175], v[56:59], a[184:187]\n"
"v_mfma_f32_16x16x32_bf16 a[184:187], v[176:179], v[72:75], a[184:187]\n"
"v_mfma_f32_16x16x32_bf16 a[188:191], v[172:175], v[60:63], a[188:191]\n"
"v_mfma_f32_16x16x32_bf16 a[188:191], v[176:179], v[76:79], a[188:191]\n"
"s_waitcnt lgkmcnt(4)\n"
"v_mfma_f32_16x16x32_bf16 a[0:3], v[148:151], v[16:19], a[0:3]\n"
"v_mfma_f32_16x16x32_bf16 a[0:3], v[152:155], v[32:35], a[0:3]\n"
"s_add_u32 m0, s55, 4096\n"
"v_mfma_f32_16x16x32_bf16 a[4:7], v[148:151], v[20:23], a[4:7]\n"
"buffer_load_dwordx4 v228, s[32:35], s95 offen offset:0 lds\n"
"v_mfma_f32_16x16x32_bf16 a[4:7], v[152:155], v[36:39], a[4:7]\n"
"v_mfma_f32_16x16x32_bf16 a[8:11], v[148:151], v[24:27], a[8:11]\n"
"ds_read_b64_tr_b16 v[164:165], v210 offset:49152\n"
"ds_read_b64_tr_b16 v[166:167], v210 offset:50688\n"
"v_mfma_f32_16x16x32_bf16 a[8:11], v[152:155], v[40:43], a[8:11]\n"
"ds_read_b64_tr_b16 v[168:169], v210 offset:61440\n"
"ds_read_b64_tr_b16 v[170:171], v210 offset:62976\n"
"v_mfma_f32_16x16x32_bf16 a[12:15], v[148:151], v[28:31], a[12:15]\n"
"v_mfma_f32_16x16x32_bf16 a[12:15], v[152:155], v[44:47], a[12:15]\n"
"s_waitcnt lgkmcnt(4)\n"
"v_mfma_f32_16x16x32_bf16 a[16:19], v[156:159], v[16:19], a[16:19]\n"
"buffer_load_dwordx4 v229, s[32:35], s95 offen offset:1024 lds\n"
"v_mfma_f32_16x16x32_bf16 a[16:19], v[160:163], v[32:35], a[16:19]\n"
"v_mfma_f32_16x16x32_bf16 a[20:23], v[156:159], v[20:23], a[20:23]\n"
"v_mfma_f32_16x16x32_bf16 a[20:23], v[160:163], v[36:39], a[20:23]\n"
"v_mfma_f32_16x16x32_bf16 a[24:27], v[156:159], v[24:27], a[24:27]\n"
"ds_read_b64_tr_b16 v[172:173], v211 offset:49152\n"
"ds_read_b64_tr_b16 v[174:175], v211 offset:50688\n"
"v_mfma_f32_16x16x32_bf16 a[24:27], v[160:163], v[40:43], a[24:27]\n"
"s_mov_b32 m0, s52\n"
"ds_read_b64_tr_b16 v[176:177], v211 offset:61440\n"
"ds_read_b64_tr_b16 v[178:179], v211 offset:62976\n"
"v_mfma_f32_16x16x32_bf16 a[28:31], v[156:159], v[28:31], a[28:31]\n"
"buffer_load_dwordx4 v216, s[28:31], s50 offen offset:0 lds\n"
"v_mfma_f32_16x16x32_bf16 a[28:31], v[160:163], v[44:47], a[28:31]\n"
"s_waitcnt lgkmcnt(4)\n"
"v_mfma_f32_16x16x32_bf16 a[32:35], v[164:167], v[16:19], a[32:35]\n"
"v_mfma_f32_16x16x32_bf16 a[32:35], v[168:171], v[32:35], a[32:35]\n"
"v_mfma_f32_16x16x32_bf16 a[36:39], v[164:167], v[20:23], a[36:39]\n"
"v_mfma_f32_16x16x32_bf16 a[36:39], v[168:171], v[36:39], a[36:39]\n"
"v_mfma_f32_16x16x32_bf16 a[40:43], v[164:167], v[24:27], a[40:43]\n"
"ds_read_b64_tr_b16 v[148:149], v208 offset:49280\n"
"ds_read_b64_tr_b16 v[150:151], v208 offset:50816\n"
"v_mfma_f32_16x16x32_bf16 a[40:43], v[168:171], v[40:43], a[40:43]\n"
"ds_read_b64_tr_b16 v[152:153], v208 offset:61568\n"
"ds_read_b64_tr_b16 v[154:155], v208 offset:63104\n"
"v_mfma_f32_16x16x32_bf16 a[44:47], v[164:167], v[28:31], a[44:47]\n"
"buffer_load_dwordx4 v217, s[28:31], s50 offen offset:1024 lds\n"
"v_mfma_f32_16x16x32_bf16 a[44:47], v[168:171], v[44:47], a[44:47]\n"
"s_waitcnt lgkmcnt(4)\n"
"v_mfma_f32_16x16x32_bf16 a[48:51], v[172:175], v[16:19], a[48:51]\n"
"v_mfma_f32_16x16x32_bf16 a[48:51], v[176:179], v[32:35], a[48:51]\n"
"v_mfma_f32_16x16x32_bf16 a[52:55], v[172:175], v[20:23], a[52:55]\n"
"v_mfma_f32_16x16x32_bf16 a[52:55], v[176:179], v[36:39], a[52:55]\n"
"v_mfma_f32_16x16x32_bf16 a[56:59], v[172:175], v[24:27], a[56:59]\n"
"ds_read_b64_tr_b16 v[156:157], v209 offset:49280\n"
"ds_read_b64_tr_b16 v[158:159], v209 offset:50816\n"
"v_mfma_f32_16x16x32_bf16 a[56:59], v[176:179], v[40:43], a[56:59]\n"
"ds_read_b64_tr_b16 v[160:161], v209 offset:61568\n"
"ds_read_b64_tr_b16 v[162:163], v209 offset:63104\n"
"v_mfma_f32_16x16x32_bf16 a[60:63], v[172:175], v[28:31], a[60:63]\n"
"buffer_load_dwordx4 v218, s[28:31], s50 offen offset:2048 lds\n"
"v_mfma_f32_16x16x32_bf16 a[60:63], v[176:179], v[44:47], a[60:63]\n"
"s_waitcnt lgkmcnt(4)\n"
"v_mfma_f32_16x16x32_bf16 a[64:67], v[148:151], v[16:19], a[64:67]\n"
"v_mfma_f32_16x16x32_bf16 a[64:67], v[152:155], v[32:35], a[64:67]\n"
"v_mfma_f32_16x16x32_bf16 a[68:71], v[148:151], v[20:23], a[68:71]\n"
"v_mfma_f32_16x16x32_bf16 a[68:71], v[152:155], v[36:39], a[68:71]\n"
"buffer_load_dwordx4 v219, s[28:31], s50 offen offset:3072 lds\n"
"v_mfma_f32_16x16x32_bf16 a[72:75], v[148:151], v[24:27], a[72:75]\n"
"ds_read_b64_tr_b16 v[164:165], v210 offset:49280\n"
"ds_read_b64_tr_b16 v[166:167], v210 offset:50816\n"
"v_mfma_f32_16x16x32_bf16 a[72:75], v[152:155], v[40:43], a[72:75]\n"
"ds_read_b64_tr_b16 v[168:169], v210 offset:61568\n"
"ds_read_b64_tr_b16 v[170:171], v210 offset:63104\n"
"v_mfma_f32_16x16x32_bf16 a[76:79], v[148:151], v[28:31], a[76:79]\n"
"v_mfma_f32_16x16x32_bf16 a[76:79], v[152:155], v[44:47], a[76:79]\n"
"s_waitcnt lgkmcnt(4)\n"
"v_mfma_f32_16x16x32_bf16 a[80:83], v[156:159], v[16:19], a[80:83]\n"
"v_mfma_f32_16x16x32_bf16 a[80:83], v[160:163], v[32:35], a[80:83]\n"
"s_add_u32 m0, s52, 4096\n"
"v_mfma_f32_16x16x32_bf16 a[84:87], v[156:159], v[20:23], a[84:87]\n"
"buffer_load_dwordx4 v220, s[28:31], s50 offen offset:0 lds\n"
"v_mfma_f32_16x16x32_bf16 a[84:87], v[160:163], v[36:39], a[84:87]\n"
"v_mfma_f32_16x16x32_bf16 a[88:91], v[156:159], v[24:27], a[88:91]\n"
"ds_read_b64_tr_b16 v[172:173], v211 offset:49280\n"
"ds_read_b64_tr_b16 v[174:175], v211 offset:50816\n"
"ds_read_b64_tr_b16 v[148:149], v208 offset:49408\n"
"ds_read_b64_tr_b16 v[150:151], v208 offset:50944\n"
"v_mfma_f32_16x16x32_bf16 a[88:91], v[160:163], v[40:43], a[88:91]\n"
"ds_read_b64_tr_b16 v[176:177], v211 offset:61568\n"
"ds_read_b64_tr_b16 v[178:179], v211 offset:63104\n"
"ds_read_b64_tr_b16 v[152:153], v208 offset:61696\n"
"ds_read_b64_tr_b16 v[154:155], v208 offset:63232\n"
"v_mfma_f32_16x16x32_bf16 a[92:95], v[156:159], v[28:31], a[92:95]\n"
"v_mfma_f32_16x16x32_bf16 a[92:95], v[160:163], v[44:47], a[92:95]\n"
"s_waitcnt lgkmcnt(8)\n"
"v_mfma_f32_16x16x32_bf16 a[96:99], v[164:167], v[16:19], a[96:99]\n"
"v_mfma_f32_16x16x32_bf16 a[96:99], v[168:171], v[32:35], a[96:99]\n"
"buffer_load_dwordx4 v221, s[28:31], s50 offen offset:1024 lds\n"
"v_mfma_f32_16x16x32_bf16 a[100:103], v[164:167], v[20:23], a[100:103]\n"
"ds_read_b64_tr_b16 v[156:157], v209 offset:49408\n"
"ds_read_b64_tr_b16 v[158:159], v209 offset:50944\n"
"v_mfma_f32_16x16x32_bf16 a[100:103], v[168:171], v[36:39], a[100:103]\n"
"ds_read_b64_tr_b16 v[160:161], v209 offset:61696\n"
"ds_read_b64_tr_b16 v[162:163], v209 offset:63232\n"
"v_mfma_f32_16x16x32_bf16 a[104:107], v[164:167], v[24:27], a[104:107]\n"
"v_mfma_f32_16x16x32_bf16 a[104:107], v[168:171], v[40:43], a[104:107]\n"
"v_mfma_f32_16x16x32_bf16 a[108:111], v[164:167], v[28:31], a[108:111]\n"
"v_mfma_f32_16x16x32_bf16 a[108:111], v[168:171], v[44:47], a[108:111]\n"
"s_waitcnt lgkmcnt(6)\n"
"v_mfma_f32_16x16x32_bf16 a[112:115], v[172:175], v[16:19], a[112:115]\n"
"buffer_load_dwordx4 v222, s[28:31], s50 offen offset:2048 lds\n"
"v_mfma_f32_16x16x32_bf16 a[112:115], v[176:179], v[32:35], a[112:115]\n"
"ds_read_b64_tr_b16 v[164:165], v210 offset:49408\n"
"ds_read_b64_tr_b16 v[166:167], v210 offset:50944\n"
"v_mfma_f32_16x16x32_bf16 a[116:119], v[172:175], v[20:23], a[116:119]\n"
"ds_read_b64_tr_b16 v[168:169], v210 offset:61696\n"
"ds_read_b64_tr_b16 v[170:171], v210 offset:63232\n"
"v_mfma_f32_16x16x32_bf16 a[116:119], v[176:179], v[36:39], a[116:119]\n"
"v_mfma_f32_16x16x32_bf16 a[120:123], v[172:175], v[24:27], a[120:123]\n"
"v_mfma_f32_16x16x32_bf16 a[120:123], v[176:179], v[40:43], a[120:123]\n"
"v_mfma_f32_16x16x32_bf16 a[124:127], v[172:175], v[28:31], a[124:127]\n"
"v_mfma_f32_16x16x32_bf16 a[124:127], v[176:179], v[44:47], a[124:127]\n"
"s_waitcnt lgkmcnt(8)\n"
"buffer_load_dwordx4 v223, s[28:31], s50 offen offset:3072 lds\n"
"v_mfma_f32_16x16x32_bf16 a[128:131], v[148:151], v[16:19], a[128:131]\n"
"ds_read_b64_tr_b16 v[172:173], v211 offset:49408\n"
"ds_read_b64_tr_b16 v[174:175], v211 offset:50944\n"
"v_mfma_f32_16x16x32_bf16 a[128:131], v[152:155], v[32:35], a[128:131]\n"
"s_mov_b32 m0, s89\n"
"ds_read_b64_tr_b16 v[176:177], v211 offset:61696\n"
"ds_read_b64_tr_b16 v[178:179], v211 offset:63232\n"
"v_mfma_f32_16x16x32_bf16 a[132:135], v[148:151], v[20:23], a[132:135]\n"
"buffer_load_dwordx4 v202, s[20:23], s86 offen offset:0 nt lds\n"
"v_mfma_f32_16x16x32_bf16 a[132:135], v[152:155], v[36:39], a[132:135]\n"
"v_mfma_f32_16x16x32_bf16 a[136:139], v[148:151], v[24:27], a[136:139]\n"
"v_mfma_f32_16x16x32_bf16 a[136:139], v[152:155], v[40:43], a[136:139]\n"
"buffer_load_dwordx4 v203, s[20:23], s86 offen offset:1024 nt lds\n"
"v_mfma_f32_16x16x32_bf16 a[140:143], v[148:151], v[28:31], a[140:143]\n"
"s_mov_b32 s95, s51\n"
"v_mfma_f32_16x16x32_bf16 a[140:143], v[152:155], v[44:47], a[140:143]\n"
"s_waitcnt lgkmcnt(8)\n"
"v_mfma_f32_16x16x32_bf16 a[144:147], v[156:159], v[16:19], a[144:147]\n"
"v_mfma_f32_16x16x32_bf16 a[144:147], v[160:163], v[32:35], a[144:147]\n"
"s_waitcnt vmcnt(10) lgkmcnt(0)\n"
"s_barrier\n"
"v_mfma_f32_16x16x32_bf16 a[148:151], v[156:159], v[20:23], a[148:151]\n"
"ds_read_b64_tr_b16 v[148:149], v208 offset:24576\n"
"ds_read_b64_tr_b16 v[150:151], v208 offset:26112\n"
"v_mfma_f32_16x16x32_bf16 a[148:151], v[160:163], v[36:39], a[148:151]\n"
"ds_read_b64_tr_b16 v[152:153], v208 offset:36864\n"
"ds_read_b64_tr_b16 v[154:155], v208 offset:38400\n"
"v_mfma_f32_16x16x32_bf16 a[152:155], v[156:159], v[24:27], a[152:155]\n"
"ds_read_b128 v[48:51], v214 offset:0\n"
"ds_read_b128 v[64:67], v215 offset:0\n"
"v_mfma_f32_16x16x32_bf16 a[152:155], v[160:163], v[40:43], a[152:155]\n"
"ds_read_b128 v[52:55], v214 offset:2048\n"
"ds_read_b128 v[68:71], v215 offset:2048\n"
"v_mfma_f32_16x16x32_bf16 a[156:159], v[156:159], v[28:31], a[156:159]\n"
"ds_read_b128 v[56:59], v214 offset:4096\n"
"ds_read_b128 v[72:75], v215 offset:4096\n"
"v_mfma_f32_16x16x32_bf16 a[156:159], v[160:163], v[44:47], a[156:159]\n"
"ds_read_b128 v[60:63], v214 offset:6144\n"
"ds_read_b128 v[76:79], v215 offset:6144\n"
"v_mfma_f32_16x16x32_bf16 a[160:163], v[164:167], v[16:19], a[160:163]\n"
"s_mov_b32 m0, s54\n"
"v_mfma_f32_16x16x32_bf16 a[160:163], v[168:171], v[32:35], a[160:163]\n"
"buffer_load_dwordx4 v224, s[32:35], s95 offen offset:0 lds\n"
"v_mfma_f32_16x16x32_bf16 a[164:167], v[164:167], v[20:23], a[164:167]\n"
"buffer_load_dwordx4 v225, s[32:35], s95 offen offset:1024 lds\n"
"v_mfma_f32_16x16x32_bf16 a[164:167], v[168:171], v[36:39], a[164:167]\n"
"s_add_u32 s59, s59, 1\n"
"s_cmp_eq_u32 s59, 10\n"
"s_cselect_b32 s59, 0, s59\n"
"v_mfma_f32_16x16x32_bf16 a[168:171], v[164:167], v[24:27], a[168:171]\n"
"s_lshl_b32 s50, s59, 7\n"
"ds_read_b64_tr_b16 v[156:157], v209 offset:24576\n"
"ds_read_b64_tr_b16 v[158:159], v209 offset:26112\n"
"v_mfma_f32_16x16x32_bf16 a[168:171], v[168:171], v[40:43], a[168:171]\n"
"s_add_u32 s50, s50, s46\n"
"ds_read_b64_tr_b16 v[160:161], v209 offset:36864\n"
"ds_read_b64_tr_b16 v[162:163], v209 offset:38400\n"
"v_mfma_f32_16x16x32_bf16 a[172:175], v[164:167], v[28:31], a[172:175]\n"
"s_mul_i32 s51, s59, 786432\n"
"buffer_load_dwordx4 v226, s[32:35], s95 offen offset:2048 lds\n"
"v_mfma_f32_16x16x32_bf16 a[172:175], v[168:171], v[44:47], a[172:175]\n"
"s_add_u32 s51, s51, s47\n"
"v_mfma_f32_16x16x32_bf16 a[176:179], v[172:175], v[16:19], a[176:179]\n"
"v_mfma_f32_16x16x32_bf16 a[176:179], v[176:179], v[32:35], a[176:179]\n"
"v_mfma_f32_16x16x32_bf16 a[180:183], v[172:175], v[20:23], a[180:183]\n"
"v_mfma_f32_16x16x32_bf16 a[180:183], v[176:179], v[36:39], a[180:183]\n"
"buffer_load_dwordx4 v227, s[32:35], s95 offen offset:3072 lds\n"
"v_mfma_f32_16x16x32_bf16 a[184:187], v[172:175], v[24:27], a[184:187]\n"
"v_mfma_f32_16x16x32_bf16 a[184:187], v[176:179], v[40:43], a[184:187]\n"
"v_mfma_f32_16x16x32_bf16 a[188:191], v[172:175], v[28:31], a[188:191]\n"
"v_mfma_f32_16x16x32_bf16 a[188:191], v[176:179], v[44:47], a[188:191]\n"
"s_waitcnt lgkmcnt(4)\n"
"v_mfma_f32_16x16x32_bf16 a[0:3], v[148:151], v[48:51], a[0:3]\n"
"v_mfma_f32_16x16x32_bf16 a[0:3], v[152:155], v[64:67], a[0:3]\n"
"s_add_u32 m0, s54, 4096\n"
"v_mfma_f32_16x16x32_bf16 a[4:7], v[148:151], v[52:55], a[4:7]\n"
"buffer_load_dwordx4 v228, s[32:35], s95 offen offset:0 lds\n"
"v_mfma_f32_16x16x32_bf16 a[4:7], v[152:155], v[68:71], a[4:7]\n"
"v_mfma_f32_16x16x32_bf16 a[8:11], v[148:151], v[56:59], a[8:11]\n"
"ds_read_b64_tr_b16 v[164:165], v210 offset:24576\n"
"ds_read_b64_tr_b16 v[166:167], v210 offset:26112\n"
"v_mfma_f32_16x16x32_bf16 a[8:11], v[152:155], v[72:75], a[8:11]\n"
"ds_read_b64_tr_b16 v[168:169], v210 offset:36864\n"
"ds_read_b64_tr_b16 v[170:171], v210 offset:38400\n"
"v_mfma_f32_16x16x32_bf16 a[12:15], v[148:151], v[60:63], a[12:15]\n"
"v_mfma_f32_16x16x32_bf16 a[12:15], v[152:155], v[76:79], a[12:15]\n"
"s_waitcnt lgkmcnt(4)\n"
"v_mfma_f32_16x16x32_bf16 a[16:19], v[156:159], v[48:51], a[16:19]\n"
"buffer_load_dwordx4 v229, s[32:35], s95 offen offset:1024 lds\n"
"v_mfma_f32_16x16x32_bf16 a[16:19], v[160:163], v[64:67], a[16:19]\n"
"v_mfma_f32_16x16x32_bf16 a[20:23], v[156:159], v[52:55], a[20:23]\n"
"v_mfma_f32_16x16x32_bf16 a[20:23], v[160:163], v[68:71], a[20:23]\n"
"v_mfma_f32_16x16x32_bf16 a[24:27], v[156:159], v[56:59], a[24:27]\n"
"ds_read_b64_tr_b16 v[172:173], v211 offset:24576\n"
"ds_read_b64_tr_b16 v[174:175], v211 offset:26112\n"
"v_mfma_f32_16x16x32_bf16 a[24:27], v[160:163], v[72:75], a[24:27]\n"
"s_mov_b32 m0, s53\n"
"ds_read_b64_tr_b16 v[176:177], v211 offset:36864\n"
"ds_read_b64_tr_b16 v[178:179], v211 offset:38400\n"
"v_mfma_f32_16x16x32_bf16 a[28:31], v[156:159], v[60:63], a[28:31]\n"
"buffer_load_dwordx4 v216, s[28:31], s50 offen offset:0 lds\n"
"v_mfma_f32_16x16x32_bf16 a[28:31], v[160:163], v[76:79], a[28:31]\n"
"s_waitcnt lgkmcnt(4)\n"
"v_mfma_f32_16x16x32_bf16 a[32:35], v[164:167], v[48:51], a[32:35]\n"
"v_mfma_f32_16x16x32_bf16 a[32:35], v[168:171], v[64:67], a[32:35]\n"
"v_mfma_f32_16x16x32_bf16 a[36:39], v[164:167], v[52:55], a[36:39]\n"
"v_mfma_f32_16x16x32_bf16 a[36:39], v[168:171], v[68:71], a[36:39]\n"
"v_mfma_f32_16x16x32_bf16 a[40:43], v[164:167], v[56:59], a[40:43]\n"
"ds_read_b64_tr_b16 v[148:149], v208 offset:24704\n"
"ds_read_b64_tr_b16 v[150:151], v208 offset:26240\n"
"v_mfma_f32_16x16x32_bf16 a[40:43], v[168:171], v[72:75], a[40:43]\n"
"ds_read_b64_tr_b16 v[152:153], v208 offset:36992\n"
"ds_read_b64_tr_b16 v[154:155], v208 offset:38528\n"
"v_mfma_f32_16x16x32_bf16 a[44:47], v[164:167], v[60:63], a[44:47]\n"
"buffer_load_dwordx4 v217, s[28:31], s50 offen offset:1024 lds\n"
"v_mfma_f32_16x16x32_bf16 a[44:47], v[168:171], v[76:79], a[44:47]\n"
"s_waitcnt lgkmcnt(4)\n"
"v_mfma_f32_16x16x32_bf16 a[48:51], v[172:175], v[48:51], a[48:51]\n"
"v_mfma_f32_16x16x32_bf16 a[48:51], v[176:179], v[64:67], a[48:51]\n"
"v_mfma_f32_16x16x32_bf16 a[52:55], v[172:175], v[52:55], a[52:55]\n"
"v_mfma_f32_16x16x32_bf16 a[52:55], v[176:179], v[68:71], a[52:55]\n"
"v_mfma_f32_16x16x32_bf16 a[56:59], v[172:175], v[56:59], a[56:59]\n"
"ds_read_b64_tr_b16 v[156:157], v209 offset:24704\n"
"ds_read_b64_tr_b16 v[158:159], v209 offset:26240\n"
"v_mfma_f32_16x16x32_bf16 a[56:59], v[176:179], v[72:75], a[56:59]\n"
"ds_read_b64_tr_b16 v[160:161], v209 offset:36992\n"
"ds_read_b64_tr_b16 v[162:163], v209 offset:38528\n"
"v_mfma_f32_16x16x32_bf16 a[60:63], v[172:175], v[60:63], a[60:63]\n"
"buffer_load_dwordx4 v218, s[28:31], s50 offen offset:2048 lds\n"
"v_mfma_f32_16x16x32_bf16 a[60:63], v[176:179], v[76:79], a[60:63]\n"
"s_waitcnt lgkmcnt(4)\n"
"v_mfma_f32_16x16x32_bf16 a[64:67], v[148:151], v[48:51], a[64:67]\n"
"v_mfma_f32_16x16x32_bf16 a[64:67], v[152:155], v[64:67], a[64:67]\n"
"v_mfma_f32_16x16x32_bf16 a[68:71], v[148:151], v[52:55], a[68:71]\n"
"v_mfma_f32_16x16x32_bf16 a[68:71], v[152:155], v[68:71], a[68:71]\n"
"buffer_load_dwordx4 v219, s[28:31], s50 offen offset:3072 lds\n"
"v_mfma_f32_16x16x32_bf16 a[72:75], v[148:151], v[56:59], a[72:75]\n"
"ds_read_b64_tr_b16 v[164:165], v210 offset:24704\n"
"ds_read_b64_tr_b16 v[166:167], v210 offset:26240\n"
"v_mfma_f32_16x16x32_bf16 a[72:75], v[152:155], v[72:75], a[72:75]\n"
"ds_read_b64_tr_b16 v[168:169], v210 offset:36992\n"
"ds_read_b64_tr_b16 v[170:171], v210 offset:38528\n"
"v_mfma_f32_16x16x32_bf16 a[76:79], v[148:151], v[60:63], a[76:79]\n"
"v_mfma_f32_16x16x32_bf16 a[76:79], v[152:155], v[76:79], a[76:79]\n"
"s_waitcnt lgkmcnt(4)\n"
"v_mfma_f32_16x16x32_bf16 a[80:83], v[156:159], v[48:51], a[80:83]\n"
"v_mfma_f32_16x16x32_bf16 a[80:83], v[160:163], v[64:67], a[80:83]\n"
"s_add_u32 m0, s53, 4096\n"
"v_mfma_f32_16x16x32_bf16 a[84:87], v[156:159], v[52:55], a[84:87]\n"
"buffer_load_dwordx4 v220, s[28:31], s50 offen offset:0 lds\n"
"v_mfma_f32_16x16x32_bf16 a[84:87], v[160:163], v[68:71], a[84:87]\n"
"v_mfma_f32_16x16x32_bf16 a[88:91], v[156:159], v[56:59], a[88:91]\n"
"ds_read_b64_tr_b16 v[172:173], v211 offset:24704\n"
"ds_read_b64_tr_b16 v[174:175], v211 offset:26240\n"
"ds_read_b64_tr_b16 v[148:149], v208 offset:24832\n"
"ds_read_b64_tr_b16 v[150:151], v208 offset:26368\n"
"v_mfma_f32_16x16x32_bf16 a[88:91], v[160:163], v[72:75], a[88:91]\n"
"ds_read_b64_tr_b16 v[176:177], v211 offset:36992\n"
"ds_read_b64_tr_b16 v[178:179], v211 offset:38528\n"
"ds_read_b64_tr_b16 v[152:153], v208 offset:37120\n"
"ds_read_b64_tr_b16 v[154:155], v208 offset:38656\n"
"v_mfma_f32_16x16x32_bf16 a[92:95], v[156:159], v[60:63], a[92:95]\n"
"v_mfma_f32_16x16x32_bf16 a[92:95], v[160:163], v[76:79], a[92:95]\n"
"s_waitcnt lgkmcnt(8)\n"
"v_mfma_f32_16x16x32_bf16 a[96:99], v[164:167], v[48:51], a[96:99]\n"
"v_mfma_f32_16x16x32_bf16 a[96:99], v[168:171], v[64:67], a[96:99]\n"
"buffer_load_dwordx4 v221, s[28:31], s50 offen offset:1024 lds\n"
"v_mfma_f32_16x16x32_bf16 a[100:103], v[164:167], v[52:55], a[100:103]\n"
"ds_read_b64_tr_b16 v[156:157], v209 offset:24832\n"
"ds_read_b64_tr_b16 v[158:159], v209 offset:26368\n"
"v_mfma_f32_16x16x32_bf16 a[100:103], v[168:171], v[68:71], a[100:103]\n"
"ds_read_b64_tr_b16 v[160:161], v209 offset:37120\n"
"ds_read_b64_tr_b16 v[162:163], v209 offset:38656\n"
"v_mfma_f32_16x16x32_bf16 a[104:107], v[164:167], v[56:59], a[104:107]\n"
"v_mfma_f32_16x16x32_bf16 a[104:107], v[168:171], v[72:75], a[104:107]\n"
"v_mfma_f32_16x16x32_bf16 a[108:111], v[164:167], v[60:63], a[108:111]\n"
"v_mfma_f32_16x16x32_bf16 a[108:111], v[168:171], v[76:79], a[108:111]\n"
"s_waitcnt lgkmcnt(6)\n"
"v_mfma_f32_16x16x32_bf16 a[112:115], v[172:175], v[48:51], a[112:115]\n"
"buffer_load_dwordx4 v222, s[28:31], s50 offen offset:2048 lds\n"
"v_mfma_f32_16x16x32_bf16 a[112:115], v[176:179], v[64:67], a[112:115]\n"
"ds_read_b64_tr_b16 v[164:165], v210 offset:24832\n"
"ds_read_b64_tr_b16 v[166:167], v210 offset:26368\n"
"v_mfma_f32_16x16x32_bf16 a[116:119], v[172:175], v[52:55], a[116:119]\n"
"ds_read_b64_tr_b16 v[168:169], v210 offset:37120\n"
"ds_read_b64_tr_b16 v[170:171], v210 offset:38656\n"
"v_mfma_f32_16x16x32_bf16 a[116:119], v[176:179], v[68:71], a[116:119]\n"
"v_mfma_f32_16x16x32_bf16 a[120:123], v[172:175], v[56:59], a[120:123]\n"
"v_mfma_f32_16x16x32_bf16 a[120:123], v[176:179], v[72:75], a[120:123]\n"
"v_mfma_f32_16x16x32_bf16 a[124:127], v[172:175], v[60:63], a[124:127]\n"
"v_mfma_f32_16x16x32_bf16 a[124:127], v[176:179], v[76:79], a[124:127]\n"
"s_waitcnt lgkmcnt(8)\n"
"buffer_load_dwordx4 v223, s[28:31], s50 offen offset:3072 lds\n"
"v_mfma_f32_16x16x32_bf16 a[128:131], v[148:151], v[48:51], a[128:131]\n"
"ds_read_b64_tr_b16 v[172:173], v211 offset:24832\n"
"ds_read_b64_tr_b16 v[174:175], v211 offset:26368\n"
"v_mfma_f32_16x16x32_bf16 a[128:131], v[152:155], v[64:67], a[128:131]\n"
"s_mov_b32 m0, s89\n"
"ds_read_b64_tr_b16 v[176:177], v211 offset:37120\n"
"ds_read_b64_tr_b16 v[178:179], v211 offset:38656\n"
"v_mfma_f32_16x16x32_bf16 a[132:135], v[148:151], v[52:55], a[132:135]\n"
"buffer_load_dwordx4 v204, s[20:23], s86 offen offset:2048 nt lds\n"
"v_mfma_f32_16x16x32_bf16 a[132:135], v[152:155], v[68:71], a[132:135]\n"
"v_mfma_f32_16x16x32_bf16 a[136:139], v[148:151], v[56:59], a[136:139]\n"
"v_mfma_f32_16x16x32_bf16 a[136:139], v[152:155], v[72:75], a[136:139]\n"
"buffer_load_dwordx4 v205, s[20:23], s86 offen offset:3072 nt lds\n"
"v_mfma_f32_16x16x32_bf16 a[140:143], v[148:151], v[60:63], a[140:143]\n"
"s_mov_b32 s95, s51\n"
"v_mfma_f32_16x16x32_bf16 a[140:143], v[152:155], v[76:79], a[140:143]\n"
"s_waitcnt lgkmcnt(8)\n"
"v_mfma_f32_16x16x32_bf16 a[144:147], v[156:159], v[48:51], a[144:147]\n"
"v_mfma_f32_16x16x32_bf16 a[144:147], v[160:163], v[64:67], a[144:147]\n"
"s_waitcnt vmcnt(10) lgkmcnt(0)\n"
"s_barrier\n"
"v_mfma_f32_16x16x32_bf16 a[148:151], v[156:159], v[52:55], a[148:151]\n"
"ds_read_b64_tr_b16 v[148:149], v208 offset:49152\n"
"ds_read_b64_tr_b16 v[150:151], v208 offset:50688\n"
"v_mfma_f32_16x16x32_bf16 a[148:151], v[160:163], v[68:71], a[148:151]\n"
"ds_read_b64_tr_b16 v[152:153], v208 offset:61440\n"
"ds_read_b64_tr_b16 v[154:155], v208 offset:62976\n"
"v_mfma_f32_16x16x32_bf16 a[152:155], v[156:159], v[56:59], a[152:155]\n"
"ds_read_b128 v[16:19], v212 offset:0\n"
"ds_read_b128 v[32:35], v213 offset:0\n"
"v_mfma_f32_16x16x32_bf16 a[152:155], v[160:163], v[72:75], a[152:155]\n"
"ds_read_b128 v[20:23], v212 offset:2048\n"
"ds_read_b128 v[36:39], v213 offset:2048\n"
"v_mfma_f32_16x16x32_bf16 a[156:159], v[156:159], v[60:63], a[156:159]\n"
"ds_read_b128 v[24:27], v212 offset:4096\n"
"ds_read_b128 v[40:43], v213 offset:4096\n"
"v_mfma_f32_16x16x32_bf16 a[156:159], v[160:163], v[76:79], a[156:159]\n"
"ds_read_b128 v[28:31], v212 offset:6144\n"
"ds_read_b128 v[44:47], v213 offset:6144\n"
"v_mfma_f32_16x16x32_bf16 a[160:163], v[164:167], v[48:51], a[160:163]\n"
"s_mov_b32 m0, s55\n"
"v_mfma_f32_16x16x32_bf16 a[160:163], v[168:171], v[64:67], a[160:163]\n"
"buffer_load_dwordx4 v224, s[32:35], s95 offen offset:0 lds\n"
"v_mfma_f32_16x16x32_bf16 a[164:167], v[164:167], v[52:55], a[164:167]\n"
"buffer_load_dwordx4 v225, s[32:35], s95 offen offset:1024 lds\n"
"v_mfma_f32_16x16x32_bf16 a[164:167], v[168:171], v[68:71], a[164:167]\n"
"s_add_u32 s59, s59, 1\n"
"s_cmp_eq_u32 s59, 10\n"
"s_cselect_b32 s59, 0, s59\n"
"v_mfma_f32_16x16x32_bf16 a[168:171], v[164:167], v[56:59], a[168:171]\n"
"s_lshl_b32 s50, s59, 7\n"
"ds_read_b64_tr_b16 v[156:157], v209 offset:49152\n"
"ds_read_b64_tr_b16 v[158:159], v209 offset:50688\n"
"v_mfma_f32_16x16x32_bf16 a[168:171], v[168:171], v[72:75], a[168:171]\n"
"s_add_u32 s50, s50, s46\n"
"ds_read_b64_tr_b16 v[160:161], v209 offset:61440\n"
"ds_read_b64_tr_b16 v[162:163], v209 offset:62976\n"
"v_mfma_f32_16x16x32_bf16 a[172:175], v[164:167], v[60:63], a[172:175]\n"
"s_mul_i32 s51, s59, 786432\n"
"buffer_load_dwordx4 v226, s[32:35], s95 offen offset:2048 lds\n"
"v_mfma_f32_16x16x32_bf16 a[172:175], v[168:171], v[76:79], a[172:175]\n"
"s_add_u32 s51, s51, s47\n"
"v_mfma_f32_16x16x32_bf16 a[176:179], v[172:175], v[48:51], a[176:179]\n"
"v_mfma_f32_16x16x32_bf16 a[176:179], v[176:179], v[64:67], a[176:179]\n"
"v_mfma_f32_16x16x32_bf16 a[180:183], v[172:175], v[52:55], a[180:183]\n"
"v_mfma_f32_16x16x32_bf16 a[180:183], v[176:179], v[68:71], a[180:183]\n"
"buffer_load_dwordx4 v227, s[32:35], s95 offen offset:3072 lds\n"
"v_mfma_f32_16x16x32_bf16 a[184:187], v[172:175], v[56:59], a[184:187]\n"
"v_mfma_f32_16x16x32_bf16 a[184:187], v[176:179], v[72:75], a[184:187]\n"
"v_mfma_f32_16x16x32_bf16 a[188:191], v[172:175], v[60:63], a[188:191]\n"
"v_mfma_f32_16x16x32_bf16 a[188:191], v[176:179], v[76:79], a[188:191]\n"
"s_waitcnt lgkmcnt(4)\n"
"v_mfma_f32_16x16x32_bf16 a[0:3], v[148:151], v[16:19], a[0:3]\n"
"v_mfma_f32_16x16x32_bf16 a[0:3], v[152:155], v[32:35], a[0:3]\n"
"s_add_u32 m0, s55, 4096\n"
"v_mfma_f32_16x16x32_bf16 a[4:7], v[148:151], v[20:23], a[4:7]\n"
"buffer_load_dwordx4 v228, s[32:35], s95 offen offset:0 lds\n"
"v_mfma_f32_16x16x32_bf16 a[4:7], v[152:155], v[36:39], a[4:7]\n"
"v_mfma_f32_16x16x32_bf16 a[8:11], v[148:151], v[24:27], a[8:11]\n"
"ds_read_b64_tr_b16 v[164:165], v210 offset:49152\n"
"ds_read_b64_tr_b16 v[166:167], v210 offset:50688\n"
"v_mfma_f32_16x16x32_bf16 a[8:11], v[152:155], v[40:43], a[8:11]\n"
"ds_read_b64_tr_b16 v[168:169], v210 offset:61440\n"
"ds_read_b64_tr_b16 v[170:171], v210 offset:62976\n"
"v_mfma_f32_16x16x32_bf16 a[12:15], v[148:151], v[28:31], a[12:15]\n"
"v_mfma_f32_16x16x32_bf16 a[12:15], v[152:155], v[44:47], a[12:15]\n"
"s_waitcnt lgkmcnt(4)\n"
"v_mfma_f32_16x16x32_bf16 a[16:19], v[156:159], v[16:19], a[16:19]\n"
"buffer_load_dwordx4 v229, s[32:35], s95 offen offset:1024 lds\n"
"v_mfma_f32_16x16x32_bf16 a[16:19], v[160:163], v[32:35], a[16:19]\n"
"v_mfma_f32_16x16x32_bf16 a[20:23], v[156:159], v[20:23], a[20:23]\n"
"v_mfma_f32_16x16x32_bf16 a[20:23], v[160:163], v[36:39], a[20:23]\n"
"v_mfma_f32_16x16x32_bf16 a[24:27], v[156:159], v[24:27], a[24:27]\n"
"ds_read_b64_tr_b16 v[172:173], v211 offset:49152\n"
"ds_read_b64_tr_b16 v[174:175], v211 offset:50688\n"
"v_mfma_f32_16x16x32_bf16 a[24:27], v[160:163], v[40:43], a[24:27]\n"
"s_mov_b32 m0, s52\n"
"ds_read_b64_tr_b16 v[176:177], v211 offset:61440\n"
"ds_read_b64_tr_b16 v[178:179], v211 offset:62976\n"
"v_mfma_f32_16x16x32_bf16 a[28:31], v[156:159], v[28:31], a[28:31]\n"
"buffer_load_dwordx4 v216, s[28:31], s50 offen offset:0 lds\n"
"v_mfma_f32_16x16x32_bf16 a[28:31], v[160:163], v[44:47], a[28:31]\n"
"s_waitcnt lgkmcnt(4)\n"
"v_mfma_f32_16x16x32_bf16 a[32:35], v[164:167], v[16:19], a[32:35]\n"
"v_mfma_f32_16x16x32_bf16 a[32:35], v[168:171], v[32:35], a[32:35]\n"
"v_mfma_f32_16x16x32_bf16 a[36:39], v[164:167], v[20:23], a[36:39]\n"
"v_mfma_f32_16x16x32_bf16 a[36:39], v[168:171], v[36:39], a[36:39]\n"
"v_mfma_f32_16x16x32_bf16 a[40:43], v[164:167], v[24:27], a[40:43]\n"
"ds_read_b64_tr_b16 v[148:149], v208 offset:49280\n"
"ds_read_b64_tr_b16 v[150:151], v208 offset:50816\n"
"v_mfma_f32_16x16x32_bf16 a[40:43], v[168:171], v[40:43], a[40:43]\n"
"ds_read_b64_tr_b16 v[152:153], v208 offset:61568\n"
"ds_read_b64_tr_b16 v[154:155], v208 offset:63104\n"
"v_mfma_f32_16x16x32_bf16 a[44:47], v[164:167], v[28:31], a[44:47]\n"
"buffer_load_dwordx4 v217, s[28:31], s50 offen offset:1024 lds\n"
"v_mfma_f32_16x16x32_bf16 a[44:47], v[168:171], v[44:47], a[44:47]\n"
"s_waitcnt lgkmcnt(4)\n"
"v_mfma_f32_16x16x32_bf16 a[48:51], v[172:175], v[16:19], a[48:51]\n"
"v_mfma_f32_16x16x32_bf16 a[48:51], v[176:179], v[32:35], a[48:51]\n"
"v_mfma_f32_16x16x32_bf16 a[52:55], v[172:175], v[20:23], a[52:55]\n"
"v_mfma_f32_16x16x32_bf16 a[52:55], v[176:179], v[36:39], a[52:55]\n"
"v_mfma_f32_16x16x32_bf16 a[56:59], v[172:175], v[24:27], a[56:59]\n"
"ds_read_b64_tr_b16 v[156:157], v209 offset:49280\n"
"ds_read_b64_tr_b16 v[158:159], v209 offset:50816\n"
"v_mfma_f32_16x16x32_bf16 a[56:59], v[176:179], v[40:43], a[56:59]\n"
"ds_read_b64_tr_b16 v[160:161], v209 offset:61568\n"
"ds_read_b64_tr_b16 v[162:163], v209 offset:63104\n"
"v_mfma_f32_16x16x32_bf16 a[60:63], v[172:175], v[28:31], a[60:63]\n"
"buffer_load_dwordx4 v218, s[28:31], s50 offen offset:2048 lds\n"
"v_mfma_f32_16x16x32_bf16 a[60:63], v[176:179], v[44:47], a[60:63]\n"
"s_waitcnt lgkmcnt(4)\n"
"v_mfma_f32_16x16x32_bf16 a[64:67], v[148:151], v[16:19], a[64:67]\n"
"v_mfma_f32_16x16x32_bf16 a[64:67], v[152:155], v[32:35], a[64:67]\n"
"v_mfma_f32_16x16x32_bf16 a[68:71], v[148:151], v[20:23], a[68:71]\n"
"v_mfma_f32_16x16x32_bf16 a[68:71], v[152:155], v[36:39], a[68:71]\n"
"buffer_load_dwordx4 v219, s[28:31], s50 offen offset:3072 lds\n"
"v_mfma_f32_16x16x32_bf16 a[72:75], v[148:151], v[24:27], a[72:75]\n"
"ds_read_b64_tr_b16 v[164:165], v210 offset:49280\n"
"ds_read_b64_tr_b16 v[166:167], v210 offset:50816\n"
"v_mfma_f32_16x16x32_bf16 a[72:75], v[152:155], v[40:43], a[72:75]\n"
"ds_read_b64_tr_b16 v[168:169], v210 offset:61568\n"
"ds_read_b64_tr_b16 v[170:171], v210 offset:63104\n"
"v_mfma_f32_16x16x32_bf16 a[76:79], v[148:151], v[28:31], a[76:79]\n"
"v_mfma_f32_16x16x32_bf16 a[76:79], v[152:155], v[44:47], a[76:79]\n"
"s_waitcnt lgkmcnt(4)\n"
"v_mfma_f32_16x16x32_bf16 a[80:83], v[156:159], v[16:19], a[80:83]\n"
"v_mfma_f32_16x16x32_bf16 a[80:83], v[160:163], v[32:35], a[80:83]\n"
"s_add_u32 m0, s52, 4096\n"
"v_mfma_f32_16x16x32_bf16 a[84:87], v[156:159], v[20:23], a[84:87]\n"
"buffer_load_dwordx4 v220, s[28:31], s50 offen offset:0 lds\n"
"v_mfma_f32_16x16x32_bf16 a[84:87], v[160:163], v[36:39], a[84:87]\n"
"v_mfma_f32_16x16x32_bf16 a[88:91], v[156:159], v[24:27], a[88:91]\n"
"ds_read_b64_tr_b16 v[172:173], v211 offset:49280\n"
"ds_read_b64_tr_b16 v[174:175], v211 offset:50816\n"
"ds_read_b64_tr_b16 v[148:149], v208 offset:49408\n"
"ds_read_b64_tr_b16 v[150:151], v208 offset:50944\n"
"v_mfma_f32_16x16x32_bf16 a[88:91], v[160:163], v[40:43], a[88:91]\n"
"ds_read_b64_tr_b16 v[176:177], v211 offset:61568\n"
"ds_read_b64_tr_b16 v[178:179], v211 offset:63104\n"
"ds_read_b64_tr_b16 v[152:153], v208 offset:61696\n"
"ds_read_b64_tr_b16 v[154:155], v208 offset:63232\n"
"v_mfma_f32_16x16x32_bf16 a[92:95], v[156:159], v[28:31], a[92:95]\n"
"v_mfma_f32_16x16x32_bf16 a[92:95], v[160:163], v[44:47], a[92:95]\n"
"s_waitcnt lgkmcnt(8)\n"
"v_mfma_f32_16x16x32_bf16 a[96:99], v[164:167], v[16:19], a[96:99]\n"
"v_mfma_f32_16x16x32_bf16 a[96:99], v[168:171], v[32:35], a[96:99]\n"
"buffer_load_dwordx4 v221, s[28:31], s50 offen offset:1024 lds\n"
"v_mfma_f32_16x16x32_bf16 a[100:103], v[164:167], v[20:23], a[100:103]\n"
"ds_read_b64_tr_b16 v[156:157], v209 offset:49408\n"
"ds_read_b64_tr_b16 v[158:159], v209 offset:50944\n"
"v_mfma_f32_16x16x32_bf16 a[100:103], v[168:171], v[36:39], a[100:103]\n"
"ds_read_b64_tr_b16 v[160:161], v209 offset:61696\n"
"ds_read_b64_tr_b16 v[162:163], v209 offset:63232\n"
"v_mfma_f32_16x16x32_bf16 a[104:107], v[164:167], v[24:27], a[104:107]\n"
"v_mfma_f32_16x16x32_bf16 a[104:107], v[168:171], v[40:43], a[104:107]\n"
"v_mfma_f32_16x16x32_bf16 a[108:111], v[164:167], v[28:31], a[108:111]\n"
"v_mfma_f32_16x16x32_bf16 a[108:111], v[168:171], v[44:47], a[108:111]\n"
"s_waitcnt lgkmcnt(6)\n"
"v_mfma_f32_16x16x32_bf16 a[112:115], v[172:175], v[16:19], a[112:115]\n"
"buffer_load_dwordx4 v222, s[28:31], s50 offen offset:2048 lds\n"
"v_mfma_f32_16x16x32_bf16 a[112:115], v[176:179], v[32:35], a[112:115]\n"
"ds_read_b64_tr_b16 v[164:165], v210 offset:49408\n"
"ds_read_b64_tr_b16 v[166:167], v210 offset:50944\n"
"v_mfma_f32_16x16x32_bf16 a[116:119], v[172:175], v[20:23], a[116:119]\n"
"ds_read_b64_tr_b16 v[168:169], v210 offset:61696\n"
"ds_read_b64_tr_b16 v[170:171], v210 offset:63232\n"
"v_mfma_f32_16x16x32_bf16 a[116:119], v[176:179], v[36:39], a[116:119]\n"
"v_mfma_f32_16x16x32_bf16 a[120:123], v[172:175], v[24:27], a[120:123]\n"
"v_mfma_f32_16x16x32_bf16 a[120:123], v[176:179], v[40:43], a[120:123]\n"
"v_mfma_f32_16x16x32_bf16 a[124:127], v[172:175], v[28:31], a[124:127]\n"
"v_mfma_f32_16x16x32_bf16 a[124:127], v[176:179], v[44:47], a[124:127]\n"
"s_waitcnt lgkmcnt(8)\n"
"buffer_load_dwordx4 v223, s[28:31], s50 offen offset:3072 lds\n"
"v_mfma_f32_16x16x32_bf16 a[128:131], v[148:151], v[16:19], a[128:131]\n"
"ds_read_b64_tr_b16 v[172:173], v211 offset:49408\n"
"ds_read_b64_tr_b16 v[174:175], v211 offset:50944\n"
"v_mfma_f32_16x16x32_bf16 a[128:131], v[152:155], v[32:35], a[128:131]\n"
"s_add_u32 m0, s89, 4096\n"
"ds_read_b64_tr_b16 v[176:177], v211 offset:61696\n"
"ds_read_b64_tr_b16 v[178:179], v211 offset:63232\n"
"v_mfma_f32_16x16x32_bf16 a[132:135], v[148:151], v[20:23], a[132:135]\n"
"buffer_load_dwordx4 v206, s[20:23], s86 offen offset:0 nt lds\n"
"v_mfma_f32_16x16x32_bf16 a[132:135], v[152:155], v[36:39], a[132:135]\n"
"v_mfma_f32_16x16x32_bf16 a[136:139], v[148:151], v[24:27], a[136:139]\n"
"v_mfma_f32_16x16x32_bf16 a[136:139], v[152:155], v[40:43], a[136:139]\n"
"buffer_load_dwordx4 v207, s[20:23], s86 offen offset:1024 nt lds\n"
"v_mfma_f32_16x16x32_bf16 a[140:143], v[148:151], v[28:31], a[140:143]\n"
"s_mov_b32 s95, s51\n"
"v_mfma_f32_16x16x32_bf16 a[140:143], v[152:155], v[44:47], a[140:143]\n"
"s_waitcnt lgkmcnt(8)\n"
"v_mfma_f32_16x16x32_bf16 a[144:147], v[156:159], v[16:19], a[144:147]\n"
"v_mfma_f32_16x16x32_bf16 a[144:147], v[160:163], v[32:35], a[144:147]\n"
"s_waitcnt vmcnt(10) lgkmcnt(0)\n"
"s_barrier\n"
"v_mfma_f32_16x16x32_bf16 a[148:151], v[156:159], v[20:23], a[148:151]\n"
"ds_read_b64_tr_b16 v[148:149], v208 offset:24576\n"
"ds_read_b64_tr_b16 v[150:151], v208 offset:26112\n"
"v_mfma_f32_16x16x32_bf16 a[148:151], v[160:163], v[36:39], a[148:151]\n"
"ds_read_b64_tr_b16 v[152:153], v208 offset:36864\n"
"ds_read_b64_tr_b16 v[154:155], v208 offset:38400\n"
"v_mfma_f32_16x16x32_bf16 a[152:155], v[156:159], v[24:27], a[152:155]\n"
"ds_read_b128 v[48:51], v214 offset:0\n"
"ds_read_b128 v[64:67], v215 offset:0\n"
"v_mfma_f32_16x16x32_bf16 a[152:155], v[160:163], v[40:43], a[152:155]\n"
"ds_read_b128 v[52:55], v214 offset:2048\n"
"ds_read_b128 v[68:71], v215 offset:2048\n"
"v_mfma_f32_16x16x32_bf16 a[156:159], v[156:159], v[28:31], a[156:159]\n"
"ds_read_b128 v[56:59], v214 offset:4096\n"
"ds_read_b128 v[72:75], v215 offset:4096\n"
"v_mfma_f32_16x16x32_bf16 a[156:159], v[160:163], v[44:47], a[156:159]\n"
"ds_read_b128 v[60:63], v214 offset:6144\n"
"ds_read_b128 v[76:79], v215 offset:6144\n"
"v_mfma_f32_16x16x32_bf16 a[160:163], v[164:167], v[16:19], a[160:163]\n"
"s_mov_b32 m0, s54\n"
"v_mfma_f32_16x16x32_bf16 a[160:163], v[168:171], v[32:35], a[160:163]\n"
"buffer_load_dwordx4 v224, s[32:35], s95 offen offset:0 lds\n"
"v_mfma_f32_16x16x32_bf16 a[164:167], v[164:167], v[20:23], a[164:167]\n"
"buffer_load_dwordx4 v225, s[32:35], s95 offen offset:1024 lds\n"
"v_mfma_f32_16x16x32_bf16 a[164:167], v[168:171], v[36:39], a[164:167]\n"
"s_add_u32 s59, s59, 1\n"
"s_cmp_eq_u32 s59, 10\n"
"s_cselect_b32 s59, 0, s59\n"
"v_mfma_f32_16x16x32_bf16 a[168:171], v[164:167], v[24:27], a[168:171]\n"
"s_lshl_b32 s50, s59, 7\n"
"ds_read_b64_tr_b16 v[156:157], v209 offset:24576\n"
"ds_read_b64_tr_b16 v[158:159], v209 offset:26112\n"
"v_mfma_f32_16x16x32_bf16 a[168:171], v[168:171], v[40:43], a[168:171]\n"
"s_add_u32 s50, s50, s46\n"
"ds_read_b64_tr_b16 v[160:161], v209 offset:36864\n"
"ds_read_b64_tr_b16 v[162:163], v209 offset:38400\n"
"v_mfma_f32_16x16x32_bf16 a[172:175], v[164:167], v[28:31], a[172:175]\n"
"s_mul_i32 s51, s59, 786432\n"
"buffer_load_dwordx4 v226, s[32:35], s95 offen offset:2048 lds\n"
"v_mfma_f32_16x16x32_bf16 a[172:175], v[168:171], v[44:47], a[172:175]\n"
"s_add_u32 s51, s51, s47\n"
"v_mfma_f32_16x16x32_bf16 a[176:179], v[172:175], v[16:19], a[176:179]\n"
"v_mfma_f32_16x16x32_bf16 a[176:179], v[176:179], v[32:35], a[176:179]\n"
"v_mfma_f32_16x16x32_bf16 a[180:183], v[172:175], v[20:23], a[180:183]\n"
"v_mfma_f32_16x16x32_bf16 a[180:183], v[176:179], v[36:39], a[180:183]\n"
"buffer_load_dwordx4 v227, s[32:35], s95 offen offset:3072 lds\n"
"v_mfma_f32_16x16x32_bf16 a[184:187], v[172:175], v[24:27], a[184:187]\n"
"v_mfma_f32_16x16x32_bf16 a[184:187], v[176:179], v[40:43], a[184:187]\n"
"v_mfma_f32_16x16x32_bf16 a[188:191], v[172:175], v[28:31], a[188:191]\n"
"v_mfma_f32_16x16x32_bf16 a[188:191], v[176:179], v[44:47], a[188:191]\n"
"s_waitcnt lgkmcnt(4)\n"
"v_mfma_f32_16x16x32_bf16 a[0:3], v[148:151], v[48:51], a[0:3]\n"
"v_mfma_f32_16x16x32_bf16 a[0:3], v[152:155], v[64:67], a[0:3]\n"
"s_add_u32 m0, s54, 4096\n"
"v_mfma_f32_16x16x32_bf16 a[4:7], v[148:151], v[52:55], a[4:7]\n"
"buffer_load_dwordx4 v228, s[32:35], s95 offen offset:0 lds\n"
"v_mfma_f32_16x16x32_bf16 a[4:7], v[152:155], v[68:71], a[4:7]\n"
"v_mfma_f32_16x16x32_bf16 a[8:11], v[148:151], v[56:59], a[8:11]\n"
"ds_read_b64_tr_b16 v[164:165], v210 offset:24576\n"
"ds_read_b64_tr_b16 v[166:167], v210 offset:26112\n"
"v_mfma_f32_16x16x32_bf16 a[8:11], v[152:155], v[72:75], a[8:11]\n"
"ds_read_b64_tr_b16 v[168:169], v210 offset:36864\n"
"ds_read_b64_tr_b16 v[170:171], v210 offset:38400\n"
"v_mfma_f32_16x16x32_bf16 a[12:15], v[148:151], v[60:63], a[12:15]\n"
"v_mfma_f32_16x16x32_bf16 a[12:15], v[152:155], v[76:79], a[12:15]\n"
"s_waitcnt lgkmcnt(4)\n"
"v_mfma_f32_16x16x32_bf16 a[16:19], v[156:159], v[48:51], a[16:19]\n"
"buffer_load_dwordx4 v229, s[32:35], s95 offen offset:1024 lds\n"
"v_mfma_f32_16x16x32_bf16 a[16:19], v[160:163], v[64:67], a[16:19]\n"
"v_mfma_f32_16x16x32_bf16 a[20:23], v[156:159], v[52:55], a[20:23]\n"
"v_mfma_f32_16x16x32_bf16 a[20:23], v[160:163], v[68:71], a[20:23]\n"
"v_mfma_f32_16x16x32_bf16 a[24:27], v[156:159], v[56:59], a[24:27]\n"
"ds_read_b64_tr_b16 v[172:173], v211 offset:24576\n"
"ds_read_b64_tr_b16 v[174:175], v211 offset:26112\n"
"v_mfma_f32_16x16x32_bf16 a[24:27], v[160:163], v[72:75], a[24:27]\n"
"s_mov_b32 m0, s53\n"
"ds_read_b64_tr_b16 v[176:177], v211 offset:36864\n"
"ds_read_b64_tr_b16 v[178:179], v211 offset:38400\n"
"v_mfma_f32_16x16x32_bf16 a[28:31], v[156:159], v[60:63], a[28:31]\n"
"buffer_load_dwordx4 v216, s[28:31], s50 offen offset:0 lds\n"
"v_mfma_f32_16x16x32_bf16 a[28:31], v[160:163], v[76:79], a[28:31]\n"
"s_waitcnt lgkmcnt(4)\n"
"v_mfma_f32_16x16x32_bf16 a[32:35], v[164:167], v[48:51], a[32:35]\n"
"v_mfma_f32_16x16x32_bf16 a[32:35], v[168:171], v[64:67], a[32:35]\n"
"v_mfma_f32_16x16x32_bf16 a[36:39], v[164:167], v[52:55], a[36:39]\n"
"v_mfma_f32_16x16x32_bf16 a[36:39], v[168:171], v[68:71], a[36:39]\n"
"v_mfma_f32_16x16x32_bf16 a[40:43], v[164:167], v[56:59], a[40:43]\n"
"ds_read_b64_tr_b16 v[148:149], v208 offset:24704\n"
"ds_read_b64_tr_b16 v[150:151], v208 offset:26240\n"
"v_mfma_f32_16x16x32_bf16 a[40:43], v[168:171], v[72:75], a[40:43]\n"
"ds_read_b64_tr_b16 v[152:153], v208 offset:36992\n"
"ds_read_b64_tr_b16 v[154:155], v208 offset:38528\n"
"v_mfma_f32_16x16x32_bf16 a[44:47], v[164:167], v[60:63], a[44:47]\n"
"buffer_load_dwordx4 v217, s[28:31], s50 offen offset:1024 lds\n"
"v_mfma_f32_16x16x32_bf16 a[44:47], v[168:171], v[76:79], a[44:47]\n"
"s_waitcnt lgkmcnt(4)\n"
"v_mfma_f32_16x16x32_bf16 a[48:51], v[172:175], v[48:51], a[48:51]\n"
"v_mfma_f32_16x16x32_bf16 a[48:51], v[176:179], v[64:67], a[48:51]\n"
"v_mfma_f32_16x16x32_bf16 a[52:55], v[172:175], v[52:55], a[52:55]\n"
"v_mfma_f32_16x16x32_bf16 a[52:55], v[176:179], v[68:71], a[52:55]\n"
"v_mfma_f32_16x16x32_bf16 a[56:59], v[172:175], v[56:59], a[56:59]\n"
"ds_read_b64_tr_b16 v[156:157], v209 offset:24704\n"
"ds_read_b64_tr_b16 v[158:159], v209 offset:26240\n"
"v_mfma_f32_16x16x32_bf16 a[56:59], v[176:179], v[72:75], a[56:59]\n"
"ds_read_b64_tr_b16 v[160:161], v209 offset:36992\n"
"ds_read_b64_tr_b16 v[162:163], v209 offset:38528\n"
"v_mfma_f32_16x16x32_bf16 a[60:63], v[172:175], v[60:63], a[60:63]\n"
"buffer_load_dwordx4 v218, s[28:31], s50 offen offset:2048 lds\n"
"v_mfma_f32_16x16x32_bf16 a[60:63], v[176:179], v[76:79], a[60:63]\n"
"s_waitcnt lgkmcnt(4)\n"
"v_mfma_f32_16x16x32_bf16 a[64:67], v[148:151], v[48:51], a[64:67]\n"
"v_mfma_f32_16x16x32_bf16 a[64:67], v[152:155], v[64:67], a[64:67]\n"
"v_mfma_f32_16x16x32_bf16 a[68:71], v[148:151], v[52:55], a[68:71]\n"
"v_mfma_f32_16x16x32_bf16 a[68:71], v[152:155], v[68:71], a[68:71]\n"
"buffer_load_dwordx4 v219, s[28:31], s50 offen offset:3072 lds\n"
"v_mfma_f32_16x16x32_bf16 a[72:75], v[148:151], v[56:59], a[72:75]\n"
"ds_read_b64_tr_b16 v[164:165], v210 offset:24704\n"
"ds_read_b64_tr_b16 v[166:167], v210 offset:26240\n"
"v_mfma_f32_16x16x32_bf16 a[72:75], v[152:155], v[72:75], a[72:75]\n"
"ds_read_b64_tr_b16 v[168:169], v210 offset:36992\n"
"ds_read_b64_tr_b16 v[170:171], v210 offset:38528\n"
"v_mfma_f32_16x16x32_bf16 a[76:79], v[148:151], v[60:63], a[76:79]\n"
"v_mfma_f32_16x16x32_bf16 a[76:79], v[152:155], v[76:79], a[76:79]\n"
"s_waitcnt lgkmcnt(4)\n"
"v_mfma_f32_16x16x32_bf16 a[80:83], v[156:159], v[48:51], a[80:83]\n"
"v_mfma_f32_16x16x32_bf16 a[80:83], v[160:163], v[64:67], a[80:83]\n"
"s_add_u32 m0, s53, 4096\n"
"v_mfma_f32_16x16x32_bf16 a[84:87], v[156:159], v[52:55], a[84:87]\n"
"buffer_load_dwordx4 v220, s[28:31], s50 offen offset:0 lds\n"
"v_mfma_f32_16x16x32_bf16 a[84:87], v[160:163], v[68:71], a[84:87]\n"
"v_mfma_f32_16x16x32_bf16 a[88:91], v[156:159], v[56:59], a[88:91]\n"
"ds_read_b64_tr_b16 v[172:173], v211 offset:24704\n"
"ds_read_b64_tr_b16 v[174:175], v211 offset:26240\n"
"ds_read_b64_tr_b16 v[148:149], v208 offset:24832\n"
"ds_read_b64_tr_b16 v[150:151], v208 offset:26368\n"
"v_mfma_f32_16x16x32_bf16 a[88:91], v[160:163], v[72:75], a[88:91]\n"
"ds_read_b64_tr_b16 v[176:177], v211 offset:36992\n"
"ds_read_b64_tr_b16 v[178:179], v211 offset:38528\n"
"ds_read_b64_tr_b16 v[152:153], v208 offset:37120\n"
"ds_read_b64_tr_b16 v[154:155], v208 offset:38656\n"
"v_mfma_f32_16x16x32_bf16 a[92:95], v[156:159], v[60:63], a[92:95]\n"
"v_mfma_f32_16x16x32_bf16 a[92:95], v[160:163], v[76:79], a[92:95]\n"
"s_waitcnt lgkmcnt(8)\n"
"v_mfma_f32_16x16x32_bf16 a[96:99], v[164:167], v[48:51], a[96:99]\n"
"v_mfma_f32_16x16x32_bf16 a[96:99], v[168:171], v[64:67], a[96:99]\n"
"buffer_load_dwordx4 v221, s[28:31], s50 offen offset:1024 lds\n"
"v_mfma_f32_16x16x32_bf16 a[100:103], v[164:167], v[52:55], a[100:103]\n"
"ds_read_b64_tr_b16 v[156:157], v209 offset:24832\n"
"ds_read_b64_tr_b16 v[158:159], v209 offset:26368\n"
"v_mfma_f32_16x16x32_bf16 a[100:103], v[168:171], v[68:71], a[100:103]\n"
"ds_read_b64_tr_b16 v[160:161], v209 offset:37120\n"
"ds_read_b64_tr_b16 v[162:163], v209 offset:38656\n"
"v_mfma_f32_16x16x32_bf16 a[104:107], v[164:167], v[56:59], a[104:107]\n"
"v_mfma_f32_16x16x32_bf16 a[104:107], v[168:171], v[72:75], a[104:107]\n"
"v_mfma_f32_16x16x32_bf16 a[108:111], v[164:167], v[60:63], a[108:111]\n"
"v_mfma_f32_16x16x32_bf16 a[108:111], v[168:171], v[76:79], a[108:111]\n"
"s_waitcnt lgkmcnt(6)\n"
"v_mfma_f32_16x16x32_bf16 a[112:115], v[172:175], v[48:51], a[112:115]\n"
"buffer_load_dwordx4 v222, s[28:31], s50 offen offset:2048 lds\n"
"v_mfma_f32_16x16x32_bf16 a[112:115], v[176:179], v[64:67], a[112:115]\n"
"ds_read_b64_tr_b16 v[164:165], v210 offset:24832\n"
"ds_read_b64_tr_b16 v[166:167], v210 offset:26368\n"
"v_mfma_f32_16x16x32_bf16 a[116:119], v[172:175], v[52:55], a[116:119]\n"
"ds_read_b64_tr_b16 v[168:169], v210 offset:37120\n"
"ds_read_b64_tr_b16 v[170:171], v210 offset:38656\n"
"v_mfma_f32_16x16x32_bf16 a[116:119], v[176:179], v[68:71], a[116:119]\n"
"v_mfma_f32_16x16x32_bf16 a[120:123], v[172:175], v[56:59], a[120:123]\n"
"v_mfma_f32_16x16x32_bf16 a[120:123], v[176:179], v[72:75], a[120:123]\n"
"v_mfma_f32_16x16x32_bf16 a[124:127], v[172:175], v[60:63], a[124:127]\n"
"v_mfma_f32_16x16x32_bf16 a[124:127], v[176:179], v[76:79], a[124:127]\n"
"s_waitcnt lgkmcnt(8)\n"
"buffer_load_dwordx4 v223, s[28:31], s50 offen offset:3072 lds\n"
"v_mfma_f32_16x16x32_bf16 a[128:131], v[148:151], v[48:51], a[128:131]\n"
"ds_read_b64_tr_b16 v[172:173], v211 offset:24832\n"
"ds_read_b64_tr_b16 v[174:175], v211 offset:26368\n"
"v_mfma_f32_16x16x32_bf16 a[128:131], v[152:155], v[64:67], a[128:131]\n"
"s_mov_b32 m0, s87\n"
"ds_read_b64_tr_b16 v[176:177], v211 offset:37120\n"
"ds_read_b64_tr_b16 v[178:179], v211 offset:38656\n"
"v_mfma_f32_16x16x32_bf16 a[132:135], v[148:151], v[52:55], a[132:135]\n"
"buffer_load_dwordx4 v202, s[24:27], s86 offen offset:0 nt lds\n"
"v_mfma_f32_16x16x32_bf16 a[132:135], v[152:155], v[68:71], a[132:135]\n"
"v_mfma_f32_16x16x32_bf16 a[136:139], v[148:151], v[56:59], a[136:139]\n"
"v_mfma_f32_16x16x32_bf16 a[136:139], v[152:155], v[72:75], a[136:139]\n"
"buffer_load_dwordx4 v203, s[24:27], s86 offen offset:1024 nt lds\n"
"v_mfma_f32_16x16x32_bf16 a[140:143], v[148:151], v[60:63], a[140:143]\n"
"s_mov_b32 s95, s51\n"
"v_mfma_f32_16x16x32_bf16 a[140:143], v[152:155], v[76:79], a[140:143]\n"
"s_waitcnt lgkmcnt(8)\n"
"v_mfma_f32_16x16x32_bf16 a[144:147], v[156:159], v[48:51], a[144:147]\n"
"v_mfma_f32_16x16x32_bf16 a[144:147], v[160:163], v[64:67], a[144:147]\n"
"s_waitcnt vmcnt(10) lgkmcnt(0)\n"
"s_barrier\n"
"v_mfma_f32_16x16x32_bf16 a[148:151], v[156:159], v[52:55], a[148:151]\n"
"ds_read_b64_tr_b16 v[148:149], v208 offset:49152\n"
"ds_read_b64_tr_b16 v[150:151], v208 offset:50688\n"
"v_mfma_f32_16x16x32_bf16 a[148:151], v[160:163], v[68:71], a[148:151]\n"
"ds_read_b64_tr_b16 v[152:153], v208 offset:61440\n"
"ds_read_b64_tr_b16 v[154:155], v208 offset:62976\n"
"v_mfma_f32_16x16x32_bf16 a[152:155], v[156:159], v[56:59], a[152:155]\n"
"ds_read_b128 v[16:19], v212 offset:0\n"
"ds_read_b128 v[32:35], v213 offset:0\n"
"v_mfma_f32_16x16x32_bf16 a[152:155], v[160:163], v[72:75], a[152:155]\n"
"ds_read_b128 v[20:23], v212 offset:2048\n"
"ds_read_b128 v[36:39], v213 offset:2048\n"
"v_mfma_f32_16x16x32_bf16 a[156:159], v[156:159], v[60:63], a[156:159]\n"
"ds_read_b128 v[24:27], v212 offset:4096\n"
"ds_read_b128 v[40:43], v213 offset:4096\n"
"v_mfma_f32_16x16x32_bf16 a[156:159], v[160:163], v[76:79], a[156:159]\n"
"ds_read_b128 v[28:31], v212 offset:6144\n"
"ds_read_b128 v[44:47], v213 offset:6144\n"
"v_mfma_f32_16x16x32_bf16 a[160:163], v[164:167], v[48:51], a[160:163]\n"
"s_mov_b32 m0, s55\n"
"v_mfma_f32_16x16x32_bf16 a[160:163], v[168:171], v[64:67], a[160:163]\n"
"buffer_load_dwordx4 v224, s[32:35], s95 offen offset:0 lds\n"
"v_mfma_f32_16x16x32_bf16 a[164:167], v[164:167], v[52:55], a[164:167]\n"
"buffer_load_dwordx4 v225, s[32:35], s95 offen offset:1024 lds\n"
"v_mfma_f32_16x16x32_bf16 a[164:167], v[168:171], v[68:71], a[164:167]\n"
"s_add_u32 s59, s59, 1\n"
"s_cmp_eq_u32 s59, 10\n"
"s_cselect_b32 s59, 0, s59\n"
"v_mfma_f32_16x16x32_bf16 a[168:171], v[164:167], v[56:59], a[168:171]\n"
"s_lshl_b32 s50, s59, 7\n"
"ds_read_b64_tr_b16 v[156:157], v209 offset:49152\n"
"ds_read_b64_tr_b16 v[158:159], v209 offset:50688\n"
"v_mfma_f32_16x16x32_bf16 a[168:171], v[168:171], v[72:75], a[168:171]\n"
"s_add_u32 s50, s50, s46\n"
"ds_read_b64_tr_b16 v[160:161], v209 offset:61440\n"
"ds_read_b64_tr_b16 v[162:163], v209 offset:62976\n"
"v_mfma_f32_16x16x32_bf16 a[172:175], v[164:167], v[60:63], a[172:175]\n"
"s_mul_i32 s51, s59, 786432\n"
"buffer_load_dwordx4 v226, s[32:35], s95 offen offset:2048 lds\n"
"v_mfma_f32_16x16x32_bf16 a[172:175], v[168:171], v[76:79], a[172:175]\n"
"s_add_u32 s51, s51, s47\n"
"v_mfma_f32_16x16x32_bf16 a[176:179], v[172:175], v[48:51], a[176:179]\n"
"v_mfma_f32_16x16x32_bf16 a[176:179], v[176:179], v[64:67], a[176:179]\n"
"v_mfma_f32_16x16x32_bf16 a[180:183], v[172:175], v[52:55], a[180:183]\n"
"v_mfma_f32_16x16x32_bf16 a[180:183], v[176:179], v[68:71], a[180:183]\n"
"buffer_load_dwordx4 v227, s[32:35], s95 offen offset:3072 lds\n"
"v_mfma_f32_16x16x32_bf16 a[184:187], v[172:175], v[56:59], a[184:187]\n"
"v_mfma_f32_16x16x32_bf16 a[184:187], v[176:179], v[72:75], a[184:187]\n"
"v_mfma_f32_16x16x32_bf16 a[188:191], v[172:175], v[60:63], a[188:191]\n"
"v_mfma_f32_16x16x32_bf16 a[188:191], v[176:179], v[76:79], a[188:191]\n"
"s_waitcnt lgkmcnt(4)\n"
"v_mfma_f32_16x16x32_bf16 a[0:3], v[148:151], v[16:19], a[0:3]\n"
"v_mfma_f32_16x16x32_bf16 a[0:3], v[152:155], v[32:35], a[0:3]\n"
"s_add_u32 m0, s55, 4096\n"
"v_mfma_f32_16x16x32_bf16 a[4:7], v[148:151], v[20:23], a[4:7]\n"
"buffer_load_dwordx4 v228, s[32:35], s95 offen offset:0 lds\n"
"v_mfma_f32_16x16x32_bf16 a[4:7], v[152:155], v[36:39], a[4:7]\n"
"v_mfma_f32_16x16x32_bf16 a[8:11], v[148:151], v[24:27], a[8:11]\n"
"ds_read_b64_tr_b16 v[164:165], v210 offset:49152\n"
"ds_read_b64_tr_b16 v[166:167], v210 offset:50688\n"
"v_mfma_f32_16x16x32_bf16 a[8:11], v[152:155], v[40:43], a[8:11]\n"
"ds_read_b64_tr_b16 v[168:169], v210 offset:61440\n"
"ds_read_b64_tr_b16 v[170:171], v210 offset:62976\n"
"v_mfma_f32_16x16x32_bf16 a[12:15], v[148:151], v[28:31], a[12:15]\n"
"v_mfma_f32_16x16x32_bf16 a[12:15], v[152:155], v[44:47], a[12:15]\n"
"s_waitcnt lgkmcnt(4)\n"
"v_mfma_f32_16x16x32_bf16 a[16:19], v[156:159], v[16:19], a[16:19]\n"
"buffer_load_dwordx4 v229, s[32:35], s95 offen offset:1024 lds\n"
"v_mfma_f32_16x16x32_bf16 a[16:19], v[160:163], v[32:35], a[16:19]\n"
"v_mfma_f32_16x16x32_bf16 a[20:23], v[156:159], v[20:23], a[20:23]\n"
"v_mfma_f32_16x16x32_bf16 a[20:23], v[160:163], v[36:39], a[20:23]\n"
"v_mfma_f32_16x16x32_bf16 a[24:27], v[156:159], v[24:27], a[24:27]\n"
"ds_read_b64_tr_b16 v[172:173], v211 offset:49152\n"
"ds_read_b64_tr_b16 v[174:175], v211 offset:50688\n"
"v_mfma_f32_16x16x32_bf16 a[24:27], v[160:163], v[40:43], a[24:27]\n"
"s_mov_b32 m0, s52\n"
"ds_read_b64_tr_b16 v[176:177], v211 offset:61440\n"
"ds_read_b64_tr_b16 v[178:179], v211 offset:62976\n"
"v_mfma_f32_16x16x32_bf16 a[28:31], v[156:159], v[28:31], a[28:31]\n"
"buffer_load_dwordx4 v216, s[28:31], s50 offen offset:0 lds\n"
"v_mfma_f32_16x16x32_bf16 a[28:31], v[160:163], v[44:47], a[28:31]\n"
"s_waitcnt lgkmcnt(4)\n"
"v_mfma_f32_16x16x32_bf16 a[32:35], v[164:167], v[16:19], a[32:35]\n"
"v_mfma_f32_16x16x32_bf16 a[32:35], v[168:171], v[32:35], a[32:35]\n"
"v_mfma_f32_16x16x32_bf16 a[36:39], v[164:167], v[20:23], a[36:39]\n"
"v_mfma_f32_16x16x32_bf16 a[36:39], v[168:171], v[36:39], a[36:39]\n"
"v_mfma_f32_16x16x32_bf16 a[40:43], v[164:167], v[24:27], a[40:43]\n"
"ds_read_b64_tr_b16 v[148:149], v208 offset:49280\n"
"ds_read_b64_tr_b16 v[150:151], v208 offset:50816\n"
"v_mfma_f32_16x16x32_bf16 a[40:43], v[168:171], v[40:43], a[40:43]\n"
"ds_read_b64_tr_b16 v[152:153], v208 offset:61568\n"
"ds_read_b64_tr_b16 v[154:155], v208 offset:63104\n"
"v_mfma_f32_16x16x32_bf16 a[44:47], v[164:167], v[28:31], a[44:47]\n"
"buffer_load_dwordx4 v217, s[28:31], s50 offen offset:1024 lds\n"
"v_mfma_f32_16x16x32_bf16 a[44:47], v[168:171], v[44:47], a[44:47]\n"
"s_waitcnt lgkmcnt(4)\n"
"v_mfma_f32_16x16x32_bf16 a[48:51], v[172:175], v[16:19], a[48:51]\n"
"v_mfma_f32_16x16x32_bf16 a[48:51], v[176:179], v[32:35], a[48:51]\n"
"v_mfma_f32_16x16x32_bf16 a[52:55], v[172:175], v[20:23], a[52:55]\n"
"v_mfma_f32_16x16x32_bf16 a[52:55], v[176:179], v[36:39], a[52:55]\n"
"v_mfma_f32_16x16x32_bf16 a[56:59], v[172:175], v[24:27], a[56:59]\n"
"ds_read_b64_tr_b16 v[156:157], v209 offset:49280\n"
"ds_read_b64_tr_b16 v[158:159], v209 offset:50816\n"
"v_mfma_f32_16x16x32_bf16 a[56:59], v[176:179], v[40:43], a[56:59]\n"
"ds_read_b64_tr_b16 v[160:161], v209 offset:61568\n"
"ds_read_b64_tr_b16 v[162:163], v209 offset:63104\n"
"v_mfma_f32_16x16x32_bf16 a[60:63], v[172:175], v[28:31], a[60:63]\n"
"buffer_load_dwordx4 v218, s[28:31], s50 offen offset:2048 lds\n"
"v_mfma_f32_16x16x32_bf16 a[60:63], v[176:179], v[44:47], a[60:63]\n"
"s_waitcnt lgkmcnt(4)\n"
"v_mfma_f32_16x16x32_bf16 a[64:67], v[148:151], v[16:19], a[64:67]\n"
"v_mfma_f32_16x16x32_bf16 a[64:67], v[152:155], v[32:35], a[64:67]\n"
"v_mfma_f32_16x16x32_bf16 a[68:71], v[148:151], v[20:23], a[68:71]\n"
"v_mfma_f32_16x16x32_bf16 a[68:71], v[152:155], v[36:39], a[68:71]\n"
"buffer_load_dwordx4 v219, s[28:31], s50 offen offset:3072 lds\n"
"v_mfma_f32_16x16x32_bf16 a[72:75], v[148:151], v[24:27], a[72:75]\n"
"ds_read_b64_tr_b16 v[164:165], v210 offset:49280\n"
"ds_read_b64_tr_b16 v[166:167], v210 offset:50816\n"
"v_mfma_f32_16x16x32_bf16 a[72:75], v[152:155], v[40:43], a[72:75]\n"
"ds_read_b64_tr_b16 v[168:169], v210 offset:61568\n"
"ds_read_b64_tr_b16 v[170:171], v210 offset:63104\n"
"v_mfma_f32_16x16x32_bf16 a[76:79], v[148:151], v[28:31], a[76:79]\n"
"v_mfma_f32_16x16x32_bf16 a[76:79], v[152:155], v[44:47], a[76:79]\n"
"s_waitcnt lgkmcnt(4)\n"
"v_mfma_f32_16x16x32_bf16 a[80:83], v[156:159], v[16:19], a[80:83]\n"
"v_mfma_f32_16x16x32_bf16 a[80:83], v[160:163], v[32:35], a[80:83]\n"
"s_add_u32 m0, s52, 4096\n"
"v_mfma_f32_16x16x32_bf16 a[84:87], v[156:159], v[20:23], a[84:87]\n"
"buffer_load_dwordx4 v220, s[28:31], s50 offen offset:0 lds\n"
"v_mfma_f32_16x16x32_bf16 a[84:87], v[160:163], v[36:39], a[84:87]\n"
"v_mfma_f32_16x16x32_bf16 a[88:91], v[156:159], v[24:27], a[88:91]\n"
"ds_read_b64_tr_b16 v[172:173], v211 offset:49280\n"
"ds_read_b64_tr_b16 v[174:175], v211 offset:50816\n"
"ds_read_b64_tr_b16 v[148:149], v208 offset:49408\n"
"ds_read_b64_tr_b16 v[150:151], v208 offset:50944\n"
"v_mfma_f32_16x16x32_bf16 a[88:91], v[160:163], v[40:43], a[88:91]\n"
"ds_read_b64_tr_b16 v[176:177], v211 offset:61568\n"
"ds_read_b64_tr_b16 v[178:179], v211 offset:63104\n"
"ds_read_b64_tr_b16 v[152:153], v208 offset:61696\n"
"ds_read_b64_tr_b16 v[154:155], v208 offset:63232\n"
"v_mfma_f32_16x16x32_bf16 a[92:95], v[156:159], v[28:31], a[92:95]\n"
"v_mfma_f32_16x16x32_bf16 a[92:95], v[160:163], v[44:47], a[92:95]\n"
"s_waitcnt lgkmcnt(8)\n"
"v_mfma_f32_16x16x32_bf16 a[96:99], v[164:167], v[16:19], a[96:99]\n"
"v_mfma_f32_16x16x32_bf16 a[96:99], v[168:171], v[32:35], a[96:99]\n"
"buffer_load_dwordx4 v221, s[28:31], s50 offen offset:1024 lds\n"
"v_mfma_f32_16x16x32_bf16 a[100:103], v[164:167], v[20:23], a[100:103]\n"
"ds_read_b64_tr_b16 v[156:157], v209 offset:49408\n"
"ds_read_b64_tr_b16 v[158:159], v209 offset:50944\n"
"v_mfma_f32_16x16x32_bf16 a[100:103], v[168:171], v[36:39], a[100:103]\n"
"ds_read_b64_tr_b16 v[160:161], v209 offset:61696\n"
"ds_read_b64_tr_b16 v[162:163], v209 offset:63232\n"
"v_mfma_f32_16x16x32_bf16 a[104:107], v[164:167], v[24:27], a[104:107]\n"
"v_mfma_f32_16x16x32_bf16 a[104:107], v[168:171], v[40:43], a[104:107]\n"
"v_mfma_f32_16x16x32_bf16 a[108:111], v[164:167], v[28:31], a[108:111]\n"
"v_mfma_f32_16x16x32_bf16 a[108:111], v[168:171], v[44:47], a[108:111]\n"
"s_waitcnt lgkmcnt(6)\n"
"v_mfma_f32_16x16x32_bf16 a[112:115], v[172:175], v[16:19], a[112:115]\n"
"buffer_load_dwordx4 v222, s[28:31], s50 offen offset:2048 lds\n"
"v_mfma_f32_16x16x32_bf16 a[112:115], v[176:179], v[32:35], a[112:115]\n"
"ds_read_b64_tr_b16 v[164:165], v210 offset:49408\n"
"ds_read_b64_tr_b16 v[166:167], v210 offset:50944\n"
"v_mfma_f32_16x16x32_bf16 a[116:119], v[172:175], v[20:23], a[116:119]\n"
"ds_read_b64_tr_b16 v[168:169], v210 offset:61696\n"
"ds_read_b64_tr_b16 v[170:171], v210 offset:63232\n"
"v_mfma_f32_16x16x32_bf16 a[116:119], v[176:179], v[36:39], a[116:119]\n"
"v_mfma_f32_16x16x32_bf16 a[120:123], v[172:175], v[24:27], a[120:123]\n"
"v_mfma_f32_16x16x32_bf16 a[120:123], v[176:179], v[40:43], a[120:123]\n"
"v_mfma_f32_16x16x32_bf16 a[124:127], v[172:175], v[28:31], a[124:127]\n"
"v_mfma_f32_16x16x32_bf16 a[124:127], v[176:179], v[44:47], a[124:127]\n"
"s_waitcnt lgkmcnt(8)\n"
"buffer_load_dwordx4 v223, s[28:31], s50 offen offset:3072 lds\n"
"v_mfma_f32_16x16x32_bf16 a[128:131], v[148:151], v[16:19], a[128:131]\n"
"ds_read_b64_tr_b16 v[172:173], v211 offset:49408\n"
"ds_read_b64_tr_b16 v[174:175], v211 offset:50944\n"
"v_mfma_f32_16x16x32_bf16 a[128:131], v[152:155], v[32:35], a[128:131]\n"
"s_mov_b32 m0, s87\n"
"ds_read_b64_tr_b16 v[176:177], v211 offset:61696\n"
"ds_read_b64_tr_b16 v[178:179], v211 offset:63232\n"
"v_mfma_f32_16x16x32_bf16 a[132:135], v[148:151], v[20:23], a[132:135]\n"
"buffer_load_dwordx4 v204, s[24:27], s86 offen offset:2048 nt lds\n"
"v_mfma_f32_16x16x32_bf16 a[132:135], v[152:155], v[36:39], a[132:135]\n"
"v_mfma_f32_16x16x32_bf16 a[136:139], v[148:151], v[24:27], a[136:139]\n"
"v_mfma_f32_16x16x32_bf16 a[136:139], v[152:155], v[40:43], a[136:139]\n"
"buffer_load_dwordx4 v205, s[24:27], s86 offen offset:3072 nt lds\n"
"v_mfma_f32_16x16x32_bf16 a[140:143], v[148:151], v[28:31], a[140:143]\n"
"s_mov_b32 s95, s51\n"
"v_mfma_f32_16x16x32_bf16 a[140:143], v[152:155], v[44:47], a[140:143]\n"
"s_waitcnt lgkmcnt(8)\n"
"v_mfma_f32_16x16x32_bf16 a[144:147], v[156:159], v[16:19], a[144:147]\n"
"v_mfma_f32_16x16x32_bf16 a[144:147], v[160:163], v[32:35], a[144:147]\n"
"s_waitcnt vmcnt(10) lgkmcnt(0)\n"
"s_barrier\n"
"v_mfma_f32_16x16x32_bf16 a[148:151], v[156:159], v[20:23], a[148:151]\n"
"ds_read_b64_tr_b16 v[148:149], v208 offset:24576\n"
"ds_read_b64_tr_b16 v[150:151], v208 offset:26112\n"
"v_mfma_f32_16x16x32_bf16 a[148:151], v[160:163], v[36:39], a[148:151]\n"
"ds_read_b64_tr_b16 v[152:153], v208 offset:36864\n"
"ds_read_b64_tr_b16 v[154:155], v208 offset:38400\n"
"v_mfma_f32_16x16x32_bf16 a[152:155], v[156:159], v[24:27], a[152:155]\n"
"ds_read_b128 v[48:51], v214 offset:0\n"
"ds_read_b128 v[64:67], v215 offset:0\n"
"v_mfma_f32_16x16x32_bf16 a[152:155], v[160:163], v[40:43], a[152:155]\n"
"ds_read_b128 v[52:55], v214 offset:2048\n"
"ds_read_b128 v[68:71], v215 offset:2048\n"
"v_mfma_f32_16x16x32_bf16 a[156:159], v[156:159], v[28:31], a[156:159]\n"
"ds_read_b128 v[56:59], v214 offset:4096\n"
"ds_read_b128 v[72:75], v215 offset:4096\n"
"v_mfma_f32_16x16x32_bf16 a[156:159], v[160:163], v[44:47], a[156:159]\n"
"ds_read_b128 v[60:63], v214 offset:6144\n"
"ds_read_b128 v[76:79], v215 offset:6144\n"
"v_mfma_f32_16x16x32_bf16 a[160:163], v[164:167], v[16:19], a[160:163]\n"
"s_mov_b32 m0, s54\n"
"v_mfma_f32_16x16x32_bf16 a[160:163], v[168:171], v[32:35], a[160:163]\n"
"buffer_load_dwordx4 v224, s[32:35], s95 offen offset:0 lds\n"
"v_mfma_f32_16x16x32_bf16 a[164:167], v[164:167], v[20:23], a[164:167]\n"
"buffer_load_dwordx4 v225, s[32:35], s95 offen offset:1024 lds\n"
"v_mfma_f32_16x16x32_bf16 a[164:167], v[168:171], v[36:39], a[164:167]\n"
"s_add_u32 s59, s59, 1\n"
"s_cmp_eq_u32 s59, 10\n"
"s_cselect_b32 s59, 0, s59\n"
"v_mfma_f32_16x16x32_bf16 a[168:171], v[164:167], v[24:27], a[168:171]\n"
"s_lshl_b32 s50, s59, 7\n"
"ds_read_b64_tr_b16 v[156:157], v209 offset:24576\n"
"ds_read_b64_tr_b16 v[158:159], v209 offset:26112\n"
"v_mfma_f32_16x16x32_bf16 a[168:171], v[168:171], v[40:43], a[168:171]\n"
"s_add_u32 s50, s50, s46\n"
"ds_read_b64_tr_b16 v[160:161], v209 offset:36864\n"
"ds_read_b64_tr_b16 v[162:163], v209 offset:38400\n"
"v_mfma_f32_16x16x32_bf16 a[172:175], v[164:167], v[28:31], a[172:175]\n"
"s_mul_i32 s51, s59, 786432\n"
"buffer_load_dwordx4 v226, s[32:35], s95 offen offset:2048 lds\n"
"v_mfma_f32_16x16x32_bf16 a[172:175], v[168:171], v[44:47], a[172:175]\n"
"s_add_u32 s51, s51, s47\n"
"v_mfma_f32_16x16x32_bf16 a[176:179], v[172:175], v[16:19], a[176:179]\n"
"v_mfma_f32_16x16x32_bf16 a[176:179], v[176:179], v[32:35], a[176:179]\n"
"v_mfma_f32_16x16x32_bf16 a[180:183], v[172:175], v[20:23], a[180:183]\n"
"v_mfma_f32_16x16x32_bf16 a[180:183], v[176:179], v[36:39], a[180:183]\n"
"buffer_load_dwordx4 v227, s[32:35], s95 offen offset:3072 lds\n"
"v_mfma_f32_16x16x32_bf16 a[184:187], v[172:175], v[24:27], a[184:187]\n"
"v_mfma_f32_16x16x32_bf16 a[184:187], v[176:179], v[40:43], a[184:187]\n"
"v_mfma_f32_16x16x32_bf16 a[188:191], v[172:175], v[28:31], a[188:191]\n"
"v_mfma_f32_16x16x32_bf16 a[188:191], v[176:179], v[44:47], a[188:191]\n"
"s_waitcnt lgkmcnt(4)\n"
"v_mfma_f32_16x16x32_bf16 a[0:3], v[148:151], v[48:51], a[0:3]\n"
"v_mfma_f32_16x16x32_bf16 a[0:3], v[152:155], v[64:67], a[0:3]\n"
"s_add_u32 m0, s54, 4096\n"
"v_mfma_f32_16x16x32_bf16 a[4:7], v[148:151], v[52:55], a[4:7]\n"
"buffer_load_dwordx4 v228, s[32:35], s95 offen offset:0 lds\n"
"v_mfma_f32_16x16x32_bf16 a[4:7], v[152:155], v[68:71], a[4:7]\n"
"v_mfma_f32_16x16x32_bf16 a[8:11], v[148:151], v[56:59], a[8:11]\n"
"ds_read_b64_tr_b16 v[164:165], v210 offset:24576\n"
"ds_read_b64_tr_b16 v[166:167], v210 offset:26112\n"
"v_mfma_f32_16x16x32_bf16 a[8:11], v[152:155], v[72:75], a[8:11]\n"
"ds_read_b64_tr_b16 v[168:169], v210 offset:36864\n"
"ds_read_b64_tr_b16 v[170:171], v210 offset:38400\n"
"v_mfma_f32_16x16x32_bf16 a[12:15], v[148:151], v[60:63], a[12:15]\n"
"v_mfma_f32_16x16x32_bf16 a[12:15], v[152:155], v[76:79], a[12:15]\n"
"s_waitcnt lgkmcnt(4)\n"
"v_mfma_f32_16x16x32_bf16 a[16:19], v[156:159], v[48:51], a[16:19]\n"
"buffer_load_dwordx4 v229, s[32:35], s95 offen offset:1024 lds\n"
"v_mfma_f32_16x16x32_bf16 a[16:19], v[160:163], v[64:67], a[16:19]\n"
"v_mfma_f32_16x16x32_bf16 a[20:23], v[156:159], v[52:55], a[20:23]\n"
"v_mfma_f32_16x16x32_bf16 a[20:23], v[160:163], v[68:71], a[20:23]\n"
"v_mfma_f32_16x16x32_bf16 a[24:27], v[156:159], v[56:59], a[24:27]\n"
"ds_read_b64_tr_b16 v[172:173], v211 offset:24576\n"
"ds_read_b64_tr_b16 v[174:175], v211 offset:26112\n"
"v_mfma_f32_16x16x32_bf16 a[24:27], v[160:163], v[72:75], a[24:27]\n"
"s_mov_b32 m0, s53\n"
"ds_read_b64_tr_b16 v[176:177], v211 offset:36864\n"
"ds_read_b64_tr_b16 v[178:179], v211 offset:38400\n"
"v_mfma_f32_16x16x32_bf16 a[28:31], v[156:159], v[60:63], a[28:31]\n"
"buffer_load_dwordx4 v216, s[28:31], s50 offen offset:0 lds\n"
"v_mfma_f32_16x16x32_bf16 a[28:31], v[160:163], v[76:79], a[28:31]\n"
"s_waitcnt lgkmcnt(4)\n"
"v_mfma_f32_16x16x32_bf16 a[32:35], v[164:167], v[48:51], a[32:35]\n"
"v_mfma_f32_16x16x32_bf16 a[32:35], v[168:171], v[64:67], a[32:35]\n"
"v_mfma_f32_16x16x32_bf16 a[36:39], v[164:167], v[52:55], a[36:39]\n"
"v_mfma_f32_16x16x32_bf16 a[36:39], v[168:171], v[68:71], a[36:39]\n"
"v_mfma_f32_16x16x32_bf16 a[40:43], v[164:167], v[56:59], a[40:43]\n"
"ds_read_b64_tr_b16 v[148:149], v208 offset:24704\n"
"ds_read_b64_tr_b16 v[150:151], v208 offset:26240\n"
"v_mfma_f32_16x16x32_bf16 a[40:43], v[168:171], v[72:75], a[40:43]\n"
"ds_read_b64_tr_b16 v[152:153], v208 offset:36992\n"
"ds_read_b64_tr_b16 v[154:155], v208 offset:38528\n"
"v_mfma_f32_16x16x32_bf16 a[44:47], v[164:167], v[60:63], a[44:47]\n"
"buffer_load_dwordx4 v217, s[28:31], s50 offen offset:1024 lds\n"
"v_mfma_f32_16x16x32_bf16 a[44:47], v[168:171], v[76:79], a[44:47]\n"
"s_waitcnt lgkmcnt(4)\n"
"v_mfma_f32_16x16x32_bf16 a[48:51], v[172:175], v[48:51], a[48:51]\n"
"v_mfma_f32_16x16x32_bf16 a[48:51], v[176:179], v[64:67], a[48:51]\n"
"v_mfma_f32_16x16x32_bf16 a[52:55], v[172:175], v[52:55], a[52:55]\n"
"v_mfma_f32_16x16x32_bf16 a[52:55], v[176:179], v[68:71], a[52:55]\n"
"v_mfma_f32_16x16x32_bf16 a[56:59], v[172:175], v[56:59], a[56:59]\n"
"ds_read_b64_tr_b16 v[156:157], v209 offset:24704\n"
"ds_read_b64_tr_b16 v[158:159], v209 offset:26240\n"
"v_mfma_f32_16x16x32_bf16 a[56:59], v[176:179], v[72:75], a[56:59]\n"
"ds_read_b64_tr_b16 v[160:161], v209 offset:36992\n"
"ds_read_b64_tr_b16 v[162:163], v209 offset:38528\n"
"v_mfma_f32_16x16x32_bf16 a[60:63], v[172:175], v[60:63], a[60:63]\n"
"buffer_load_dwordx4 v218, s[28:31], s50 offen offset:2048 lds\n"
"v_mfma_f32_16x16x32_bf16 a[60:63], v[176:179], v[76:79], a[60:63]\n"
"s_waitcnt lgkmcnt(4)\n"
"v_mfma_f32_16x16x32_bf16 a[64:67], v[148:151], v[48:51], a[64:67]\n"
"v_mfma_f32_16x16x32_bf16 a[64:67], v[152:155], v[64:67], a[64:67]\n"
"v_mfma_f32_16x16x32_bf16 a[68:71], v[148:151], v[52:55], a[68:71]\n"
"v_mfma_f32_16x16x32_bf16 a[68:71], v[152:155], v[68:71], a[68:71]\n"
"buffer_load_dwordx4 v219, s[28:31], s50 offen offset:3072 lds\n"
"v_mfma_f32_16x16x32_bf16 a[72:75], v[148:151], v[56:59], a[72:75]\n"
"ds_read_b64_tr_b16 v[164:165], v210 offset:24704\n"
"ds_read_b64_tr_b16 v[166:167], v210 offset:26240\n"
"v_mfma_f32_16x16x32_bf16 a[72:75], v[152:155], v[72:75], a[72:75]\n"
"ds_read_b64_tr_b16 v[168:169], v210 offset:36992\n"
"ds_read_b64_tr_b16 v[170:171], v210 offset:38528\n"
"v_mfma_f32_16x16x32_bf16 a[76:79], v[148:151], v[60:63], a[76:79]\n"
"v_mfma_f32_16x16x32_bf16 a[76:79], v[152:155], v[76:79], a[76:79]\n"
"s_waitcnt lgkmcnt(4)\n"
"v_mfma_f32_16x16x32_bf16 a[80:83], v[156:159], v[48:51], a[80:83]\n"
"v_mfma_f32_16x16x32_bf16 a[80:83], v[160:163], v[64:67], a[80:83]\n"
"s_add_u32 m0, s53, 4096\n"
"v_mfma_f32_16x16x32_bf16 a[84:87], v[156:159], v[52:55], a[84:87]\n"
"buffer_load_dwordx4 v220, s[28:31], s50 offen offset:0 lds\n"
"v_mfma_f32_16x16x32_bf16 a[84:87], v[160:163], v[68:71], a[84:87]\n"
"v_mfma_f32_16x16x32_bf16 a[88:91], v[156:159], v[56:59], a[88:91]\n"
"ds_read_b64_tr_b16 v[172:173], v211 offset:24704\n"
"ds_read_b64_tr_b16 v[174:175], v211 offset:26240\n"
"ds_read_b64_tr_b16 v[148:149], v208 offset:24832\n"
"ds_read_b64_tr_b16 v[150:151], v208 offset:26368\n"
"v_mfma_f32_16x16x32_bf16 a[88:91], v[160:163], v[72:75], a[88:91]\n"
"ds_read_b64_tr_b16 v[176:177], v211 offset:36992\n"
"ds_read_b64_tr_b16 v[178:179], v211 offset:38528\n"
"ds_read_b64_tr_b16 v[152:153], v208 offset:37120\n"
"ds_read_b64_tr_b16 v[154:155], v208 offset:38656\n"
"v_mfma_f32_16x16x32_bf16 a[92:95], v[156:159], v[60:63], a[92:95]\n"
"v_mfma_f32_16x16x32_bf16 a[92:95], v[160:163], v[76:79], a[92:95]\n"
"s_waitcnt lgkmcnt(8)\n"
"v_mfma_f32_16x16x32_bf16 a[96:99], v[164:167], v[48:51], a[96:99]\n"
"v_mfma_f32_16x16x32_bf16 a[96:99], v[168:171], v[64:67], a[96:99]\n"
"buffer_load_dwordx4 v221, s[28:31], s50 offen offset:1024 lds\n"
"v_mfma_f32_16x16x32_bf16 a[100:103], v[164:167], v[52:55], a[100:103]\n"
"ds_read_b64_tr_b16 v[156:157], v209 offset:24832\n"
"ds_read_b64_tr_b16 v[158:159], v209 offset:26368\n"
"v_mfma_f32_16x16x32_bf16 a[100:103], v[168:171], v[68:71], a[100:103]\n"
"ds_read_b64_tr_b16 v[160:161], v209 offset:37120\n"
"ds_read_b64_tr_b16 v[162:163], v209 offset:38656\n"
"v_mfma_f32_16x16x32_bf16 a[104:107], v[164:167], v[56:59], a[104:107]\n"
"v_mfma_f32_16x16x32_bf16 a[104:107], v[168:171], v[72:75], a[104:107]\n"
"v_mfma_f32_16x16x32_bf16 a[108:111], v[164:167], v[60:63], a[108:111]\n"
"v_mfma_f32_16x16x32_bf16 a[108:111], v[168:171], v[76:79], a[108:111]\n"
"s_waitcnt lgkmcnt(6)\n"
"v_mfma_f32_16x16x32_bf16 a[112:115], v[172:175], v[48:51], a[112:115]\n"
"buffer_load_dwordx4 v222, s[28:31], s50 offen offset:2048 lds\n"
"v_mfma_f32_16x16x32_bf16 a[112:115], v[176:179], v[64:67], a[112:115]\n"
"ds_read_b64_tr_b16 v[164:165], v210 offset:24832\n"
"ds_read_b64_tr_b16 v[166:167], v210 offset:26368\n"
"v_mfma_f32_16x16x32_bf16 a[116:119], v[172:175], v[52:55], a[116:119]\n"
"ds_read_b64_tr_b16 v[168:169], v210 offset:37120\n"
"ds_read_b64_tr_b16 v[170:171], v210 offset:38656\n"
"v_mfma_f32_16x16x32_bf16 a[116:119], v[176:179], v[68:71], a[116:119]\n"
"v_mfma_f32_16x16x32_bf16 a[120:123], v[172:175], v[56:59], a[120:123]\n"
"v_mfma_f32_16x16x32_bf16 a[120:123], v[176:179], v[72:75], a[120:123]\n"
"v_mfma_f32_16x16x32_bf16 a[124:127], v[172:175], v[60:63], a[124:127]\n"
"v_mfma_f32_16x16x32_bf16 a[124:127], v[176:179], v[76:79], a[124:127]\n"
"s_waitcnt lgkmcnt(8)\n"
"buffer_load_dwordx4 v223, s[28:31], s50 offen offset:3072 lds\n"
"v_mfma_f32_16x16x32_bf16 a[128:131], v[148:151], v[48:51], a[128:131]\n"
"ds_read_b64_tr_b16 v[172:173], v211 offset:24832\n"
"ds_read_b64_tr_b16 v[174:175], v211 offset:26368\n"
"v_mfma_f32_16x16x32_bf16 a[128:131], v[152:155], v[64:67], a[128:131]\n"
"s_add_u32 m0, s87, 4096\n"
"ds_read_b64_tr_b16 v[176:177], v211 offset:37120\n"
"ds_read_b64_tr_b16 v[178:179], v211 offset:38656\n"
"v_mfma_f32_16x16x32_bf16 a[132:135], v[148:151], v[52:55], a[132:135]\n"
"buffer_load_dwordx4 v206, s[24:27], s86 offen offset:0 nt lds\n"
"v_mfma_f32_16x16x32_bf16 a[132:135], v[152:155], v[68:71], a[132:135]\n"
"v_mfma_f32_16x16x32_bf16 a[136:139], v[148:151], v[56:59], a[136:139]\n"
"v_mfma_f32_16x16x32_bf16 a[136:139], v[152:155], v[72:75], a[136:139]\n"
"buffer_load_dwordx4 v207, s[24:27], s86 offen offset:1024 nt lds\n"
"v_mfma_f32_16x16x32_bf16 a[140:143], v[148:151], v[60:63], a[140:143]\n"
"s_mov_b32 s95, s51\n"
"v_mfma_f32_16x16x32_bf16 a[140:143], v[152:155], v[76:79], a[140:143]\n"
"s_waitcnt lgkmcnt(8)\n"
"v_mfma_f32_16x16x32_bf16 a[144:147], v[156:159], v[48:51], a[144:147]\n"
"v_mfma_f32_16x16x32_bf16 a[144:147], v[160:163], v[64:67], a[144:147]\n"
"s_waitcnt vmcnt(10) lgkmcnt(0)\n"
"s_barrier\n"
"v_mfma_f32_16x16x32_bf16 a[148:151], v[156:159], v[52:55], a[148:151]\n"
"ds_read_b64_tr_b16 v[148:149], v208 offset:49152\n"
"ds_read_b64_tr_b16 v[150:151], v208 offset:50688\n"
"v_mfma_f32_16x16x32_bf16 a[148:151], v[160:163], v[68:71], a[148:151]\n"
"ds_read_b64_tr_b16 v[152:153], v208 offset:61440\n"
"ds_read_b64_tr_b16 v[154:155], v208 offset:62976\n"
"v_mfma_f32_16x16x32_bf16 a[152:155], v[156:159], v[56:59], a[152:155]\n"
"ds_read_b128 v[16:19], v212 offset:0\n"
"ds_read_b128 v[32:35], v213 offset:0\n"
"v_mfma_f32_16x16x32_bf16 a[152:155], v[160:163], v[72:75], a[152:155]\n"
"ds_read_b128 v[20:23], v212 offset:2048\n"
"ds_read_b128 v[36:39], v213 offset:2048\n"
"v_mfma_f32_16x16x32_bf16 a[156:159], v[156:159], v[60:63], a[156:159]\n"
"ds_read_b128 v[24:27], v212 offset:4096\n"
"ds_read_b128 v[40:43], v213 offset:4096\n"
"v_mfma_f32_16x16x32_bf16 a[156:159], v[160:163], v[76:79], a[156:159]\n"
"ds_read_b128 v[28:31], v212 offset:6144\n"
"ds_read_b128 v[44:47], v213 offset:6144\n"
"v_mfma_f32_16x16x32_bf16 a[160:163], v[164:167], v[48:51], a[160:163]\n"
"s_mov_b32 m0, s55\n"
"v_mfma_f32_16x16x32_bf16 a[160:163], v[168:171], v[64:67], a[160:163]\n"
"buffer_load_dwordx4 v224, s[32:35], s95 offen offset:0 lds\n"
"v_mfma_f32_16x16x32_bf16 a[164:167], v[164:167], v[52:55], a[164:167]\n"
"buffer_load_dwordx4 v225, s[32:35], s95 offen offset:1024 lds\n"
"v_mfma_f32_16x16x32_bf16 a[164:167], v[168:171], v[68:71], a[164:167]\n"
"s_add_u32 s59, s59, 1\n"
"s_cmp_eq_u32 s59, 10\n"
"s_cselect_b32 s59, 0, s59\n"
"v_mfma_f32_16x16x32_bf16 a[168:171], v[164:167], v[56:59], a[168:171]\n"
"s_lshl_b32 s50, s59, 7\n"
"ds_read_b64_tr_b16 v[156:157], v209 offset:49152\n"
"ds_read_b64_tr_b16 v[158:159], v209 offset:50688\n"
"v_mfma_f32_16x16x32_bf16 a[168:171], v[168:171], v[72:75], a[168:171]\n"
"s_add_u32 s50, s50, s46\n"
"ds_read_b64_tr_b16 v[160:161], v209 offset:61440\n"
"ds_read_b64_tr_b16 v[162:163], v209 offset:62976\n"
"v_mfma_f32_16x16x32_bf16 a[172:175], v[164:167], v[60:63], a[172:175]\n"
"s_mul_i32 s51, s59, 786432\n"
"buffer_load_dwordx4 v226, s[32:35], s95 offen offset:2048 lds\n"
"v_mfma_f32_16x16x32_bf16 a[172:175], v[168:171], v[76:79], a[172:175]\n"
"s_add_u32 s51, s51, s47\n"
"v_mfma_f32_16x16x32_bf16 a[176:179], v[172:175], v[48:51], a[176:179]\n"
"v_mfma_f32_16x16x32_bf16 a[176:179], v[176:179], v[64:67], a[176:179]\n"
"v_mfma_f32_16x16x32_bf16 a[180:183], v[172:175], v[52:55], a[180:183]\n"
"v_mfma_f32_16x16x32_bf16 a[180:183], v[176:179], v[68:71], a[180:183]\n"
"buffer_load_dwordx4 v227, s[32:35], s95 offen offset:3072 lds\n"
"v_mfma_f32_16x16x32_bf16 a[184:187], v[172:175], v[56:59], a[184:187]\n"
"v_mfma_f32_16x16x32_bf16 a[184:187], v[176:179], v[72:75], a[184:187]\n"
"v_mfma_f32_16x16x32_bf16 a[188:191], v[172:175], v[60:63], a[188:191]\n"
"v_mfma_f32_16x16x32_bf16 a[188:191], v[176:179], v[76:79], a[188:191]\n"
"s_waitcnt lgkmcnt(4)\n"
"v_mfma_f32_16x16x32_bf16 a[0:3], v[148:151], v[16:19], a[0:3]\n"
"v_mfma_f32_16x16x32_bf16 a[0:3], v[152:155], v[32:35], a[0:3]\n"
"s_add_u32 m0, s55, 4096\n"
"v_mfma_f32_16x16x32_bf16 a[4:7], v[148:151], v[20:23], a[4:7]\n"
"buffer_load_dwordx4 v228, s[32:35], s95 offen offset:0 lds\n"
"v_mfma_f32_16x16x32_bf16 a[4:7], v[152:155], v[36:39], a[4:7]\n"
"v_mfma_f32_16x16x32_bf16 a[8:11], v[148:151], v[24:27], a[8:11]\n"
"ds_read_b64_tr_b16 v[164:165], v210 offset:49152\n"
"ds_read_b64_tr_b16 v[166:167], v210 offset:50688\n"
"v_mfma_f32_16x16x32_bf16 a[8:11], v[152:155], v[40:43], a[8:11]\n"
"ds_read_b64_tr_b16 v[168:169], v210 offset:61440\n"
"ds_read_b64_tr_b16 v[170:171], v210 offset:62976\n"
"v_mfma_f32_16x16x32_bf16 a[12:15], v[148:151], v[28:31], a[12:15]\n"
"v_mfma_f32_16x16x32_bf16 a[12:15], v[152:155], v[44:47], a[12:15]\n"
"s_waitcnt lgkmcnt(4)\n"
"v_mfma_f32_16x16x32_bf16 a[16:19], v[156:159], v[16:19], a[16:19]\n"
"buffer_load_dwordx4 v229, s[32:35], s95 offen offset:1024 lds\n"
"v_mfma_f32_16x16x32_bf16 a[16:19], v[160:163], v[32:35], a[16:19]\n"
"v_mfma_f32_16x16x32_bf16 a[20:23], v[156:159], v[20:23], a[20:23]\n"
"v_mfma_f32_16x16x32_bf16 a[20:23], v[160:163], v[36:39], a[20:23]\n"
"v_mfma_f32_16x16x32_bf16 a[24:27], v[156:159], v[24:27], a[24:27]\n"
"ds_read_b64_tr_b16 v[172:173], v211 offset:49152\n"
"ds_read_b64_tr_b16 v[174:175], v211 offset:50688\n"
"v_mfma_f32_16x16x32_bf16 a[24:27], v[160:163], v[40:43], a[24:27]\n"
"ds_read_b64_tr_b16 v[176:177], v211 offset:61440\n"
"ds_read_b64_tr_b16 v[178:179], v211 offset:62976\n"
"v_mfma_f32_16x16x32_bf16 a[28:31], v[156:159], v[28:31], a[28:31]\n"
"v_mfma_f32_16x16x32_bf16 a[28:31], v[160:163], v[44:47], a[28:31]\n"
"s_waitcnt lgkmcnt(4)\n"
"v_mfma_f32_16x16x32_bf16 a[32:35], v[164:167], v[16:19], a[32:35]\n"
"v_mfma_f32_16x16x32_bf16 a[32:35], v[168:171], v[32:35], a[32:35]\n"
"v_mfma_f32_16x16x32_bf16 a[36:39], v[164:167], v[20:23], a[36:39]\n"
"v_mfma_f32_16x16x32_bf16 a[36:39], v[168:171], v[36:39], a[36:39]\n"
"v_mfma_f32_16x16x32_bf16 a[40:43], v[164:167], v[24:27], a[40:43]\n"
"ds_read_b64_tr_b16 v[148:149], v208 offset:49280\n"
"ds_read_b64_tr_b16 v[150:151], v208 offset:50816\n"
"v_mfma_f32_16x16x32_bf16 a[40:43], v[168:171], v[40:43], a[40:43]\n"
"ds_read_b64_tr_b16 v[152:153], v208 offset:61568\n"
"ds_read_b64_tr_b16 v[154:155], v208 offset:63104\n"
"v_mfma_f32_16x16x32_bf16 a[44:47], v[164:167], v[28:31], a[44:47]\n"
"v_mfma_f32_16x16x32_bf16 a[44:47], v[168:171], v[44:47], a[44:47]\n"
"s_waitcnt lgkmcnt(4)\n"
"v_mfma_f32_16x16x32_bf16 a[48:51], v[172:175], v[16:19], a[48:51]\n"
"v_mfma_f32_16x16x32_bf16 a[48:51], v[176:179], v[32:35], a[48:51]\n"
"v_mfma_f32_16x16x32_bf16 a[52:55], v[172:175], v[20:23], a[52:55]\n"
"v_mfma_f32_16x16x32_bf16 a[52:55], v[176:179], v[36:39], a[52:55]\n"
"v_mfma_f32_16x16x32_bf16 a[56:59], v[172:175], v[24:27], a[56:59]\n"
"ds_read_b64_tr_b16 v[156:157], v209 offset:49280\n"
"ds_read_b64_tr_b16 v[158:159], v209 offset:50816\n"
"v_mfma_f32_16x16x32_bf16 a[56:59], v[176:179], v[40:43], a[56:59]\n"
"ds_read_b64_tr_b16 v[160:161], v209 offset:61568\n"
"ds_read_b64_tr_b16 v[162:163], v209 offset:63104\n"
"v_mfma_f32_16x16x32_bf16 a[60:63], v[172:175], v[28:31], a[60:63]\n"
"s_add_u32 s61, s89, 49152\n"
"v_mfma_f32_16x16x32_bf16 a[60:63], v[176:179], v[44:47], a[60:63]\n"
"s_waitcnt lgkmcnt(4)\n"
"s_add_u32 s64, s89, 122880\n"
"v_mfma_f32_16x16x32_bf16 a[64:67], v[148:151], v[16:19], a[64:67]\n"
"s_add_u32 s65, s89, 73728\n"
"v_mfma_f32_16x16x32_bf16 a[64:67], v[152:155], v[32:35], a[64:67]\n"
"s_add_u32 s62, s88, 1\n"
"v_mfma_f32_16x16x32_bf16 a[68:71], v[148:151], v[20:23], a[68:71]\n"
"s_and_b32 s62, s62, 15\n"
"v_mfma_f32_16x16x32_bf16 a[68:71], v[152:155], v[36:39], a[68:71]\n"
"s_mul_i32 s62, s62, 24576\n"
"v_mfma_f32_16x16x32_bf16 a[72:75], v[148:151], v[24:27], a[72:75]\n"
"s_add_u32 s62, s62, s89\n"
"ds_read_b64_tr_b16 v[164:165], v210 offset:49280\n"
"ds_read_b64_tr_b16 v[166:167], v210 offset:50816\n"
"v_mfma_f32_16x16x32_bf16 a[72:75], v[152:155], v[40:43], a[72:75]\n"
"s_add_u32 s63, s88, 0\n"
"ds_read_b64_tr_b16 v[168:169], v210 offset:61568\n"
"ds_read_b64_tr_b16 v[170:171], v210 offset:63104\n"
"v_mfma_f32_16x16x32_bf16 a[76:79], v[148:151], v[28:31], a[76:79]\n"
"s_and_b32 s63, s63, 15\n"
"v_mfma_f32_16x16x32_bf16 a[76:79], v[152:155], v[44:47], a[76:79]\n"
"s_waitcnt lgkmcnt(4)\n"
"s_mul_i32 s63, s63, 24576\n"
"v_mfma_f32_16x16x32_bf16 a[80:83], v[156:159], v[16:19], a[80:83]\n"
"s_add_u32 s63, s63, s89\n"
"v_mfma_f32_16x16x32_bf16 a[80:83], v[160:163], v[32:35], a[80:83]\n"
"v_mfma_f32_16x16x32_bf16 a[84:87], v[156:159], v[20:23], a[84:87]\n"
"v_mfma_f32_16x16x32_bf16 a[84:87], v[160:163], v[36:39], a[84:87]\n"
"v_mfma_f32_16x16x32_bf16 a[88:91], v[156:159], v[24:27], a[88:91]\n"
"ds_read_b64_tr_b16 v[172:173], v211 offset:49280\n"
"ds_read_b64_tr_b16 v[174:175], v211 offset:50816\n"
"ds_read_b64_tr_b16 v[148:149], v208 offset:49408\n"
"ds_read_b64_tr_b16 v[150:151], v208 offset:50944\n"
"v_mfma_f32_16x16x32_bf16 a[88:91], v[160:163], v[40:43], a[88:91]\n"
"ds_read_b64_tr_b16 v[176:177], v211 offset:61568\n"
"ds_read_b64_tr_b16 v[178:179], v211 offset:63104\n"
"ds_read_b64_tr_b16 v[152:153], v208 offset:61696\n"
"ds_read_b64_tr_b16 v[154:155], v208 offset:63232\n"
"v_mfma_f32_16x16x32_bf16 a[92:95], v[156:159], v[28:31], a[92:95]\n"
"v_mfma_f32_16x16x32_bf16 a[92:95], v[160:163], v[44:47], a[92:95]\n"
"s_waitcnt lgkmcnt(8)\n"
"v_mfma_f32_16x16x32_bf16 a[96:99], v[164:167], v[16:19], a[96:99]\n"
"v_mfma_f32_16x16x32_bf16 a[96:99], v[168:171], v[32:35], a[96:99]\n"
"v_mfma_f32_16x16x32_bf16 a[100:103], v[164:167], v[20:23], a[100:103]\n"
"ds_read_b64_tr_b16 v[156:157], v209 offset:49408\n"
"ds_read_b64_tr_b16 v[158:159], v209 offset:50944\n"
"v_mfma_f32_16x16x32_bf16 a[100:103], v[168:171], v[36:39], a[100:103]\n"
"ds_read_b64_tr_b16 v[160:161], v209 offset:61696\n"
"ds_read_b64_tr_b16 v[162:163], v209 offset:63232\n"
"v_mfma_f32_16x16x32_bf16 a[104:107], v[164:167], v[24:27], a[104:107]\n"
"v_mfma_f32_16x16x32_bf16 a[104:107], v[168:171], v[40:43], a[104:107]\n"
"v_mfma_f32_16x16x32_bf16 a[108:111], v[164:167], v[28:31], a[108:111]\n"
"v_mfma_f32_16x16x32_bf16 a[108:111], v[168:171], v[44:47], a[108:111]\n"
"s_waitcnt lgkmcnt(6)\n"
"v_mfma_f32_16x16x32_bf16 a[112:115], v[172:175], v[16:19], a[112:115]\n"
"v_mfma_f32_16x16x32_bf16 a[112:115], v[176:179], v[32:35], a[112:115]\n"
"ds_read_b64_tr_b16 v[164:165], v210 offset:49408\n"
"ds_read_b64_tr_b16 v[166:167], v210 offset:50944\n"
"v_mfma_f32_16x16x32_bf16 a[116:119], v[172:175], v[20:23], a[116:119]\n"
"ds_read_b64_tr_b16 v[168:169], v210 offset:61696\n"
"ds_read_b64_tr_b16 v[170:171], v210 offset:63232\n"
"v_mfma_f32_16x16x32_bf16 a[116:119], v[176:179], v[36:39], a[116:119]\n"
"v_mfma_f32_16x16x32_bf16 a[120:123], v[172:175], v[24:27], a[120:123]\n"
"v_mfma_f32_16x16x32_bf16 a[120:123], v[176:179], v[40:43], a[120:123]\n"
"v_mfma_f32_16x16x32_bf16 a[124:127], v[172:175], v[28:31], a[124:127]\n"
"v_mfma_f32_16x16x32_bf16 a[124:127], v[176:179], v[44:47], a[124:127]\n"
"s_waitcnt lgkmcnt(8)\n"
"v_mfma_f32_16x16x32_bf16 a[128:131], v[148:151], v[16:19], a[128:131]\n"
"ds_read_b64_tr_b16 v[172:173], v211 offset:49408\n"
"ds_read_b64_tr_b16 v[174:175], v211 offset:50944\n"
"v_mfma_f32_16x16x32_bf16 a[128:131], v[152:155], v[32:35], a[128:131]\n"
"ds_read_b64_tr_b16 v[176:177], v211 offset:61696\n"
"ds_read_b64_tr_b16 v[178:179], v211 offset:63232\n"
"v_mfma_f32_16x16x32_bf16 a[132:135], v[148:151], v[20:23], a[132:135]\n"
"v_mfma_f32_16x16x32_bf16 a[132:135], v[152:155], v[36:39], a[132:135]\n"
"v_mfma_f32_16x16x32_bf16 a[136:139], v[148:151], v[24:27], a[136:139]\n"
"v_mfma_f32_16x16x32_bf16 a[136:139], v[152:155], v[40:43], a[136:139]\n"
"v_mfma_f32_16x16x32_bf16 a[140:143], v[148:151], v[28:31], a[140:143]\n"
"v_mfma_f32_16x16x32_bf16 a[140:143], v[152:155], v[44:47], a[140:143]\n"
"s_waitcnt lgkmcnt(8)\n"
"v_mfma_f32_16x16x32_bf16 a[144:147], v[156:159], v[16:19], a[144:147]\n"
"v_mfma_f32_16x16x32_bf16 a[144:147], v[160:163], v[32:35], a[144:147]\n"
"s_waitcnt vmcnt(0) lgkmcnt(0)\n"
"s_barrier\n"
"v_mfma_f32_16x16x32_bf16 a[148:151], v[156:159], v[20:23], a[148:151]\n"
"ds_read_b64_tr_b16 v[148:149], v208 offset:24576\n"
"ds_read_b64_tr_b16 v[150:151], v208 offset:26112\n"
"v_mfma_f32_16x16x32_bf16 a[148:151], v[160:163], v[36:39], a[148:151]\n"
"ds_read_b64_tr_b16 v[152:153], v208 offset:36864\n"
"ds_read_b64_tr_b16 v[154:155], v208 offset:38400\n"
"v_mfma_f32_16x16x32_bf16 a[152:155], v[156:159], v[24:27], a[152:155]\n"
"ds_read_b128 v[48:51], v214 offset:0\n"
"ds_read_b128 v[64:67], v215 offset:0\n"
"v_mfma_f32_16x16x32_bf16 a[152:155], v[160:163], v[40:43], a[152:155]\n"
"ds_read_b128 v[52:55], v214 offset:2048\n"
"ds_read_b128 v[68:71], v215 offset:2048\n"
"v_mfma_f32_16x16x32_bf16 a[156:159], v[156:159], v[28:31], a[156:159]\n"
"ds_read_b128 v[56:59], v214 offset:4096\n"
"ds_read_b128 v[72:75], v215 offset:4096\n"
"v_mfma_f32_16x16x32_bf16 a[156:159], v[160:163], v[44:47], a[156:159]\n"
"ds_read_b128 v[60:63], v214 offset:6144\n"
"ds_read_b128 v[76:79], v215 offset:6144\n"
"v_mfma_f32_16x16x32_bf16 a[160:163], v[164:167], v[16:19], a[160:163]\n"
"s_mov_b32 m0, s61\n"
"v_mfma_f32_16x16x32_bf16 a[160:163], v[168:171], v[32:35], a[160:163]\n"
"buffer_load_dwordx4 v202, s[20:23], s62 offen offset:0 nt lds\n"
"v_mfma_f32_16x16x32_bf16 a[164:167], v[164:167], v[20:23], a[164:167]\n"
"buffer_load_dwordx4 v203, s[20:23], s62 offen offset:1024 nt lds\n"
"v_mfma_f32_16x16x32_bf16 a[164:167], v[168:171], v[36:39], a[164:167]\n"
"v_mfma_f32_16x16x32_bf16 a[168:171], v[164:167], v[24:27], a[168:171]\n"
"ds_read_b64_tr_b16 v[156:157], v209 offset:24576\n"
"ds_read_b64_tr_b16 v[158:159], v209 offset:26112\n"
"v_mfma_f32_16x16x32_bf16 a[168:171], v[168:171], v[40:43], a[168:171]\n"
"ds_read_b64_tr_b16 v[160:161], v209 offset:36864\n"
"ds_read_b64_tr_b16 v[162:163], v209 offset:38400\n"
"v_mfma_f32_16x16x32_bf16 a[172:175], v[164:167], v[28:31], a[172:175]\n"
"buffer_load_dwordx4 v204, s[20:23], s62 offen offset:2048 nt lds\n"
"v_mfma_f32_16x16x32_bf16 a[172:175], v[168:171], v[44:47], a[172:175]\n"
"v_mfma_f32_16x16x32_bf16 a[176:179], v[172:175], v[16:19], a[176:179]\n"
"v_mfma_f32_16x16x32_bf16 a[176:179], v[176:179], v[32:35], a[176:179]\n"
"v_mfma_f32_16x16x32_bf16 a[180:183], v[172:175], v[20:23], a[180:183]\n"
"buffer_load_dwordx4 v205, s[20:23], s62 offen offset:3072 nt lds\n"
"v_mfma_f32_16x16x32_bf16 a[180:183], v[176:179], v[36:39], a[180:183]\n"
"v_mfma_f32_16x16x32_bf16 a[184:187], v[172:175], v[24:27], a[184:187]\n"
"v_mfma_f32_16x16x32_bf16 a[184:187], v[176:179], v[40:43], a[184:187]\n"
"v_mfma_f32_16x16x32_bf16 a[188:191], v[172:175], v[28:31], a[188:191]\n"
"v_mfma_f32_16x16x32_bf16 a[188:191], v[176:179], v[44:47], a[188:191]\n"
"s_waitcnt lgkmcnt(4)\n"
"v_mfma_f32_16x16x32_bf16 a[0:3], v[148:151], v[48:51], a[0:3]\n"
"s_add_u32 m0, s61, 4096\n"
"v_mfma_f32_16x16x32_bf16 a[0:3], v[152:155], v[64:67], a[0:3]\n"
"buffer_load_dwordx4 v206, s[20:23], s62 offen offset:0 nt lds\n"
"v_mfma_f32_16x16x32_bf16 a[4:7], v[148:151], v[52:55], a[4:7]\n"
"v_mfma_f32_16x16x32_bf16 a[4:7], v[152:155], v[68:71], a[4:7]\n"
"v_mfma_f32_16x16x32_bf16 a[8:11], v[148:151], v[56:59], a[8:11]\n"
"ds_read_b64_tr_b16 v[164:165], v210 offset:24576\n"
"ds_read_b64_tr_b16 v[166:167], v210 offset:26112\n"
"v_mfma_f32_16x16x32_bf16 a[8:11], v[152:155], v[72:75], a[8:11]\n"
"ds_read_b64_tr_b16 v[168:169], v210 offset:36864\n"
"ds_read_b64_tr_b16 v[170:171], v210 offset:38400\n"
"v_mfma_f32_16x16x32_bf16 a[12:15], v[148:151], v[60:63], a[12:15]\n"
"v_mfma_f32_16x16x32_bf16 a[12:15], v[152:155], v[76:79], a[12:15]\n"
"s_waitcnt lgkmcnt(4)\n"
"v_mfma_f32_16x16x32_bf16 a[16:19], v[156:159], v[48:51], a[16:19]\n"
"v_mfma_f32_16x16x32_bf16 a[16:19], v[160:163], v[64:67], a[16:19]\n"
"buffer_load_dwordx4 v207, s[20:23], s62 offen offset:1024 nt lds\n"
"v_mfma_f32_16x16x32_bf16 a[20:23], v[156:159], v[52:55], a[20:23]\n"
"v_mfma_f32_16x16x32_bf16 a[20:23], v[160:163], v[68:71], a[20:23]\n"
"v_mfma_f32_16x16x32_bf16 a[24:27], v[156:159], v[56:59], a[24:27]\n"
"ds_read_b64_tr_b16 v[172:173], v211 offset:24576\n"
"ds_read_b64_tr_b16 v[174:175], v211 offset:26112\n"
"v_mfma_f32_16x16x32_bf16 a[24:27], v[160:163], v[72:75], a[24:27]\n"
"ds_read_b64_tr_b16 v[176:177], v211 offset:36864\n"
"ds_read_b64_tr_b16 v[178:179], v211 offset:38400\n"
"v_mfma_f32_16x16x32_bf16 a[28:31], v[156:159], v[60:63], a[28:31]\n"
"v_mfma_f32_16x16x32_bf16 a[28:31], v[160:163], v[76:79], a[28:31]\n"
"s_waitcnt lgkmcnt(4)\n"
"v_mfma_f32_16x16x32_bf16 a[32:35], v[164:167], v[48:51], a[32:35]\n"
"v_mfma_f32_16x16x32_bf16 a[32:35], v[168:171], v[64:67], a[32:35]\n"
"s_mov_b32 m0, s64\n"
"v_mfma_f32_16x16x32_bf16 a[36:39], v[164:167], v[52:55], a[36:39]\n"
"buffer_load_dwordx4 v202, s[24:27], s62 offen offset:0 nt lds\n"
"v_mfma_f32_16x16x32_bf16 a[36:39], v[168:171], v[68:71], a[36:39]\n"
"v_mfma_f32_16x16x32_bf16 a[40:43], v[164:167], v[56:59], a[40:43]\n"
"ds_read_b64_tr_b16 v[148:149], v208 offset:24704\n"
"ds_read_b64_tr_b16 v[150:151], v208 offset:26240\n"
"v_mfma_f32_16x16x32_bf16 a[40:43], v[168:171], v[72:75], a[40:43]\n"
"ds_read_b64_tr_b16 v[152:153], v208 offset:36992\n"
"ds_read_b64_tr_b16 v[154:155], v208 offset:38528\n"
"v_mfma_f32_16x16x32_bf16 a[44:47], v[164:167], v[60:63], a[44:47]\n"
"v_mfma_f32_16x16x32_bf16 a[44:47], v[168:171], v[76:79], a[44:47]\n"
"s_waitcnt lgkmcnt(4)\n"
"v_mfma_f32_16x16x32_bf16 a[48:51], v[172:175], v[48:51], a[48:51]\n"
"v_mfma_f32_16x16x32_bf16 a[48:51], v[176:179], v[64:67], a[48:51]\n"
"v_mfma_f32_16x16x32_bf16 a[52:55], v[172:175], v[52:55], a[52:55]\n"
"buffer_load_dwordx4 v203, s[24:27], s62 offen offset:1024 nt lds\n"
"v_mfma_f32_16x16x32_bf16 a[52:55], v[176:179], v[68:71], a[52:55]\n"
"v_mfma_f32_16x16x32_bf16 a[56:59], v[172:175], v[56:59], a[56:59]\n"
"ds_read_b64_tr_b16 v[156:157], v209 offset:24704\n"
"ds_read_b64_tr_b16 v[158:159], v209 offset:26240\n"
"v_mfma_f32_16x16x32_bf16 a[56:59], v[176:179], v[72:75], a[56:59]\n"
"ds_read_b64_tr_b16 v[160:161], v209 offset:36992\n"
"ds_read_b64_tr_b16 v[162:163], v209 offset:38528\n"
"v_mfma_f32_16x16x32_bf16 a[60:63], v[172:175], v[60:63], a[60:63]\n"
"v_mfma_f32_16x16x32_bf16 a[60:63], v[176:179], v[76:79], a[60:63]\n"
"s_waitcnt lgkmcnt(4)\n"
"v_mfma_f32_16x16x32_bf16 a[64:67], v[148:151], v[48:51], a[64:67]\n"
"v_mfma_f32_16x16x32_bf16 a[64:67], v[152:155], v[64:67], a[64:67]\n"
"v_mfma_f32_16x16x32_bf16 a[68:71], v[148:151], v[52:55], a[68:71]\n"
"v_mfma_f32_16x16x32_bf16 a[68:71], v[152:155], v[68:71], a[68:71]\n"
"buffer_load_dwordx4 v204, s[24:27], s62 offen offset:2048 nt lds\n"
"v_mfma_f32_16x16x32_bf16 a[72:75], v[148:151], v[56:59], a[72:75]\n"
"ds_read_b64_tr_b16 v[164:165], v210 offset:24704\n"
"ds_read_b64_tr_b16 v[166:167], v210 offset:26240\n"
"v_mfma_f32_16x16x32_bf16 a[72:75], v[152:155], v[72:75], a[72:75]\n"
"ds_read_b64_tr_b16 v[168:169], v210 offset:36992\n"
"ds_read_b64_tr_b16 v[170:171], v210 offset:38528\n"
"v_mfma_f32_16x16x32_bf16 a[76:79], v[148:151], v[60:63], a[76:79]\n"
"v_mfma_f32_16x16x32_bf16 a[76:79], v[152:155], v[76:79], a[76:79]\n"
"s_waitcnt lgkmcnt(4)\n"
"v_mfma_f32_16x16x32_bf16 a[80:83], v[156:159], v[48:51], a[80:83]\n"
"v_mfma_f32_16x16x32_bf16 a[80:83], v[160:163], v[64:67], a[80:83]\n"
"v_mfma_f32_16x16x32_bf16 a[84:87], v[156:159], v[52:55], a[84:87]\n"
"v_mfma_f32_16x16x32_bf16 a[84:87], v[160:163], v[68:71], a[84:87]\n"
"buffer_load_dwordx4 v205, s[24:27], s62 offen offset:3072 nt lds\n"
"v_mfma_f32_16x16x32_bf16 a[88:91], v[156:159], v[56:59], a[88:91]\n"
"ds_read_b64_tr_b16 v[172:173], v211 offset:24704\n"
"ds_read_b64_tr_b16 v[174:175], v211 offset:26240\n"
"ds_read_b64_tr_b16 v[148:149], v208 offset:24832\n"
"ds_read_b64_tr_b16 v[150:151], v208 offset:26368\n"
"v_mfma_f32_16x16x32_bf16 a[88:91], v[160:163], v[72:75], a[88:91]\n"
"ds_read_b64_tr_b16 v[176:177], v211 offset:36992\n"
"ds_read_b64_tr_b16 v[178:179], v211 offset:38528\n"
"ds_read_b64_tr_b16 v[152:153], v208 offset:37120\n"
"ds_read_b64_tr_b16 v[154:155], v208 offset:38656\n"
"v_mfma_f32_16x16x32_bf16 a[92:95], v[156:159], v[60:63], a[92:95]\n"
"v_mfma_f32_16x16x32_bf16 a[92:95], v[160:163], v[76:79], a[92:95]\n"
"s_waitcnt lgkmcnt(8)\n"
"v_mfma_f32_16x16x32_bf16 a[96:99], v[164:167], v[48:51], a[96:99]\n"
"v_mfma_f32_16x16x32_bf16 a[96:99], v[168:171], v[64:67], a[96:99]\n"
"v_mfma_f32_16x16x32_bf16 a[100:103], v[164:167], v[52:55], a[100:103]\n"
"ds_read_b64_tr_b16 v[156:157], v209 offset:24832\n"
"ds_read_b64_tr_b16 v[158:159], v209 offset:26368\n"
"v_mfma_f32_16x16x32_bf16 a[100:103], v[168:171], v[68:71], a[100:103]\n"
"s_add_u32 m0, s64, 4096\n"
"ds_read_b64_tr_b16 v[160:161], v209 offset:37120\n"
"ds_read_b64_tr_b16 v[162:163], v209 offset:38656\n"
"v_mfma_f32_16x16x32_bf16 a[104:107], v[164:167], v[56:59], a[104:107]\n"
"buffer_load_dwordx4 v206, s[24:27], s62 offen offset:0 nt lds\n"
"v_mfma_f32_16x16x32_bf16 a[104:107], v[168:171], v[72:75], a[104:107]\n"
"v_mfma_f32_16x16x32_bf16 a[108:111], v[164:167], v[60:63], a[108:111]\n"
"v_mfma_f32_16x16x32_bf16 a[108:111], v[168:171], v[76:79], a[108:111]\n"
"s_waitcnt lgkmcnt(6)\n"
"v_mfma_f32_16x16x32_bf16 a[112:115], v[172:175], v[48:51], a[112:115]\n"
"v_mfma_f32_16x16x32_bf16 a[112:115], v[176:179], v[64:67], a[112:115]\n"
"ds_read_b64_tr_b16 v[164:165], v210 offset:24832\n"
"ds_read_b64_tr_b16 v[166:167], v210 offset:26368\n"
"v_mfma_f32_16x16x32_bf16 a[116:119], v[172:175], v[52:55], a[116:119]\n"
"ds_read_b64_tr_b16 v[168:169], v210 offset:37120\n"
"ds_read_b64_tr_b16 v[170:171], v210 offset:38656\n"
"v_mfma_f32_16x16x32_bf16 a[116:119], v[176:179], v[68:71], a[116:119]\n"
"v_mfma_f32_16x16x32_bf16 a[120:123], v[172:175], v[56:59], a[120:123]\n"
"buffer_load_dwordx4 v207, s[24:27], s62 offen offset:1024 nt lds\n"
"v_mfma_f32_16x16x32_bf16 a[120:123], v[176:179], v[72:75], a[120:123]\n"
"v_mfma_f32_16x16x32_bf16 a[124:127], v[172:175], v[60:63], a[124:127]\n"
"s_add_u32 s42, s89, 98304\n"
"v_mfma_f32_16x16x32_bf16 a[124:127], v[176:179], v[76:79], a[124:127]\n"
"s_waitcnt lgkmcnt(8)\n"
"s_add_u32 s44, s88, 2\n"
"v_mfma_f32_16x16x32_bf16 a[128:131], v[148:151], v[48:51], a[128:131]\n"
"s_and_b32 s44, s44, 15\n"
"ds_read_b64_tr_b16 v[172:173], v211 offset:24832\n"
"ds_read_b64_tr_b16 v[174:175], v211 offset:26368\n"
"v_mfma_f32_16x16x32_bf16 a[128:131], v[152:155], v[64:67], a[128:131]\n"
"s_mul_i32 s44, s44, 24576\n"
"ds_read_b64_tr_b16 v[176:177], v211 offset:37120\n"
"ds_read_b64_tr_b16 v[178:179], v211 offset:38656\n"
"v_mfma_f32_16x16x32_bf16 a[132:135], v[148:151], v[52:55], a[132:135]\n"
"s_add_u32 s44, s44, s89\n"
"v_mfma_f32_16x16x32_bf16 a[132:135], v[152:155], v[68:71], a[132:135]\n"
"v_mfma_f32_16x16x32_bf16 a[136:139], v[148:151], v[56:59], a[136:139]\n"
"v_mfma_f32_16x16x32_bf16 a[136:139], v[152:155], v[72:75], a[136:139]\n"
"v_mfma_f32_16x16x32_bf16 a[140:143], v[148:151], v[60:63], a[140:143]\n"
"v_mfma_f32_16x16x32_bf16 a[140:143], v[152:155], v[76:79], a[140:143]\n"
"s_waitcnt lgkmcnt(8)\n"
"v_mfma_f32_16x16x32_bf16 a[144:147], v[156:159], v[48:51], a[144:147]\n"
"v_mfma_f32_16x16x32_bf16 a[144:147], v[160:163], v[64:67], a[144:147]\n"
"s_waitcnt lgkmcnt(0)\n"
"s_barrier\n"
"v_mfma_f32_16x16x32_bf16 a[148:151], v[156:159], v[52:55], a[148:151]\n"
"s_mov_b32 m0, s42\n"
"v_mfma_f32_16x16x32_bf16 a[148:151], v[160:163], v[68:71], a[148:151]\n"
"buffer_load_dwordx4 v202, s[20:23], s44 offen offset:0 nt lds\n"
"v_mfma_f32_16x16x32_bf16 a[152:155], v[156:159], v[56:59], a[152:155]\n"
"v_mfma_f32_16x16x32_bf16 a[152:155], v[160:163], v[72:75], a[152:155]\n"
"v_mfma_f32_16x16x32_bf16 a[156:159], v[156:159], v[60:63], a[156:159]\n"
"buffer_load_dwordx4 v203, s[20:23], s44 offen offset:1024 nt lds\n"
"v_mfma_f32_16x16x32_bf16 a[156:159], v[160:163], v[76:79], a[156:159]\n"
"v_mfma_f32_16x16x32_bf16 a[160:163], v[164:167], v[48:51], a[160:163]\n"
"v_mfma_f32_16x16x32_bf16 a[160:163], v[168:171], v[64:67], a[160:163]\n"
"v_mfma_f32_16x16x32_bf16 a[164:167], v[164:167], v[52:55], a[164:167]\n"
"buffer_load_dwordx4 v204, s[20:23], s44 offen offset:2048 nt lds\n"
"v_mfma_f32_16x16x32_bf16 a[164:167], v[168:171], v[68:71], a[164:167]\n"
"v_mfma_f32_16x16x32_bf16 a[168:171], v[164:167], v[56:59], a[168:171]\n"
"v_mfma_f32_16x16x32_bf16 a[168:171], v[168:171], v[72:75], a[168:171]\n"
"v_mfma_f32_16x16x32_bf16 a[172:175], v[164:167], v[60:63], a[172:175]\n"
"buffer_load_dwordx4 v205, s[20:23], s44 offen offset:3072 nt lds\n"
"v_mfma_f32_16x16x32_bf16 a[172:175], v[168:171], v[76:79], a[172:175]\n"
"v_mfma_f32_16x16x32_bf16 a[176:179], v[172:175], v[48:51], a[176:179]\n"
"s_add_u32 m0, s42, 4096\n"
"v_mfma_f32_16x16x32_bf16 a[176:179], v[176:179], v[64:67], a[176:179]\n"
"buffer_load_dwordx4 v206, s[20:23], s44 offen offset:0 nt lds\n"
"v_mfma_f32_16x16x32_bf16 a[180:183], v[172:175], v[52:55], a[180:183]\n"
"v_mfma_f32_16x16x32_bf16 a[180:183], v[176:179], v[68:71], a[180:183]\n"
"v_mfma_f32_16x16x32_bf16 a[184:187], v[172:175], v[56:59], a[184:187]\n"
"v_mfma_f32_16x16x32_bf16 a[184:187], v[176:179], v[72:75], a[184:187]\n"
"buffer_load_dwordx4 v207, s[20:23], s44 offen offset:1024 nt lds\n"
"v_mfma_f32_16x16x32_bf16 a[188:191], v[172:175], v[60:63], a[188:191]\n"
"v_mfma_f32_16x16x32_bf16 a[188:191], v[176:179], v[76:79], a[188:191]\n"
"s_nop 7\n"
"v_accvgpr_read_b32 v230, a0\n"
"v_accvgpr_read_b32 v231, a1\n"
"v_accvgpr_read_b32 v232, a2\n"
"v_accvgpr_read_b32 v233, a3\n"
"v_accvgpr_read_b32 v234, a16\n"
"v_accvgpr_read_b32 v235, a17\n"
"v_accvgpr_read_b32 v236, a18\n"
"v_accvgpr_read_b32 v237, a19\n"
"v_pk_mul_f32 v[230:231], v[230:231], s[56:57]\n"
"v_pk_mul_f32 v[232:233], v[232:233], s[56:57]\n"
"v_pk_mul_f32 v[234:235], v[234:235], s[56:57]\n"
"v_pk_mul_f32 v[236:237], v[236:237], s[56:57]\n"
"v_cvt_pk_bf16_f32 v16, v230, v231\n"
"v_cvt_pk_bf16_f32 v17, v232, v233\n"
"v_cvt_pk_bf16_f32 v18, v234, v235\n"
"v_cvt_pk_bf16_f32 v19, v236, v237\n"
"v_accvgpr_read_b32 v238, a32\n"
"v_accvgpr_read_b32 v239, a33\n"
"v_accvgpr_read_b32 v240, a34\n"
"v_accvgpr_read_b32 v241, a35\n"
"v_accvgpr_read_b32 v242, a48\n"
"v_accvgpr_read_b32 v243, a49\n"
"v_accvgpr_read_b32 v244, a50\n"
"v_accvgpr_read_b32 v245, a51\n"
"v_pk_mul_f32 v[238:239], v[238:239], s[56:57]\n"
"v_pk_mul_f32 v[240:241], v[240:241], s[56:57]\n"
"v_pk_mul_f32 v[242:243], v[242:243], s[56:57]\n"
"v_pk_mul_f32 v[244:245], v[244:245], s[56:57]\n"
"v_cvt_pk_bf16_f32 v20, v238, v239\n"
"v_cvt_pk_bf16_f32 v21, v240, v241\n"
"v_cvt_pk_bf16_f32 v22, v242, v243\n"
"v_cvt_pk_bf16_f32 v23, v244, v245\n"
"v_accvgpr_read_b32 v230, a64\n"
"v_accvgpr_read_b32 v231, a65\n"
"v_accvgpr_read_b32 v232, a66\n"
"v_accvgpr_read_b32 v233, a67\n"
"v_accvgpr_read_b32 v234, a80\n"
"v_accvgpr_read_b32 v235, a81\n"
"v_accvgpr_read_b32 v236, a82\n"
"v_accvgpr_read_b32 v237, a83\n"
"v_pk_mul_f32 v[230:231], v[230:231], s[56:57]\n"
"v_pk_mul_f32 v[232:233], v[232:233], s[56:57]\n"
"v_pk_mul_f32 v[234:235], v[234:235], s[56:57]\n"
"v_pk_mul_f32 v[236:237], v[236:237], s[56:57]\n"
"v_cvt_pk_bf16_f32 v24, v230, v231\n"
"v_cvt_pk_bf16_f32 v25, v232, v233\n"
"v_cvt_pk_bf16_f32 v26, v234, v235\n"
"v_cvt_pk_bf16_f32 v27, v236, v237\n"
"v_accvgpr_read_b32 v238, a96\n"
"v_accvgpr_read_b32 v239, a97\n"
"v_accvgpr_read_b32 v240, a98\n"
"v_accvgpr_read_b32 v241, a99\n"
"v_accvgpr_read_b32 v242, a112\n"
"v_accvgpr_read_b32 v243, a113\n"
"v_accvgpr_read_b32 v244, a114\n"
"v_accvgpr_read_b32 v245, a115\n"
"v_pk_mul_f32 v[238:239], v[238:239], s[56:57]\n"
"v_pk_mul_f32 v[240:241], v[240:241], s[56:57]\n"
"v_pk_mul_f32 v[242:243], v[242:243], s[56:57]\n"
"v_pk_mul_f32 v[244:245], v[244:245], s[56:57]\n"
"v_cvt_pk_bf16_f32 v28, v238, v239\n"
"v_cvt_pk_bf16_f32 v29, v240, v241\n"
"v_cvt_pk_bf16_f32 v30, v242, v243\n"
"v_cvt_pk_bf16_f32 v31, v244, v245\n"
"v_accvgpr_read_b32 v230, a128\n"
"v_accvgpr_read_b32 v231, a129\n"
"v_accvgpr_read_b32 v232, a130\n"
"v_accvgpr_read_b32 v233, a131\n"
"v_accvgpr_read_b32 v234, a144\n"
"v_accvgpr_read_b32 v235, a145\n"
"v_accvgpr_read_b32 v236, a146\n"
"v_accvgpr_read_b32 v237, a147\n"
"v_pk_mul_f32 v[230:231], v[230:231], s[56:57]\n"
"v_pk_mul_f32 v[232:233], v[232:233], s[56:57]\n"
"v_pk_mul_f32 v[234:235], v[234:235], s[56:57]\n"
"v_pk_mul_f32 v[236:237], v[236:237], s[56:57]\n"
"v_cvt_pk_bf16_f32 v32, v230, v231\n"
"v_cvt_pk_bf16_f32 v33, v232, v233\n"
"v_cvt_pk_bf16_f32 v34, v234, v235\n"
"v_cvt_pk_bf16_f32 v35, v236, v237\n"
"v_accvgpr_read_b32 v238, a160\n"
"v_accvgpr_read_b32 v239, a161\n"
"v_accvgpr_read_b32 v240, a162\n"
"v_accvgpr_read_b32 v241, a163\n"
"v_accvgpr_read_b32 v242, a176\n"
"v_accvgpr_read_b32 v243, a177\n"
"v_accvgpr_read_b32 v244, a178\n"
"v_accvgpr_read_b32 v245, a179\n"
"v_pk_mul_f32 v[238:239], v[238:239], s[56:57]\n"
"v_pk_mul_f32 v[240:241], v[240:241], s[56:57]\n"
"v_pk_mul_f32 v[242:243], v[242:243], s[56:57]\n"
"v_pk_mul_f32 v[244:245], v[244:245], s[56:57]\n"
"v_cvt_pk_bf16_f32 v36, v238, v239\n"
"v_cvt_pk_bf16_f32 v37, v240, v241\n"
"v_cvt_pk_bf16_f32 v38, v242, v243\n"
"v_cvt_pk_bf16_f32 v39, v244, v245\n"
"v_accvgpr_read_b32 v230, a4\n"
"v_accvgpr_read_b32 v231, a5\n"
"v_accvgpr_read_b32 v232, a6\n"
"v_accvgpr_read_b32 v233, a7\n"
"v_accvgpr_read_b32 v234, a20\n"
"v_accvgpr_read_b32 v235, a21\n"
"v_accvgpr_read_b32 v236, a22\n"
"v_accvgpr_read_b32 v237, a23\n"
"v_pk_mul_f32 v[230:231], v[230:231], s[56:57]\n"
"v_pk_mul_f32 v[232:233], v[232:233], s[56:57]\n"
"v_pk_mul_f32 v[234:235], v[234:235], s[56:57]\n"
"v_pk_mul_f32 v[236:237], v[236:237], s[56:57]\n"
"v_cvt_pk_bf16_f32 v40, v230, v231\n"
"v_cvt_pk_bf16_f32 v41, v232, v233\n"
"v_cvt_pk_bf16_f32 v42, v234, v235\n"
"v_cvt_pk_bf16_f32 v43, v236, v237\n"
"v_accvgpr_read_b32 v238, a36\n"
"v_accvgpr_read_b32 v239, a37\n"
"v_accvgpr_read_b32 v240, a38\n"
"v_accvgpr_read_b32 v241, a39\n"
"v_accvgpr_read_b32 v242, a52\n"
"v_accvgpr_read_b32 v243, a53\n"
"v_accvgpr_read_b32 v244, a54\n"
"v_accvgpr_read_b32 v245, a55\n"
"v_pk_mul_f32 v[238:239], v[238:239], s[56:57]\n"
"v_pk_mul_f32 v[240:241], v[240:241], s[56:57]\n"
"v_pk_mul_f32 v[242:243], v[242:243], s[56:57]\n"
"v_pk_mul_f32 v[244:245], v[244:245], s[56:57]\n"
"v_cvt_pk_bf16_f32 v44, v238, v239\n"
"v_cvt_pk_bf16_f32 v45, v240, v241\n"
"v_cvt_pk_bf16_f32 v46, v242, v243\n"
"v_cvt_pk_bf16_f32 v47, v244, v245\n"
"v_accvgpr_read_b32 v230, a68\n"
"v_accvgpr_read_b32 v231, a69\n"
"v_accvgpr_read_b32 v232, a70\n"
"v_accvgpr_read_b32 v233, a71\n"
"v_accvgpr_read_b32 v234, a84\n"
"v_accvgpr_read_b32 v235, a85\n"
"v_accvgpr_read_b32 v236, a86\n"
"v_accvgpr_read_b32 v237, a87\n"
"v_pk_mul_f32 v[230:231], v[230:231], s[56:57]\n"
"v_pk_mul_f32 v[232:233], v[232:233], s[56:57]\n"
"v_pk_mul_f32 v[234:235], v[234:235], s[56:57]\n"
"v_pk_mul_f32 v[236:237], v[236:237], s[56:57]\n"
"v_cvt_pk_bf16_f32 v48, v230, v231\n"
"v_cvt_pk_bf16_f32 v49, v232, v233\n"
"v_cvt_pk_bf16_f32 v50, v234, v235\n"
"v_cvt_pk_bf16_f32 v51, v236, v237\n"
"v_accvgpr_read_b32 v238, a100\n"
"v_accvgpr_read_b32 v239, a101\n"
"v_accvgpr_read_b32 v240, a102\n"
"v_accvgpr_read_b32 v241, a103\n"
"v_accvgpr_read_b32 v242, a116\n"
"v_accvgpr_read_b32 v243, a117\n"
"v_accvgpr_read_b32 v244, a118\n"
"v_accvgpr_read_b32 v245, a119\n"
"v_pk_mul_f32 v[238:239], v[238:239], s[56:57]\n"
"v_pk_mul_f32 v[240:241], v[240:241], s[56:57]\n"
"v_pk_mul_f32 v[242:243], v[242:243], s[56:57]\n"
"v_pk_mul_f32 v[244:245], v[244:245], s[56:57]\n"
"v_cvt_pk_bf16_f32 v52, v238, v239\n"
"v_cvt_pk_bf16_f32 v53, v240, v241\n"
"v_cvt_pk_bf16_f32 v54, v242, v243\n"
"v_cvt_pk_bf16_f32 v55, v244, v245\n"
"v_accvgpr_read_b32 v230, a132\n"
"v_accvgpr_read_b32 v231, a133\n"
"v_accvgpr_read_b32 v232, a134\n"
"v_accvgpr_read_b32 v233, a135\n"
"v_accvgpr_read_b32 v234, a148\n"
"v_accvgpr_read_b32 v235, a149\n"
"v_accvgpr_read_b32 v236, a150\n"
"v_accvgpr_read_b32 v237, a151\n"
"v_pk_mul_f32 v[230:231], v[230:231], s[56:57]\n"
"v_pk_mul_f32 v[232:233], v[232:233], s[56:57]\n"
"v_pk_mul_f32 v[234:235], v[234:235], s[56:57]\n"
"v_pk_mul_f32 v[236:237], v[236:237], s[56:57]\n"
"v_cvt_pk_bf16_f32 v56, v230, v231\n"
"v_cvt_pk_bf16_f32 v57, v232, v233\n"
"v_cvt_pk_bf16_f32 v58, v234, v235\n"
"v_cvt_pk_bf16_f32 v59, v236, v237\n"
"v_accvgpr_read_b32 v238, a164\n"
"v_accvgpr_read_b32 v239, a165\n"
"v_accvgpr_read_b32 v240, a166\n"
"v_accvgpr_read_b32 v241, a167\n"
"v_accvgpr_read_b32 v242, a180\n"
"v_accvgpr_read_b32 v243, a181\n"
"v_accvgpr_read_b32 v244, a182\n"
"v_accvgpr_read_b32 v245, a183\n"
"v_pk_mul_f32 v[238:239], v[238:239], s[56:57]\n"
"v_pk_mul_f32 v[240:241], v[240:241], s[56:57]\n"
"v_pk_mul_f32 v[242:243], v[242:243], s[56:57]\n"
"v_pk_mul_f32 v[244:245], v[244:245], s[56:57]\n"
"v_cvt_pk_bf16_f32 v60, v238, v239\n"
"v_cvt_pk_bf16_f32 v61, v240, v241\n"
"v_cvt_pk_bf16_f32 v62, v242, v243\n"
"v_cvt_pk_bf16_f32 v63, v244, v245\n"
"v_accvgpr_read_b32 v230, a8\n"
"v_accvgpr_read_b32 v231, a9\n"
"v_accvgpr_read_b32 v232, a10\n"
"v_accvgpr_read_b32 v233, a11\n"
"v_accvgpr_read_b32 v234, a24\n"
"v_accvgpr_read_b32 v235, a25\n"
"v_accvgpr_read_b32 v236, a26\n"
"v_accvgpr_read_b32 v237, a27\n"
"v_pk_mul_f32 v[230:231], v[230:231], s[56:57]\n"
"v_pk_mul_f32 v[232:233], v[232:233], s[56:57]\n"
"v_pk_mul_f32 v[234:235], v[234:235], s[56:57]\n"
"v_pk_mul_f32 v[236:237], v[236:237], s[56:57]\n"
"v_cvt_pk_bf16_f32 v64, v230, v231\n"
"v_cvt_pk_bf16_f32 v65, v232, v233\n"
"v_cvt_pk_bf16_f32 v66, v234, v235\n"
"v_cvt_pk_bf16_f32 v67, v236, v237\n"
"v_accvgpr_read_b32 v238, a40\n"
"v_accvgpr_read_b32 v239, a41\n"
"v_accvgpr_read_b32 v240, a42\n"
"v_accvgpr_read_b32 v241, a43\n"
"v_accvgpr_read_b32 v242, a56\n"
"v_accvgpr_read_b32 v243, a57\n"
"v_accvgpr_read_b32 v244, a58\n"
"v_accvgpr_read_b32 v245, a59\n"
"v_pk_mul_f32 v[238:239], v[238:239], s[56:57]\n"
"v_pk_mul_f32 v[240:241], v[240:241], s[56:57]\n"
"v_pk_mul_f32 v[242:243], v[242:243], s[56:57]\n"
"v_pk_mul_f32 v[244:245], v[244:245], s[56:57]\n"
"v_cvt_pk_bf16_f32 v68, v238, v239\n"
"v_cvt_pk_bf16_f32 v69, v240, v241\n"
"v_cvt_pk_bf16_f32 v70, v242, v243\n"
"v_cvt_pk_bf16_f32 v71, v244, v245\n"
"v_accvgpr_read_b32 v230, a72\n"
"v_accvgpr_read_b32 v231, a73\n"
"v_accvgpr_read_b32 v232, a74\n"
"v_accvgpr_read_b32 v233, a75\n"
"v_accvgpr_read_b32 v234, a88\n"
"v_accvgpr_read_b32 v235, a89\n"
"v_accvgpr_read_b32 v236, a90\n"
"v_accvgpr_read_b32 v237, a91\n"
"v_pk_mul_f32 v[230:231], v[230:231], s[56:57]\n"
"v_pk_mul_f32 v[232:233], v[232:233], s[56:57]\n"
"v_pk_mul_f32 v[234:235], v[234:235], s[56:57]\n"
"v_pk_mul_f32 v[236:237], v[236:237], s[56:57]\n"
"v_cvt_pk_bf16_f32 v72, v230, v231\n"
"v_cvt_pk_bf16_f32 v73, v232, v233\n"
"v_cvt_pk_bf16_f32 v74, v234, v235\n"
"v_cvt_pk_bf16_f32 v75, v236, v237\n"
"v_accvgpr_read_b32 v238, a104\n"
"v_accvgpr_read_b32 v239, a105\n"
"v_accvgpr_read_b32 v240, a106\n"
"v_accvgpr_read_b32 v241, a107\n"
"v_accvgpr_read_b32 v242, a120\n"
"v_accvgpr_read_b32 v243, a121\n"
"v_accvgpr_read_b32 v244, a122\n"
"v_accvgpr_read_b32 v245, a123\n"
"v_pk_mul_f32 v[238:239], v[238:239], s[56:57]\n"
"v_pk_mul_f32 v[240:241], v[240:241], s[56:57]\n"
"v_pk_mul_f32 v[242:243], v[242:243], s[56:57]\n"
"v_pk_mul_f32 v[244:245], v[244:245], s[56:57]\n"
"v_cvt_pk_bf16_f32 v76, v238, v239\n"
"v_cvt_pk_bf16_f32 v77, v240, v241\n"
"v_cvt_pk_bf16_f32 v78, v242, v243\n"
"v_cvt_pk_bf16_f32 v79, v244, v245\n"
"v_accvgpr_read_b32 v230, a136\n"
"v_accvgpr_read_b32 v231, a137\n"
"v_accvgpr_read_b32 v232, a138\n"
"v_accvgpr_read_b32 v233, a139\n"
"v_accvgpr_read_b32 v234, a152\n"
"v_accvgpr_read_b32 v235, a153\n"
"v_accvgpr_read_b32 v236, a154\n"
"v_accvgpr_read_b32 v237, a155\n"
"v_pk_mul_f32 v[230:231], v[230:231], s[56:57]\n"
"v_pk_mul_f32 v[232:233], v[232:233], s[56:57]\n"
"v_pk_mul_f32 v[234:235], v[234:235], s[56:57]\n"
"v_pk_mul_f32 v[236:237], v[236:237], s[56:57]\n"
"v_cvt_pk_bf16_f32 v116, v230, v231\n"
"v_cvt_pk_bf16_f32 v117, v232, v233\n"
"v_cvt_pk_bf16_f32 v118, v234, v235\n"
"v_cvt_pk_bf16_f32 v119, v236, v237\n"
"v_accvgpr_read_b32 v238, a168\n"
"v_accvgpr_read_b32 v239, a169\n"
"v_accvgpr_read_b32 v240, a170\n"
"v_accvgpr_read_b32 v241, a171\n"
"v_accvgpr_read_b32 v242, a184\n"
"v_accvgpr_read_b32 v243, a185\n"
"v_accvgpr_read_b32 v244, a186\n"
"v_accvgpr_read_b32 v245, a187\n"
"v_pk_mul_f32 v[238:239], v[238:239], s[56:57]\n"
"v_pk_mul_f32 v[240:241], v[240:241], s[56:57]\n"
"v_pk_mul_f32 v[242:243], v[242:243], s[56:57]\n"
"v_pk_mul_f32 v[244:245], v[244:245], s[56:57]\n"
"v_cvt_pk_bf16_f32 v120, v238, v239\n"
"v_cvt_pk_bf16_f32 v121, v240, v241\n"
"v_cvt_pk_bf16_f32 v122, v242, v243\n"
"v_cvt_pk_bf16_f32 v123, v244, v245\n"
"v_accvgpr_read_b32 v230, a12\n"
"v_accvgpr_read_b32 v231, a13\n"
"v_accvgpr_read_b32 v232, a14\n"
"v_accvgpr_read_b32 v233, a15\n"
"v_accvgpr_read_b32 v234, a28\n"
"v_accvgpr_read_b32 v235, a29\n"
"v_accvgpr_read_b32 v236, a30\n"
"v_accvgpr_read_b32 v237, a31\n"
"v_pk_mul_f32 v[230:231], v[230:231], s[56:57]\n"
"v_pk_mul_f32 v[232:233], v[232:233], s[56:57]\n"
"v_pk_mul_f32 v[234:235], v[234:235], s[56:57]\n"
"v_pk_mul_f32 v[236:237], v[236:237], s[56:57]\n"
"v_cvt_pk_bf16_f32 v124, v230, v231\n"
"v_cvt_pk_bf16_f32 v125, v232, v233\n"
"v_cvt_pk_bf16_f32 v126, v234, v235\n"
"v_cvt_pk_bf16_f32 v127, v236, v237\n"
"v_accvgpr_read_b32 v238, a44\n"
"v_accvgpr_read_b32 v239, a45\n"
"v_accvgpr_read_b32 v240, a46\n"
"v_accvgpr_read_b32 v241, a47\n"
"v_accvgpr_read_b32 v242, a60\n"
"v_accvgpr_read_b32 v243, a61\n"
"v_accvgpr_read_b32 v244, a62\n"
"v_accvgpr_read_b32 v245, a63\n"
"v_pk_mul_f32 v[238:239], v[238:239], s[56:57]\n"
"v_pk_mul_f32 v[240:241], v[240:241], s[56:57]\n"
"v_pk_mul_f32 v[242:243], v[242:243], s[56:57]\n"
"v_pk_mul_f32 v[244:245], v[244:245], s[56:57]\n"
"v_cvt_pk_bf16_f32 v128, v238, v239\n"
"v_cvt_pk_bf16_f32 v129, v240, v241\n"
"v_cvt_pk_bf16_f32 v130, v242, v243\n"
"v_cvt_pk_bf16_f32 v131, v244, v245\n"
"v_accvgpr_read_b32 v230, a76\n"
"v_accvgpr_read_b32 v231, a77\n"
"v_accvgpr_read_b32 v232, a78\n"
"v_accvgpr_read_b32 v233, a79\n"
"v_accvgpr_read_b32 v234, a92\n"
"v_accvgpr_read_b32 v235, a93\n"
"v_accvgpr_read_b32 v236, a94\n"
"v_accvgpr_read_b32 v237, a95\n"
"v_pk_mul_f32 v[230:231], v[230:231], s[56:57]\n"
"v_pk_mul_f32 v[232:233], v[232:233], s[56:57]\n"
"v_pk_mul_f32 v[234:235], v[234:235], s[56:57]\n"
"v_pk_mul_f32 v[236:237], v[236:237], s[56:57]\n"
"v_cvt_pk_bf16_f32 v132, v230, v231\n"
"v_cvt_pk_bf16_f32 v133, v232, v233\n"
"v_cvt_pk_bf16_f32 v134, v234, v235\n"
"v_cvt_pk_bf16_f32 v135, v236, v237\n"
"v_accvgpr_read_b32 v238, a108\n"
"v_accvgpr_read_b32 v239, a109\n"
"v_accvgpr_read_b32 v240, a110\n"
"v_accvgpr_read_b32 v241, a111\n"
"v_accvgpr_read_b32 v242, a124\n"
"v_accvgpr_read_b32 v243, a125\n"
"v_accvgpr_read_b32 v244, a126\n"
"v_accvgpr_read_b32 v245, a127\n"
"v_pk_mul_f32 v[238:239], v[238:239], s[56:57]\n"
"v_pk_mul_f32 v[240:241], v[240:241], s[56:57]\n"
"v_pk_mul_f32 v[242:243], v[242:243], s[56:57]\n"
"v_pk_mul_f32 v[244:245], v[244:245], s[56:57]\n"
"v_cvt_pk_bf16_f32 v136, v238, v239\n"
"v_cvt_pk_bf16_f32 v137, v240, v241\n"
"v_cvt_pk_bf16_f32 v138, v242, v243\n"
"v_cvt_pk_bf16_f32 v139, v244, v245\n"
"v_accvgpr_read_b32 v230, a140\n"
"v_accvgpr_read_b32 v231, a141\n"
"v_accvgpr_read_b32 v232, a142\n"
"v_accvgpr_read_b32 v233, a143\n"
"v_accvgpr_read_b32 v234, a156\n"
"v_accvgpr_read_b32 v235, a157\n"
"v_accvgpr_read_b32 v236, a158\n"
"v_accvgpr_read_b32 v237, a159\n"
"v_pk_mul_f32 v[230:231], v[230:231], s[56:57]\n"
"v_pk_mul_f32 v[232:233], v[232:233], s[56:57]\n"
"v_pk_mul_f32 v[234:235], v[234:235], s[56:57]\n"
"v_pk_mul_f32 v[236:237], v[236:237], s[56:57]\n"
"v_cvt_pk_bf16_f32 v140, v230, v231\n"
"v_cvt_pk_bf16_f32 v141, v232, v233\n"
"v_cvt_pk_bf16_f32 v142, v234, v235\n"
"v_cvt_pk_bf16_f32 v143, v236, v237\n"
"v_accvgpr_read_b32 v238, a172\n"
"v_accvgpr_read_b32 v239, a173\n"
"v_accvgpr_read_b32 v240, a174\n"
"v_accvgpr_read_b32 v241, a175\n"
"v_accvgpr_read_b32 v242, a188\n"
"v_accvgpr_read_b32 v243, a189\n"
"v_accvgpr_read_b32 v244, a190\n"
"v_accvgpr_read_b32 v245, a191\n"
"v_pk_mul_f32 v[238:239], v[238:239], s[56:57]\n"
"v_pk_mul_f32 v[240:241], v[240:241], s[56:57]\n"
"v_pk_mul_f32 v[242:243], v[242:243], s[56:57]\n"
"v_pk_mul_f32 v[244:245], v[244:245], s[56:57]\n"
"v_cvt_pk_bf16_f32 v144, v238, v239\n"
"v_cvt_pk_bf16_f32 v145, v240, v241\n"
"v_cvt_pk_bf16_f32 v146, v242, v243\n"
"v_cvt_pk_bf16_f32 v147, v244, v245\n"
"s_mov_b32 exec_lo, 0xffff0000\n"
"s_mov_b32 exec_hi, 0xffff0000\n"
"v_swap_b32 v16, v18\n"
"v_swap_b32 v17, v19\n"
"v_swap_b32 v20, v22\n"
"v_swap_b32 v21, v23\n"
"v_swap_b32 v24, v26\n"
"v_swap_b32 v25, v27\n"
"v_swap_b32 v28, v30\n"
"v_swap_b32 v29, v31\n"
"v_swap_b32 v32, v34\n"
"v_swap_b32 v33, v35\n"
"v_swap_b32 v36, v38\n"
"v_swap_b32 v37, v39\n"
"v_swap_b32 v40, v42\n"
"v_swap_b32 v41, v43\n"
"v_swap_b32 v44, v46\n"
"v_swap_b32 v45, v47\n"
"v_swap_b32 v48, v50\n"
"v_swap_b32 v49, v51\n"
"v_swap_b32 v52, v54\n"
"v_swap_b32 v53, v55\n"
"v_swap_b32 v56, v58\n"
"v_swap_b32 v57, v59\n"
"v_swap_b32 v60, v62\n"
"v_swap_b32 v61, v63\n"
"v_swap_b32 v64, v66\n"
"v_swap_b32 v65, v67\n"
"v_swap_b32 v68, v70\n"
"v_swap_b32 v69, v71\n"
"v_swap_b32 v72, v74\n"
"v_swap_b32 v73, v75\n"
"v_swap_b32 v76, v78\n"
"v_swap_b32 v77, v79\n"
"v_swap_b32 v116, v118\n"
"v_swap_b32 v117, v119\n"
"v_swap_b32 v120, v122\n"
"v_swap_b32 v121, v123\n"
"v_swap_b32 v124, v126\n"
"v_swap_b32 v125, v127\n"
"v_swap_b32 v128, v130\n"
"v_swap_b32 v129, v131\n"
"v_swap_b32 v132, v134\n"
"v_swap_b32 v133, v135\n"
"v_swap_b32 v136, v138\n"
"v_swap_b32 v137, v139\n"
"v_swap_b32 v140, v142\n"
"v_swap_b32 v141, v143\n"
"v_swap_b32 v144, v146\n"
"v_swap_b32 v145, v147\n"
"s_mov_b64 exec, -1\n"
"v_accvgpr_write_b32 a192, v16\n"
"v_accvgpr_write_b32 a193, v17\n"
"v_accvgpr_write_b32 a194, v18\n"
"v_accvgpr_write_b32 a195, v19\n"
"v_accvgpr_write_b32 a196, v20\n"
"v_accvgpr_write_b32 a197, v21\n"
"v_accvgpr_write_b32 a198, v22\n"
"v_accvgpr_write_b32 a199, v23\n"
"v_accvgpr_write_b32 a200, v24\n"
"v_accvgpr_write_b32 a201, v25\n"
"v_accvgpr_write_b32 a202, v26\n"
"v_accvgpr_write_b32 a203, v27\n"
"v_accvgpr_write_b32 a204, v28\n"
"v_accvgpr_write_b32 a205, v29\n"
"v_accvgpr_write_b32 a206, v30\n"
"v_accvgpr_write_b32 a207, v31\n"
"v_accvgpr_write_b32 a208, v32\n"
"v_accvgpr_write_b32 a209, v33\n"
"v_accvgpr_write_b32 a210, v34\n"
"v_accvgpr_write_b32 a211, v35\n"
"v_accvgpr_write_b32 a212, v36\n"
"v_accvgpr_write_b32 a213, v37\n"
"v_accvgpr_write_b32 a214, v38\n"
"v_accvgpr_write_b32 a215, v39\n"
"v_accvgpr_write_b32 a216, v40\n"
"v_accvgpr_write_b32 a217, v41\n"
"v_accvgpr_write_b32 a218, v42\n"
"v_accvgpr_write_b32 a219, v43\n"
"v_accvgpr_write_b32 a220, v44\n"
"v_accvgpr_write_b32 a221, v45\n"
"v_accvgpr_write_b32 a222, v46\n"
"v_accvgpr_write_b32 a223, v47\n"
"v_accvgpr_write_b32 a224, v48\n"
"v_accvgpr_write_b32 a225, v49\n"
"v_accvgpr_write_b32 a226, v50\n"
"v_accvgpr_write_b32 a227, v51\n"
"v_accvgpr_write_b32 a228, v52\n"
"v_accvgpr_write_b32 a229, v53\n"
"v_accvgpr_write_b32 a230, v54\n"
"v_accvgpr_write_b32 a231, v55\n"
"v_accvgpr_write_b32 a232, v56\n"
"v_accvgpr_write_b32 a233, v57\n"
"v_accvgpr_write_b32 a234, v58\n"
"v_accvgpr_write_b32 a235, v59\n"
"v_accvgpr_write_b32 a236, v60\n"
"v_accvgpr_write_b32 a237, v61\n"
"v_accvgpr_write_b32 a238, v62\n"
"v_accvgpr_write_b32 a239, v63\n"
"v_accvgpr_write_b32 a240, v64\n"
"v_accvgpr_write_b32 a241, v65\n"
"v_accvgpr_write_b32 a242, v66\n"
"v_accvgpr_write_b32 a243, v67\n"
"v_accvgpr_write_b32 a244, v68\n"
"v_accvgpr_write_b32 a245, v69\n"
"v_accvgpr_write_b32 a246, v70\n"
"v_accvgpr_write_b32 a247, v71\n"
"v_accvgpr_write_b32 a248, v72\n"
"v_accvgpr_write_b32 a249, v73\n"
"v_accvgpr_write_b32 a250, v74\n"
"v_accvgpr_write_b32 a251, v75\n"
"v_accvgpr_write_b32 a252, v76\n"
"v_accvgpr_write_b32 a253, v77\n"
"v_accvgpr_write_b32 a254, v78\n"
"v_accvgpr_write_b32 a255, v79\n"
"s_mul_i32 s60, %[blk], 240\n"
"s_mul_i32 s61, s40, 60\n"
"s_add_u32 s60, s60, s61\n"
"s_lshl_b32 s60, s60, 7\n"
"v_min_u32 v5, 59, v4\n"
"v_lshlrev_b32 v5, 7, v5\n"
"v_add_u32 v5, s60, v5\n"
"global_load_dword v254, v5, %[wo]\n"
"s_mov_b32 s49, 0\n"
"s_mov_b32 s68, 49152\n"
"s_mov_b32 s69, 4294868992\n"
"s_mov_b32 s48, 49152\n"
"s_mul_i32 s42, s40, 6144\n"
"s_add_u32 s44, s88, 3\n"
"s_and_b32 s44, s44, 15\n"
"s_mul_i32 s44, s44, 24576\n"
"s_add_u32 s44, s44, s89\n"
"s_add_u32 s45, s88, 2\n"
"s_and_b32 s45, s45, 15\n"
"s_mul_i32 s45, s45, 24576\n"
"s_add_u32 s45, s45, s89\n"
"s_mov_b32 s90, 3\n"
"ds_read_b128 v[164:167], v196 offset:0\n"
"ds_read_b128 v[168:171], v197 offset:0\n"
"ds_read_b128 v[172:175], v196 offset:128\n"
"ds_read_b128 v[176:179], v197 offset:128\n"
"s_waitcnt lgkmcnt(2)\n"
"v_mfma_f32_16x16x32_bf16 v[16:19], v[164:167], a[192:195], 0\n"
"v_mfma_f32_16x16x32_bf16 v[16:19], v[168:171], a[196:199], v[16:19]\n"
"v_mfma_f32_16x16x32_bf16 v[20:23], v[164:167], a[216:219], 0\n"
"v_mfma_f32_16x16x32_bf16 v[20:23], v[168:171], a[220:223], v[20:23]\n"
"v_mfma_f32_16x16x32_bf16 v[24:27], v[164:167], a[240:243], 0\n"
"ds_read_b128 v[148:151], v196 offset:256\n"
"ds_read_b128 v[152:155], v197 offset:256\n"
"v_mfma_f32_16x16x32_bf16 v[24:27], v[168:171], a[244:247], v[24:27]\n"
"v_mfma_f32_16x16x32_bf16 v[28:31], v[164:167], v[124:127], 0\n"
"v_mfma_f32_16x16x32_bf16 v[28:31], v[168:171], v[128:131], v[28:31]\n"
"s_waitcnt lgkmcnt(2)\n"
"v_mfma_f32_16x16x32_bf16 v[16:19], v[172:175], a[200:203], v[16:19]\n"
"v_mfma_f32_16x16x32_bf16 v[16:19], v[176:179], a[204:207], v[16:19]\n"
"v_mfma_f32_16x16x32_bf16 v[20:23], v[172:175], a[224:227], v[20:23]\n"
"v_mfma_f32_16x16x32_bf16 v[20:23], v[176:179], a[228:231], v[20:23]\n"
"v_mfma_f32_16x16x32_bf16 v[24:27], v[172:175], a[248:251], v[24:27]\n"
"ds_read_b128 v[156:159], v196 offset:6144\n"
"ds_read_b128 v[160:163], v197 offset:6144\n"
"v_mfma_f32_16x16x32_bf16 v[24:27], v[176:179], a[252:255], v[24:27]\n"
"v_mfma_f32_16x16x32_bf16 v[28:31], v[172:175], v[132:135], v[28:31]\n"
"v_mfma_f32_16x16x32_bf16 v[28:31], v[176:179], v[136:139], v[28:31]\n"
"s_waitcnt lgkmcnt(2)\n"
"v_mfma_f32_16x16x32_bf16 v[16:19], v[148:151], a[208:211], v[16:19]\n"
"v_mfma_f32_16x16x32_bf16 v[16:19], v[152:155], a[212:215], v[16:19]\n"
"v_mfma_f32_16x16x32_bf16 v[20:23], v[148:151], a[232:235], v[20:23]\n"
"v_mfma_f32_16x16x32_bf16 v[20:23], v[152:155], a[236:239], v[20:23]\n"
"v_mfma_f32_16x16x32_bf16 v[24:27], v[148:151], v[116:119], v[24:27]\n"
"ds_read_b128 v[164:167], v196 offset:6272\n"
"ds_read_b128 v[168:171], v197 offset:6272\n"
"v_mfma_f32_16x16x32_bf16 v[24:27], v[152:155], v[120:123], v[24:27]\n"
"v_mfma_f32_16x16x32_bf16 v[28:31], v[148:151], v[140:143], v[28:31]\n"
"v_mfma_f32_16x16x32_bf16 v[28:31], v[152:155], v[144:147], v[28:31]\n"
"s_waitcnt lgkmcnt(2)\n"
"v_mfma_f32_16x16x32_bf16 v[32:35], v[156:159], a[192:195], 0\n"
"v_mfma_f32_16x16x32_bf16 v[32:35], v[160:163], a[196:199], v[32:35]\n"
"v_mfma_f32_16x16x32_bf16 v[36:39], v[156:159], a[216:219], 0\n"
"v_mfma_f32_16x16x32_bf16 v[36:39], v[160:163], a[220:223], v[36:39]\n"
"v_mfma_f32_16x16x32_bf16 v[40:43], v[156:159], a[240:243], 0\n"
"ds_read_b128 v[172:175], v196 offset:6400\n"
"ds_read_b128 v[176:179], v197 offset:6400\n"
"v_mfma_f32_16x16x32_bf16 v[40:43], v[160:163], a[244:247], v[40:43]\n"
"v_mfma_f32_16x16x32_bf16 v[44:47], v[156:159], v[124:127], 0\n"
"v_mfma_f32_16x16x32_bf16 v[44:47], v[160:163], v[128:131], v[44:47]\n"
"s_waitcnt lgkmcnt(2)\n"
"v_mfma_f32_16x16x32_bf16 v[32:35], v[164:167], a[200:203], v[32:35]\n"
"v_mfma_f32_16x16x32_bf16 v[32:35], v[168:171], a[204:207], v[32:35]\n"
"v_mfma_f32_16x16x32_bf16 v[36:39], v[164:167], a[224:227], v[36:39]\n"
"v_mfma_f32_16x16x32_bf16 v[36:39], v[168:171], a[228:231], v[36:39]\n"
"v_mfma_f32_16x16x32_bf16 v[40:43], v[164:167], a[248:251], v[40:43]\n"
"v_mfma_f32_16x16x32_bf16 v[40:43], v[168:171], a[252:255], v[40:43]\n"
"v_mfma_f32_16x16x32_bf16 v[44:47], v[164:167], v[132:135], v[44:47]\n"
"v_mfma_f32_16x16x32_bf16 v[44:47], v[168:171], v[136:139], v[44:47]\n"
"s_waitcnt lgkmcnt(0)\n"
"v_mfma_f32_16x16x32_bf16 v[32:35], v[172:175], a[208:211], v[32:35]\n"
"v_mfma_f32_16x16x32_bf16 v[32:35], v[176:179], a[212:215], v[32:35]\n"
"v_mfma_f32_16x16x32_bf16 v[36:39], v[172:175], a[232:235], v[36:39]\n"
"v_mfma_f32_16x16x32_bf16 v[36:39], v[176:179], a[236:239], v[36:39]\n"
"v_mfma_f32_16x16x32_bf16 v[40:43], v[172:175], v[116:119], v[40:43]\n"
"v_mfma_f32_16x16x32_bf16 v[40:43], v[176:179], v[120:123], v[40:43]\n"
"v_mfma_f32_16x16x32_bf16 v[44:47], v[172:175], v[140:143], v[44:47]\n"
"v_mfma_f32_16x16x32_bf16 v[44:47], v[176:179], v[144:147], v[44:47]\n"
"ds_read_b128 v[164:167], v196 offset:12288\n"
"ds_read_b128 v[168:171], v197 offset:12288\n"
"ds_read_b128 v[172:175], v196 offset:12416\n"
"ds_read_b128 v[176:179], v197 offset:12416\n"
"s_nop 7\n"
"v_max3_f32 v230, v16, v17, v18\n"
"v_max3_f32 v230, v230, v19, v32\n"
"v_max3_f32 v230, v230, v33, v34\n"
"v_max_f32 v230, v230, v35\n"
"v_max3_f32 v231, v20, v21, v22\n"
"v_max3_f32 v231, v231, v23, v36\n"
"v_max3_f32 v231, v231, v37, v38\n"
"v_max_f32 v231, v231, v39\n"
"v_max3_f32 v232, v24, v25, v26\n"
"v_max3_f32 v232, v232, v27, v40\n"
"v_max3_f32 v232, v232, v41, v42\n"
"v_max_f32 v232, v232, v43\n"
"v_max3_f32 v233, v28, v29, v30\n"
"v_max3_f32 v233, v233, v31, v44\n"
"v_max3_f32 v233, v233, v45, v46\n"
"v_max_f32 v233, v233, v47\n"
"ds_swizzle_b32 v234, v230 offset:0x401F\n"
"ds_swizzle_b32 v235, v231 offset:0x401F\n"
"ds_swizzle_b32 v236, v232 offset:0x401F\n"
"ds_swizzle_b32 v237, v233 offset:0x401F\n"
"s_waitcnt lgkmcnt(0)\n"
"v_max_f32 v230, v230, v234\n"
"v_mov_b32 v234, v230\n"
"v_max_f32 v231, v231, v235\n"
"v_mov_b32 v235, v231\n"
"v_max_f32 v232, v232, v236\n"
"v_mov_b32 v236, v232\n"
"v_max_f32 v233, v233, v237\n"
"v_mov_b32 v237, v233\n"
"s_nop 1\n"
"v_permlane32_swap_b32 v230, v234\n"
"v_permlane32_swap_b32 v231, v235\n"
"v_permlane32_swap_b32 v232, v236\n"
"v_permlane32_swap_b32 v233, v237\n"
"v_max_f32 v230, v230, v234\n"
"v_max_f32 v231, v231, v235\n"
"v_max_f32 v232, v232, v236\n"
"v_max_f32 v233, v233, v237\n"
"v_sub_f32 v80, 0, v230\n"
"v_sub_f32 v81, 0, v230\n"
"v_sub_f32 v82, 0, v230\n"
"v_sub_f32 v83, 0, v230\n"
"v_sub_f32 v84, 0, v231\n"
"v_sub_f32 v85, 0, v231\n"
"v_sub_f32 v86, 0, v231\n"
"v_sub_f32 v87, 0, v231\n"
"v_sub_f32 v88, 0, v232\n"
"v_sub_f32 v89, 0, v232\n"
"v_sub_f32 v90, 0, v232\n"
"v_sub_f32 v91, 0, v232\n"
"v_sub_f32 v92, 0, v233\n"
"v_sub_f32 v93, 0, v233\n"
"v_sub_f32 v94, 0, v233\n"
"v_sub_f32 v95, 0, v233\n"
"v_sub_f32 v16, v16, v230\n"
"v_sub_f32 v17, v17, v230\n"
"v_sub_f32 v18, v18, v230\n"
"v_sub_f32 v19, v19, v230\n"
"v_sub_f32 v32, v32, v230\n"
"v_sub_f32 v33, v33, v230\n"
"v_sub_f32 v34, v34, v230\n"
"v_sub_f32 v35, v35, v230\n"
"v_sub_f32 v20, v20, v231\n"
"v_sub_f32 v21, v21, v231\n"
"v_sub_f32 v22, v22, v231\n"
"v_sub_f32 v23, v23, v231\n"
"v_sub_f32 v36, v36, v231\n"
"v_sub_f32 v37, v37, v231\n"
"v_sub_f32 v38, v38, v231\n"
"v_sub_f32 v39, v39, v231\n"
"v_sub_f32 v24, v24, v232\n"
"v_sub_f32 v25, v25, v232\n"
"v_sub_f32 v26, v26, v232\n"
"v_sub_f32 v27, v27, v232\n"
"v_sub_f32 v40, v40, v232\n"
"v_sub_f32 v41, v41, v232\n"
"v_sub_f32 v42, v42, v232\n"
"v_sub_f32 v43, v43, v232\n"
"v_sub_f32 v28, v28, v233\n"
"v_sub_f32 v29, v29, v233\n"
"v_sub_f32 v30, v30, v233\n"
"v_sub_f32 v31, v31, v233\n"
"v_sub_f32 v44, v44, v233\n"
"v_sub_f32 v45, v45, v233\n"
"v_sub_f32 v46, v46, v233\n"
"v_sub_f32 v47, v47, v233\n"
"s_add_u32 s43, s89, 24576\n"
"s_mov_b32 m0, s43\n"
"s_nop 0\n"
"buffer_load_dwordx4 v202, s[24:27], s45 offen offset:0 nt lds\n"
"v_exp_f32_e32 v16, v16\n"
"v_exp_f32_e32 v17, v17\n"
"v_exp_f32_e32 v18, v18\n"
"v_exp_f32_e32 v19, v19\n"
"v_exp_f32_e32 v32, v32\n"
"buffer_load_dwordx4 v203, s[24:27], s45 offen offset:1024 nt lds\n"
"v_exp_f32_e32 v33, v33\n"
"v_exp_f32_e32 v34, v34\n"
"v_exp_f32_e32 v35, v35\n"
"v_exp_f32_e32 v20, v20\n"
"v_exp_f32_e32 v21, v21\n"
"buffer_load_dwordx4 v204, s[24:27], s45 offen offset:2048 nt lds\n"
"v_exp_f32_e32 v22, v22\n"
"v_exp_f32_e32 v23, v23\n"
"v_exp_f32_e32 v36, v36\n"
"v_exp_f32_e32 v37, v37\n"
"v_exp_f32_e32 v38, v38\n"
"buffer_load_dwordx4 v205, s[24:27], s45 offen offset:3072 nt lds\n"
"v_exp_f32_e32 v39, v39\n"
"v_exp_f32_e32 v24, v24\n"
"v_exp_f32_e32 v25, v25\n"
"v_exp_f32_e32 v26, v26\n"
"v_exp_f32_e32 v27, v27\n"
"s_add_u32 m0, s43, 4096\n"
"s_nop 0\n"
"buffer_load_dwordx4 v206, s[24:27], s45 offen offset:0 nt lds\n"
"v_exp_f32_e32 v40, v40\n"
"v_exp_f32_e32 v41, v41\n"
"v_exp_f32_e32 v42, v42\n"
"v_exp_f32_e32 v43, v43\n"
"v_exp_f32_e32 v28, v28\n"
"buffer_load_dwordx4 v207, s[24:27], s45 offen offset:1024 nt lds\n"
"v_exp_f32_e32 v29, v29\n"
"v_exp_f32_e32 v30, v30\n"
"v_exp_f32_e32 v31, v31\n"
"v_exp_f32_e32 v44, v44\n"
"v_exp_f32_e32 v45, v45\n"
"v_exp_f32_e32 v46, v46\n"
"v_exp_f32_e32 v47, v47\n"
"s_nop 0\n"
"v_cvt_pk_bf16_f32 v48, v16, v17\n"
"v_cvt_pk_bf16_f32 v49, v18, v19\n"
"v_cvt_pk_bf16_f32 v50, v32, v33\n"
"v_cvt_pk_bf16_f32 v51, v34, v35\n"
"v_cvt_pk_bf16_f32 v52, v20, v21\n"
"v_cvt_pk_bf16_f32 v53, v22, v23\n"
"v_cvt_pk_bf16_f32 v54, v36, v37\n"
"v_cvt_pk_bf16_f32 v55, v38, v39\n"
"v_cvt_pk_bf16_f32 v56, v24, v25\n"
"v_cvt_pk_bf16_f32 v57, v26, v27\n"
"v_cvt_pk_bf16_f32 v58, v40, v41\n"
"v_cvt_pk_bf16_f32 v59, v42, v43\n"
"v_cvt_pk_bf16_f32 v60, v28, v29\n"
"v_cvt_pk_bf16_f32 v61, v30, v31\n"
"v_cvt_pk_bf16_f32 v62, v44, v45\n"
"v_cvt_pk_bf16_f32 v63, v46, v47\n"
"s_nop 1\n"
"v_mfma_f32_16x16x32_bf16 v[16:19], v[164:167], a[192:195], v[80:83]\n"
"ds_read_b128 v[148:151], v196 offset:12544\n"
"v_mfma_f32_16x16x32_bf16 v[16:19], v[168:171], a[196:199], v[16:19]\n"
"ds_read_b128 v[152:155], v197 offset:12544\n"
"v_mfma_f32_16x16x32_bf16 v[20:23], v[164:167], a[216:219], v[84:87]\n"
"ds_read_b128 v[156:159], v196 offset:18432\n"
"v_mfma_f32_16x16x32_bf16 v[20:23], v[168:171], a[220:223], v[20:23]\n"
"ds_read_b128 v[160:163], v197 offset:18432\n"
"v_mfma_f32_16x16x32_bf16 v[24:27], v[164:167], a[240:243], v[88:91]\n"
"v_mfma_f32_16x16x32_bf16 v[24:27], v[168:171], a[244:247], v[24:27]\n"
"v_mfma_f32_16x16x32_bf16 v[28:31], v[164:167], v[124:127], v[92:95]\n"
"v_mfma_f32_16x16x32_bf16 v[28:31], v[168:171], v[128:131], v[28:31]\n"
"v_mfma_f32_16x16x32_bf16 v[16:19], v[172:175], a[200:203], v[16:19]\n"
"ds_read_b128 v[164:167], v196 offset:18560\n"
"v_mfma_f32_16x16x32_bf16 v[16:19], v[176:179], a[204:207], v[16:19]\n"
"ds_read_b128 v[168:171], v197 offset:18560\n"
"v_mfma_f32_16x16x32_bf16 v[20:23], v[172:175], a[224:227], v[20:23]\n"
"v_mfma_f32_16x16x32_bf16 v[20:23], v[176:179], a[228:231], v[20:23]\n"
"v_mfma_f32_16x16x32_bf16 v[24:27], v[172:175], a[248:251], v[24:27]\n"
"v_mfma_f32_16x16x32_bf16 v[24:27], v[176:179], a[252:255], v[24:27]\n"
"v_mfma_f32_16x16x32_bf16 v[28:31], v[172:175], v[132:135], v[28:31]\n"
"v_mfma_f32_16x16x32_bf16 v[28:31], v[176:179], v[136:139], v[28:31]\n"
"s_waitcnt lgkmcnt(4)\n"
"v_mfma_f32_16x16x32_bf16 v[16:19], v[148:151], a[208:211], v[16:19]\n"
"ds_read_b128 v[172:175], v196 offset:18688\n"
"v_mfma_f32_16x16x32_bf16 v[16:19], v[152:155], a[212:215], v[16:19]\n"
"ds_read_b128 v[176:179], v197 offset:18688\n"
"v_mfma_f32_16x16x32_bf16 v[20:23], v[148:151], a[232:235], v[20:23]\n"
"v_mfma_f32_16x16x32_bf16 v[20:23], v[152:155], a[236:239], v[20:23]\n"
"v_add_u32 v196, s48, v196\n"
"v_add_u32 v197, s48, v197\n"
"v_mfma_f32_16x16x32_bf16 v[24:27], v[148:151], v[116:119], v[24:27]\n"
"v_add_u32 v198, s48, v198\n"
"v_add_u32 v199, s48, v199\n"
"v_mfma_f32_16x16x32_bf16 v[24:27], v[152:155], v[120:123], v[24:27]\n"
"v_add_u32 v200, s48, v200\n"
"v_add_u32 v201, s48, v201\n"
"v_mfma_f32_16x16x32_bf16 v[28:31], v[148:151], v[140:143], v[28:31]\n"
"v_mfma_f32_16x16x32_bf16 v[28:31], v[152:155], v[144:147], v[28:31]\n"
"s_waitcnt lgkmcnt(4)\n"
"v_mfma_f32_16x16x32_bf16 v[32:35], v[156:159], a[192:195], v[80:83]\n"
"v_mfma_f32_16x16x32_bf16 v[32:35], v[160:163], a[196:199], v[32:35]\n"
"v_mfma_f32_16x16x32_bf16 v[36:39], v[156:159], a[216:219], v[84:87]\n"
"v_mfma_f32_16x16x32_bf16 v[36:39], v[160:163], a[220:223], v[36:39]\n"
"s_waitcnt vmcnt(12) lgkmcnt(0)\n"
"s_barrier\n"
"v_mfma_f32_16x16x32_bf16 v[40:43], v[156:159], a[240:243], v[88:91]\n"
"s_add_u32 s43, s42, 24576\n"
"v_mfma_f32_16x16x32_bf16 v[40:43], v[160:163], a[244:247], v[40:43]\n"
"v_mfma_f32_16x16x32_bf16 v[44:47], v[156:159], v[124:127], v[92:95]\n"
"v_exp_f32_e32 v16, v16\n"
"v_mfma_f32_16x16x32_bf16 v[44:47], v[160:163], v[128:131], v[44:47]\n"
"ds_read_b64_tr_b16 v[148:149], v198 offset:24576\n"
"ds_read_b64_tr_b16 v[150:151], v198 offset:30720\n"
"v_mfma_f32_16x16x32_bf16 v[32:35], v[164:167], a[200:203], v[32:35]\n"
"s_mov_b32 m0, s42\n"
"v_exp_f32_e32 v17, v17\n"
"v_mfma_f32_16x16x32_bf16 v[32:35], v[168:171], a[204:207], v[32:35]\n"
"buffer_load_dwordx4 v202, s[20:23], s44 offen offset:0 nt lds\n"
"v_cvt_pk_bf16_f32 v64, v16, v17\n"
"v_mfma_f32_16x16x32_bf16 v[36:39], v[164:167], a[224:227], v[36:39]\n"
"v_mfma_f32_16x16x32_bf16 v[36:39], v[168:171], a[228:231], v[36:39]\n"
"ds_read_b64_tr_b16 v[152:153], v199 offset:24576\n"
"ds_read_b64_tr_b16 v[154:155], v199 offset:30720\n"
"v_mfma_f32_16x16x32_bf16 v[40:43], v[164:167], a[248:251], v[40:43]\n"
"v_exp_f32_e32 v18, v18\n"
"v_mfma_f32_16x16x32_bf16 v[40:43], v[168:171], a[252:255], v[40:43]\n"
"v_exp_f32_e32 v19, v19\n"
"v_mfma_f32_16x16x32_bf16 v[44:47], v[164:167], v[132:135], v[44:47]\n"
"v_cvt_pk_bf16_f32 v65, v18, v19\n"
"v_mfma_f32_16x16x32_bf16 v[44:47], v[168:171], v[136:139], v[44:47]\n"
"ds_read_b64_tr_b16 v[156:157], v200 offset:24576\n"
"ds_read_b64_tr_b16 v[158:159], v200 offset:30720\n"
"v_mfma_f32_16x16x32_bf16 v[32:35], v[172:175], a[208:211], v[32:35]\n"
"buffer_load_dwordx4 v203, s[20:23], s44 offen offset:1024 nt lds\n"
"v_mfma_f32_16x16x32_bf16 v[32:35], v[176:179], a[212:215], v[32:35]\n"
"v_exp_f32_e32 v20, v20\n"
"v_mfma_f32_16x16x32_bf16 v[36:39], v[172:175], a[232:235], v[36:39]\n"
"v_exp_f32_e32 v21, v21\n"
"v_mfma_f32_16x16x32_bf16 v[36:39], v[176:179], a[236:239], v[36:39]\n"
"v_cvt_pk_bf16_f32 v68, v20, v21\n"
"v_mfma_f32_16x16x32_bf16 v[40:43], v[172:175], v[116:119], v[40:43]\n"
"v_mfma_f32_16x16x32_bf16 v[40:43], v[176:179], v[120:123], v[40:43]\n"
"v_exp_f32_e32 v22, v22\n"
"v_mfma_f32_16x16x32_bf16 v[44:47], v[172:175], v[140:143], v[44:47]\n"
"v_mfma_f32_16x16x32_bf16 v[44:47], v[176:179], v[144:147], v[44:47]\n"
"s_waitcnt lgkmcnt(2)\n"
"v_exp_f32_e32 v23, v23\n"
"v_mfma_f32_16x16x32_bf16 a[0:3], v[148:151], v[48:51], 0\n"
"ds_read_b64_tr_b16 v[160:161], v201 offset:24576\n"
"ds_read_b64_tr_b16 v[162:163], v201 offset:30720\n"
"v_mfma_f32_16x16x32_bf16 a[4:7], v[148:151], v[52:55], 0\n"
"buffer_load_dwordx4 v204, s[20:23], s44 offen offset:2048 nt lds\n"
"v_cvt_pk_bf16_f32 v69, v22, v23\n"
"v_mfma_f32_16x16x32_bf16 a[8:11], v[148:151], v[56:59], 0\n"
"v_exp_f32_e32 v24, v24\n"
"v_mfma_f32_16x16x32_bf16 a[12:15], v[148:151], v[60:63], 0\n"
"v_mfma_f32_16x16x32_bf16 a[16:19], v[152:155], v[48:51], 0\n"
"ds_read_b64_tr_b16 v[164:165], v198 offset:24704\n"
"ds_read_b64_tr_b16 v[166:167], v198 offset:30848\n"
"v_mfma_f32_16x16x32_bf16 a[20:23], v[152:155], v[52:55], 0\n"
"v_exp_f32_e32 v25, v25\n"
"v_mfma_f32_16x16x32_bf16 a[24:27], v[152:155], v[56:59], 0\n"
"v_cvt_pk_bf16_f32 v72, v24, v25\n"
"v_mfma_f32_16x16x32_bf16 a[28:31], v[152:155], v[60:63], 0\n"
"s_waitcnt lgkmcnt(2)\n"
"v_exp_f32_e32 v26, v26\n"
"v_mfma_f32_16x16x32_bf16 a[32:35], v[156:159], v[48:51], 0\n"
"ds_read_b64_tr_b16 v[168:169], v199 offset:24704\n"
"ds_read_b64_tr_b16 v[170:171], v199 offset:30848\n"
"v_mfma_f32_16x16x32_bf16 a[36:39], v[156:159], v[52:55], 0\n"
"v_exp_f32_e32 v27, v27\n"
"v_mfma_f32_16x16x32_bf16 a[40:43], v[156:159], v[56:59], 0\n"
"buffer_load_dwordx4 v205, s[20:23], s44 offen offset:3072 nt lds\n"
"v_cvt_pk_bf16_f32 v73, v26, v27\n"
"v_mfma_f32_16x16x32_bf16 a[44:47], v[156:159], v[60:63], 0\n"
"v_mfma_f32_16x16x32_bf16 a[48:51], v[160:163], v[48:51], 0\n"
"ds_read_b64_tr_b16 v[172:173], v200 offset:24704\n"
"ds_read_b64_tr_b16 v[174:175], v200 offset:30848\n"
"v_mfma_f32_16x16x32_bf16 a[52:55], v[160:163], v[52:55], 0\n"
"v_exp_f32_e32 v28, v28\n"
"v_mfma_f32_16x16x32_bf16 a[56:59], v[160:163], v[56:59], 0\n"
"v_exp_f32_e32 v29, v29\n"
"v_mfma_f32_16x16x32_bf16 a[60:63], v[160:163], v[60:63], 0\n"
"s_waitcnt lgkmcnt(2)\n"
"v_cvt_pk_bf16_f32 v76, v28, v29\n"
"v_mfma_f32_16x16x32_bf16 a[64:67], v[164:167], v[48:51], 0\n"
"ds_read_b64_tr_b16 v[176:177], v201 offset:24704\n"
"ds_read_b64_tr_b16 v[178:179], v201 offset:30848\n"
"v_mfma_f32_16x16x32_bf16 a[68:71], v[164:167], v[52:55], 0\n"
"v_exp_f32_e32 v30, v30\n"
"v_mfma_f32_16x16x32_bf16 a[72:75], v[164:167], v[56:59], 0\n"
"s_add_u32 m0, s42, 4096\n"
"v_mfma_f32_16x16x32_bf16 a[76:79], v[164:167], v[60:63], 0\n"
"buffer_load_dwordx4 v206, s[20:23], s44 offen offset:0 nt lds\n"
"v_mfma_f32_16x16x32_bf16 a[80:83], v[168:171], v[48:51], 0\n"
"ds_read_b64_tr_b16 v[148:149], v198 offset:24832\n"
"ds_read_b64_tr_b16 v[150:151], v198 offset:30976\n"
"v_mfma_f32_16x16x32_bf16 a[84:87], v[168:171], v[52:55], 0\n"
"v_exp_f32_e32 v31, v31\n"
"v_mfma_f32_16x16x32_bf16 a[88:91], v[168:171], v[56:59], 0\n"
"v_exp_f32_e32 v32, v32\n"
"v_mfma_f32_16x16x32_bf16 a[92:95], v[168:171], v[60:63], 0\n"
"s_waitcnt lgkmcnt(2)\n"
"v_cvt_pk_bf16_f32 v77, v30, v31\n"
"v_mfma_f32_16x16x32_bf16 a[96:99], v[172:175], v[48:51], 0\n"
"ds_read_b64_tr_b16 v[152:153], v199 offset:24832\n"
"ds_read_b64_tr_b16 v[154:155], v199 offset:30976\n"
"v_mfma_f32_16x16x32_bf16 a[100:103], v[172:175], v[52:55], 0\n"
"v_exp_f32_e32 v33, v33\n"
"v_mfma_f32_16x16x32_bf16 a[104:107], v[172:175], v[56:59], 0\n"
"v_cvt_pk_bf16_f32 v66, v32, v33\n"
"v_mfma_f32_16x16x32_bf16 a[108:111], v[172:175], v[60:63], 0\n"
"v_exp_f32_e32 v34, v34\n"
"v_mfma_f32_16x16x32_bf16 a[112:115], v[176:179], v[48:51], 0\n"
"ds_read_b64_tr_b16 v[156:157], v200 offset:24832\n"
"ds_read_b64_tr_b16 v[158:159], v200 offset:30976\n"
"v_mfma_f32_16x16x32_bf16 a[116:119], v[176:179], v[52:55], 0\n"
"buffer_load_dwordx4 v207, s[20:23], s44 offen offset:1024 nt lds\n"
"v_mfma_f32_16x16x32_bf16 a[120:123], v[176:179], v[56:59], 0\n"
"v_exp_f32_e32 v35, v35\n"
"v_mfma_f32_16x16x32_bf16 a[124:127], v[176:179], v[60:63], 0\n"
"s_waitcnt lgkmcnt(2)\n"
"v_cvt_pk_bf16_f32 v67, v34, v35\n"
"v_mfma_f32_16x16x32_bf16 a[128:131], v[148:151], v[48:51], 0\n"
"ds_read_b64_tr_b16 v[160:161], v201 offset:24832\n"
"ds_read_b64_tr_b16 v[162:163], v201 offset:30976\n"
"v_mfma_f32_16x16x32_bf16 a[132:135], v[148:151], v[52:55], 0\n"
"v_exp_f32_e32 v36, v36\n"
"v_mfma_f32_16x16x32_bf16 a[136:139], v[148:151], v[56:59], 0\n"
"v_mfma_f32_16x16x32_bf16 a[140:143], v[148:151], v[60:63], 0\n"
"v_exp_f32_e32 v37, v37\n"
"v_mfma_f32_16x16x32_bf16 a[144:147], v[152:155], v[48:51], 0\n"
"s_mov_b32 m0, s43\n"
"v_cvt_pk_bf16_f32 v70, v36, v37\n"
"v_mfma_f32_16x16x32_bf16 a[148:151], v[152:155], v[52:55], 0\n"
"v_mfma_f32_16x16x32_bf16 a[152:155], v[152:155], v[56:59], 0\n"
"v_exp_f32_e32 v38, v38\n"
"v_mfma_f32_16x16x32_bf16 a[156:159], v[152:155], v[60:63], 0\n"
"s_waitcnt lgkmcnt(0)\n"
"v_mfma_f32_16x16x32_bf16 a[160:163], v[156:159], v[48:51], 0\n"
"ds_read_b128 v[164:167], v196 offset:0\n"
"ds_read_b128 v[168:171], v197 offset:0\n"
"v_mfma_f32_16x16x32_bf16 a[164:167], v[156:159], v[52:55], 0\n"
"v_exp_f32_e32 v39, v39\n"
"v_mfma_f32_16x16x32_bf16 a[168:171], v[156:159], v[56:59], 0\n"
"v_cvt_pk_bf16_f32 v71, v38, v39\n"
"v_mfma_f32_16x16x32_bf16 a[172:175], v[156:159], v[60:63], 0\n"
"v_exp_f32_e32 v40, v40\n"
"v_mfma_f32_16x16x32_bf16 a[176:179], v[160:163], v[48:51], 0\n"
"v_mfma_f32_16x16x32_bf16 a[180:183], v[160:163], v[52:55], 0\n"
"v_mfma_f32_16x16x32_bf16 a[184:187], v[160:163], v[56:59], 0\n"
"v_mfma_f32_16x16x32_bf16 a[188:191], v[160:163], v[60:63], 0\n"
"v_exp_f32_e32 v41, v41\n"
"v_mfma_f32_16x16x32_bf16 v[96:99], v[112:115], v[48:51], 0\n"
"ds_read_b128 v[172:175], v196 offset:128\n"
"ds_read_b128 v[176:179], v197 offset:128\n"
"v_mfma_f32_16x16x32_bf16 v[100:103], v[112:115], v[52:55], 0\n"
"v_exp_f32_e32 v42, v42\n"
"v_mfma_f32_16x16x32_bf16 v[104:107], v[112:115], v[56:59], 0\n"
"v_cvt_pk_bf16_f32 v74, v40, v41\n"
"v_mfma_f32_16x16x32_bf16 v[108:111], v[112:115], v[60:63], 0\n"
"s_waitcnt lgkmcnt(2)\n"
"v_exp_f32_e32 v43, v43\n"
"v_mfma_f32_16x16x32_bf16 v[16:19], v[164:167], a[192:195], v[80:83]\n"
"v_cvt_pk_bf16_f32 v75, v42, v43\n"
"v_mfma_f32_16x16x32_bf16 v[16:19], v[168:171], a[196:199], v[16:19]\n"
"v_mfma_f32_16x16x32_bf16 v[20:23], v[164:167], a[216:219], v[84:87]\n"
"v_exp_f32_e32 v44, v44\n"
"v_mfma_f32_16x16x32_bf16 v[20:23], v[168:171], a[220:223], v[20:23]\n"
"v_mfma_f32_16x16x32_bf16 v[24:27], v[164:167], a[240:243], v[88:91]\n"
"ds_read_b128 v[148:151], v196 offset:256\n"
"ds_read_b128 v[152:155], v197 offset:256\n"
"v_mfma_f32_16x16x32_bf16 v[24:27], v[168:171], a[244:247], v[24:27]\n"
"v_exp_f32_e32 v45, v45\n"
"v_mfma_f32_16x16x32_bf16 v[28:31], v[164:167], v[124:127], v[92:95]\n"
"v_cvt_pk_bf16_f32 v78, v44, v45\n"
"v_mfma_f32_16x16x32_bf16 v[28:31], v[168:171], v[128:131], v[28:31]\n"
"s_waitcnt lgkmcnt(2)\n"
"v_exp_f32_e32 v46, v46\n"
"v_mfma_f32_16x16x32_bf16 v[16:19], v[172:175], a[200:203], v[16:19]\n"
"v_mfma_f32_16x16x32_bf16 v[16:19], v[176:179], a[204:207], v[16:19]\n"
"v_mfma_f32_16x16x32_bf16 v[20:23], v[172:175], a[224:227], v[20:23]\n"
"v_exp_f32_e32 v47, v47\n"
"v_mfma_f32_16x16x32_bf16 v[20:23], v[176:179], a[228:231], v[20:23]\n"
"v_cvt_pk_bf16_f32 v79, v46, v47\n"
"v_mfma_f32_16x16x32_bf16 v[24:27], v[172:175], a[248:251], v[24:27]\n"
"ds_read_b128 v[156:159], v196 offset:6144\n"
"ds_read_b128 v[160:163], v197 offset:6144\n"
"v_mfma_f32_16x16x32_bf16 v[24:27], v[176:179], a[252:255], v[24:27]\n"
"v_mfma_f32_16x16x32_bf16 v[28:31], v[172:175], v[132:135], v[28:31]\n"
"v_mfma_f32_16x16x32_bf16 v[28:31], v[176:179], v[136:139], v[28:31]\n"
"s_waitcnt lgkmcnt(2)\n"
"v_mfma_f32_16x16x32_bf16 v[16:19], v[148:151], a[208:211], v[16:19]\n"
"v_mfma_f32_16x16x32_bf16 v[16:19], v[152:155], a[212:215], v[16:19]\n"
"v_mfma_f32_16x16x32_bf16 v[20:23], v[148:151], a[232:235], v[20:23]\n"
"v_mfma_f32_16x16x32_bf16 v[20:23], v[152:155], a[236:239], v[20:23]\n"
"v_mfma_f32_16x16x32_bf16 v[24:27], v[148:151], v[116:119], v[24:27]\n"
"s_add_u32 m0, s43, 4096\n"
"ds_read_b128 v[164:167], v196 offset:6272\n"
"ds_read_b128 v[168:171], v197 offset:6272\n"
"v_mfma_f32_16x16x32_bf16 v[24:27], v[152:155], v[120:123], v[24:27]\n"
"v_mfma_f32_16x16x32_bf16 v[28:31], v[148:151], v[140:143], v[28:31]\n"
"v_mfma_f32_16x16x32_bf16 v[28:31], v[152:155], v[144:147], v[28:31]\n"
"s_waitcnt lgkmcnt(2)\n"
"v_mfma_f32_16x16x32_bf16 v[32:35], v[156:159], a[192:195], v[80:83]\n"
"v_mfma_f32_16x16x32_bf16 v[32:35], v[160:163], a[196:199], v[32:35]\n"
"v_mfma_f32_16x16x32_bf16 v[36:39], v[156:159], a[216:219], v[84:87]\n"
"v_mfma_f32_16x16x32_bf16 v[36:39], v[160:163], a[220:223], v[36:39]\n"
"v_mfma_f32_16x16x32_bf16 v[40:43], v[156:159], a[240:243], v[88:91]\n"
"ds_read_b128 v[172:175], v196 offset:6400\n"
"ds_read_b128 v[176:179], v197 offset:6400\n"
"v_mfma_f32_16x16x32_bf16 v[40:43], v[160:163], a[244:247], v[40:43]\n"
"v_mfma_f32_16x16x32_bf16 v[44:47], v[156:159], v[124:127], v[92:95]\n"
"v_mfma_f32_16x16x32_bf16 v[44:47], v[160:163], v[128:131], v[44:47]\n"
"s_waitcnt lgkmcnt(2)\n"
"s_mov_b32 s45, s44\n"
"v_mfma_f32_16x16x32_bf16 v[32:35], v[164:167], a[200:203], v[32:35]\n"
"s_add_u32 s90, s90, 1\n"
"v_exp_f32_e32 v16, v16\n"
"v_mfma_f32_16x16x32_bf16 v[32:35], v[168:171], a[204:207], v[32:35]\n"
"s_add_u32 s44, s88, s90\n"
"v_exp_f32_e32 v17, v17\n"
"v_mfma_f32_16x16x32_bf16 v[36:39], v[164:167], a[224:227], v[36:39]\n"
"s_and_b32 s44, s44, 15\n"
"v_cvt_pk_bf16_f32 v48, v16, v17\n"
"v_mfma_f32_16x16x32_bf16 v[36:39], v[168:171], a[228:231], v[36:39]\n"
"s_mul_i32 s44, s44, 24576\n"
"v_exp_f32_e32 v18, v18\n"
"v_mfma_f32_16x16x32_bf16 v[40:43], v[164:167], a[248:251], v[40:43]\n"
"s_add_u32 s44, s44, s89\n"
"ds_read_b64_tr_b16 v[148:149], v198 offset:36864\n"
"ds_read_b64_tr_b16 v[150:151], v198 offset:43008\n"
"v_mfma_f32_16x16x32_bf16 v[40:43], v[168:171], a[252:255], v[40:43]\n"
"s_cmp_ge_u32 s90, 16\n"
"s_cselect_b32 s22, 0, 393216\n"
"v_mfma_f32_16x16x32_bf16 v[44:47], v[164:167], v[132:135], v[44:47]\n"
"s_cmp_ge_u32 s90, 17\n"
"s_cselect_b32 s26, 0, 393216\n"
"v_mfma_f32_16x16x32_bf16 v[44:47], v[168:171], v[136:139], v[44:47]\n"
"s_waitcnt lgkmcnt(2)\n"
"s_add_u32 s42, s42, s48\n"
"v_mfma_f32_16x16x32_bf16 v[32:35], v[172:175], a[208:211], v[32:35]\n"
"s_add_u32 s49, s49, 1\n"
"ds_read_b64_tr_b16 v[152:153], v199 offset:36864\n"
"ds_read_b64_tr_b16 v[154:155], v199 offset:43008\n"
"v_mfma_f32_16x16x32_bf16 v[32:35], v[176:179], a[212:215], v[32:35]\n"
"s_cmp_eq_u32 s49, 3\n"
"s_cselect_b32 s49, 0, s49\n"
"v_mfma_f32_16x16x32_bf16 v[36:39], v[172:175], a[232:235], v[36:39]\n"
"s_cmp_eq_u32 s49, 2\n"
"s_cselect_b32 s48, s69, s68\n"
"v_mfma_f32_16x16x32_bf16 v[36:39], v[176:179], a[236:239], v[36:39]\n"
"v_exp_f32_e32 v19, v19\n"
"v_mfma_f32_16x16x32_bf16 v[40:43], v[172:175], v[116:119], v[40:43]\n"
"ds_read_b64_tr_b16 v[156:157], v200 offset:36864\n"
"ds_read_b64_tr_b16 v[158:159], v200 offset:43008\n"
"v_mfma_f32_16x16x32_bf16 v[40:43], v[176:179], v[120:123], v[40:43]\n"
"v_exp_f32_e32 v20, v20\n"
"v_mfma_f32_16x16x32_bf16 v[44:47], v[172:175], v[140:143], v[44:47]\n"
"v_exp_f32_e32 v21, v21\n"
"v_mfma_f32_16x16x32_bf16 v[44:47], v[176:179], v[144:147], v[44:47]\n"
"s_waitcnt lgkmcnt(2)\n"
"v_exp_f32_e32 v22, v22\n"
"v_mfma_f32_16x16x32_bf16 a[0:3], v[148:151], v[64:67], a[0:3]\n"
"ds_read_b64_tr_b16 v[160:161], v201 offset:36864\n"
"ds_read_b64_tr_b16 v[162:163], v201 offset:43008\n"
"v_mfma_f32_16x16x32_bf16 a[4:7], v[148:151], v[68:71], a[4:7]\n"
"v_exp_f32_e32 v23, v23\n"
"v_mfma_f32_16x16x32_bf16 a[8:11], v[148:151], v[72:75], a[8:11]\n"
"v_exp_f32_e32 v24, v24\n"
"v_mfma_f32_16x16x32_bf16 a[12:15], v[148:151], v[76:79], a[12:15]\n"
"v_cvt_pk_bf16_f32 v49, v18, v19\n"
"v_cvt_pk_bf16_f32 v52, v20, v21\n"
"v_mfma_f32_16x16x32_bf16 a[16:19], v[152:155], v[64:67], a[16:19]\n"
"ds_read_b64_tr_b16 v[164:165], v198 offset:36992\n"
"ds_read_b64_tr_b16 v[166:167], v198 offset:43136\n"
"v_mfma_f32_16x16x32_bf16 a[20:23], v[152:155], v[68:71], a[20:23]\n"
"v_exp_f32_e32 v25, v25\n"
"v_mfma_f32_16x16x32_bf16 a[24:27], v[152:155], v[72:75], a[24:27]\n"
"v_cvt_pk_bf16_f32 v53, v22, v23\n"
"v_cvt_pk_bf16_f32 v56, v24, v25\n"
"v_mfma_f32_16x16x32_bf16 a[28:31], v[152:155], v[76:79], a[28:31]\n"
"s_waitcnt lgkmcnt(2)\n"
"v_exp_f32_e32 v26, v26\n"
"v_mfma_f32_16x16x32_bf16 a[32:35], v[156:159], v[64:67], a[32:35]\n"
"ds_read_b64_tr_b16 v[168:169], v199 offset:36992\n"
"ds_read_b64_tr_b16 v[170:171], v199 offset:43136\n"
"v_mfma_f32_16x16x32_bf16 a[36:39], v[156:159], v[68:71], a[36:39]\n"
"v_exp_f32_e32 v27, v27\n"
"v_mfma_f32_16x16x32_bf16 a[40:43], v[156:159], v[72:75], a[40:43]\n"
"v_cvt_pk_bf16_f32 v57, v26, v27\n"
"v_mfma_f32_16x16x32_bf16 a[44:47], v[156:159], v[76:79], a[44:47]\n"
"v_mfma_f32_16x16x32_bf16 a[48:51], v[160:163], v[64:67], a[48:51]\n"
"ds_read_b64_tr_b16 v[172:173], v200 offset:36992\n"
"ds_read_b64_tr_b16 v[174:175], v200 offset:43136\n"
"v_mfma_f32_16x16x32_bf16 a[52:55], v[160:163], v[68:71], a[52:55]\n"
"v_exp_f32_e32 v28, v28\n"
"v_mfma_f32_16x16x32_bf16 a[56:59], v[160:163], v[72:75], a[56:59]\n"
"v_exp_f32_e32 v29, v29\n"
"v_mfma_f32_16x16x32_bf16 a[60:63], v[160:163], v[76:79], a[60:63]\n"
"s_waitcnt lgkmcnt(2)\n"
"v_cvt_pk_bf16_f32 v60, v28, v29\n"
"v_mfma_f32_16x16x32_bf16 a[64:67], v[164:167], v[64:67], a[64:67]\n"
"ds_read_b64_tr_b16 v[176:177], v201 offset:36992\n"
"ds_read_b64_tr_b16 v[178:179], v201 offset:43136\n"
"v_mfma_f32_16x16x32_bf16 a[68:71], v[164:167], v[68:71], a[68:71]\n"
"v_exp_f32_e32 v30, v30\n"
"v_mfma_f32_16x16x32_bf16 a[72:75], v[164:167], v[72:75], a[72:75]\n"
"v_mfma_f32_16x16x32_bf16 a[76:79], v[164:167], v[76:79], a[76:79]\n"
"v_mfma_f32_16x16x32_bf16 a[80:83], v[168:171], v[64:67], a[80:83]\n"
"ds_read_b64_tr_b16 v[148:149], v198 offset:37120\n"
"ds_read_b64_tr_b16 v[150:151], v198 offset:43264\n"
"v_mfma_f32_16x16x32_bf16 a[84:87], v[168:171], v[68:71], a[84:87]\n"
"v_exp_f32_e32 v31, v31\n"
"v_mfma_f32_16x16x32_bf16 a[88:91], v[168:171], v[72:75], a[88:91]\n"
"v_exp_f32_e32 v32, v32\n"
"v_mfma_f32_16x16x32_bf16 a[92:95], v[168:171], v[76:79], a[92:95]\n"
"s_waitcnt lgkmcnt(2)\n"
"v_cvt_pk_bf16_f32 v61, v30, v31\n"
"v_mfma_f32_16x16x32_bf16 a[96:99], v[172:175], v[64:67], a[96:99]\n"
"ds_read_b64_tr_b16 v[152:153], v199 offset:37120\n"
"ds_read_b64_tr_b16 v[154:155], v199 offset:43264\n"
"v_mfma_f32_16x16x32_bf16 a[100:103], v[172:175], v[68:71], a[100:103]\n"
"v_exp_f32_e32 v33, v33\n"
"v_mfma_f32_16x16x32_bf16 a[104:107], v[172:175], v[72:75], a[104:107]\n"
"v_cvt_pk_bf16_f32 v50, v32, v33\n"
"v_mfma_f32_16x16x32_bf16 a[108:111], v[172:175], v[76:79], a[108:111]\n"
"v_exp_f32_e32 v34, v34\n"
"v_mfma_f32_16x16x32_bf16 a[112:115], v[176:179], v[64:67], a[112:115]\n"
"ds_read_b64_tr_b16 v[156:157], v200 offset:37120\n"
"ds_read_b64_tr_b16 v[158:159], v200 offset:43264\n"
"v_mfma_f32_16x16x32_bf16 a[116:119], v[176:179], v[68:71], a[116:119]\n"
"v_mfma_f32_16x16x32_bf16 a[120:123], v[176:179], v[72:75], a[120:123]\n"
"v_exp_f32_e32 v35, v35\n"
"v_mfma_f32_16x16x32_bf16 a[124:127], v[176:179], v[76:79], a[124:127]\n"
"s_waitcnt lgkmcnt(2)\n"
"v_cvt_pk_bf16_f32 v51, v34, v35\n"
"v_mfma_f32_16x16x32_bf16 a[128:131], v[148:151], v[64:67], a[128:131]\n"
"ds_read_b64_tr_b16 v[160:161], v201 offset:37120\n"
"ds_read_b64_tr_b16 v[162:163], v201 offset:43264\n"
"v_mfma_f32_16x16x32_bf16 a[132:135], v[148:151], v[68:71], a[132:135]\n"
"v_exp_f32_e32 v36, v36\n"
"v_mfma_f32_16x16x32_bf16 a[136:139], v[148:151], v[72:75], a[136:139]\n"
"v_mfma_f32_16x16x32_bf16 a[140:143], v[148:151], v[76:79], a[140:143]\n"
"v_exp_f32_e32 v37, v37\n"
"v_mfma_f32_16x16x32_bf16 a[144:147], v[152:155], v[64:67], a[144:147]\n"
"v_cvt_pk_bf16_f32 v54, v36, v37\n"
"v_mfma_f32_16x16x32_bf16 a[148:151], v[152:155], v[68:71], a[148:151]\n"
"v_mfma_f32_16x16x32_bf16 a[152:155], v[152:155], v[72:75], a[152:155]\n"
"v_exp_f32_e32 v38, v38\n"
"v_mfma_f32_16x16x32_bf16 a[156:159], v[152:155], v[76:79], a[156:159]\n"
"s_waitcnt lgkmcnt(0)\n"
"v_mfma_f32_16x16x32_bf16 a[160:163], v[156:159], v[64:67], a[160:163]\n"
"ds_read_b128 v[164:167], v196 offset:12288\n"
"ds_read_b128 v[168:171], v197 offset:12288\n"
"v_mfma_f32_16x16x32_bf16 a[164:167], v[156:159], v[68:71], a[164:167]\n"
"v_exp_f32_e32 v39, v39\n"
"v_mfma_f32_16x16x32_bf16 a[168:171], v[156:159], v[72:75], a[168:171]\n"
"v_cvt_pk_bf16_f32 v55, v38, v39\n"
"v_mfma_f32_16x16x32_bf16 a[172:175], v[156:159], v[76:79], a[172:175]\n"
"v_exp_f32_e32 v40, v40\n"
"v_mfma_f32_16x16x32_bf16 a[176:179], v[160:163], v[64:67], a[176:179]\n"
"v_mfma_f32_16x16x32_bf16 a[180:183], v[160:163], v[68:71], a[180:183]\n"
"v_mfma_f32_16x16x32_bf16 a[184:187], v[160:163], v[72:75], a[184:187]\n"
"v_exp_f32_e32 v41, v41\n"
"v_mfma_f32_16x16x32_bf16 a[188:191], v[160:163], v[76:79], a[188:191]\n"
"v_cvt_pk_bf16_f32 v58, v40, v41\n"
"v_mfma_f32_16x16x32_bf16 v[96:99], v[112:115], v[64:67], v[96:99]\n"
"ds_read_b128 v[172:175], v196 offset:12416\n"
"ds_read_b128 v[176:179], v197 offset:12416\n"
"v_mfma_f32_16x16x32_bf16 v[100:103], v[112:115], v[68:71], v[100:103]\n"
"v_exp_f32_e32 v42, v42\n"
"v_mfma_f32_16x16x32_bf16 v[104:107], v[112:115], v[72:75], v[104:107]\n"
"v_mfma_f32_16x16x32_bf16 v[108:111], v[112:115], v[76:79], v[108:111]\n"
"s_waitcnt lgkmcnt(2)\n"
"v_exp_f32_e32 v43, v43\n"
"v_mfma_f32_16x16x32_bf16 v[16:19], v[164:167], a[192:195], v[80:83]\n"
"ds_read_b128 v[148:151], v196 offset:12544\n"
"v_cvt_pk_bf16_f32 v59, v42, v43\n"
"v_mfma_f32_16x16x32_bf16 v[16:19], v[168:171], a[196:199], v[16:19]\n"
"ds_read_b128 v[152:155], v197 offset:12544\n"
"v_mfma_f32_16x16x32_bf16 v[20:23], v[164:167], a[216:219], v[84:87]\n"
"ds_read_b128 v[156:159], v196 offset:18432\n"
"v_mfma_f32_16x16x32_bf16 v[20:23], v[168:171], a[220:223], v[20:23]\n"
"ds_read_b128 v[160:163], v197 offset:18432\n"
"v_mfma_f32_16x16x32_bf16 v[24:27], v[164:167], a[240:243], v[88:91]\n"
"v_exp_f32_e32 v44, v44\n"
"v_mfma_f32_16x16x32_bf16 v[24:27], v[168:171], a[244:247], v[24:27]\n"
"v_exp_f32_e32 v45, v45\n"
"v_mfma_f32_16x16x32_bf16 v[28:31], v[164:167], v[124:127], v[92:95]\n"
"v_cvt_pk_bf16_f32 v62, v44, v45\n"
"v_mfma_f32_16x16x32_bf16 v[28:31], v[168:171], v[128:131], v[28:31]\n"
"s_waitcnt lgkmcnt(4)\n"
"v_exp_f32_e32 v46, v46\n"
"v_mfma_f32_16x16x32_bf16 v[16:19], v[172:175], a[200:203], v[16:19]\n"
"ds_read_b128 v[164:167], v196 offset:18560\n"
"v_mfma_f32_16x16x32_bf16 v[16:19], v[176:179], a[204:207], v[16:19]\n"
"ds_read_b128 v[168:171], v197 offset:18560\n"
"v_mfma_f32_16x16x32_bf16 v[20:23], v[172:175], a[224:227], v[20:23]\n"
"v_exp_f32_e32 v47, v47\n"
"v_mfma_f32_16x16x32_bf16 v[20:23], v[176:179], a[228:231], v[20:23]\n"
"v_cvt_pk_bf16_f32 v63, v46, v47\n"
"v_mfma_f32_16x16x32_bf16 v[24:27], v[172:175], a[248:251], v[24:27]\n"
"v_mfma_f32_16x16x32_bf16 v[24:27], v[176:179], a[252:255], v[24:27]\n"
"v_mfma_f32_16x16x32_bf16 v[28:31], v[172:175], v[132:135], v[28:31]\n"
"v_mfma_f32_16x16x32_bf16 v[28:31], v[176:179], v[136:139], v[28:31]\n"
"s_waitcnt lgkmcnt(4)\n"
"v_mfma_f32_16x16x32_bf16 v[16:19], v[148:151], a[208:211], v[16:19]\n"
"ds_read_b128 v[172:175], v196 offset:18688\n"
"v_mfma_f32_16x16x32_bf16 v[16:19], v[152:155], a[212:215], v[16:19]\n"
"ds_read_b128 v[176:179], v197 offset:18688\n"
"v_mfma_f32_16x16x32_bf16 v[20:23], v[148:151], a[232:235], v[20:23]\n"
"v_mfma_f32_16x16x32_bf16 v[20:23], v[152:155], a[236:239], v[20:23]\n"
"v_add_u32 v196, s48, v196\n"
"v_add_u32 v197, s48, v197\n"
"v_mfma_f32_16x16x32_bf16 v[24:27], v[148:151], v[116:119], v[24:27]\n"
"v_add_u32 v198, s48, v198\n"
"v_add_u32 v199, s48, v199\n"
"v_mfma_f32_16x16x32_bf16 v[24:27], v[152:155], v[120:123], v[24:27]\n"
"v_add_u32 v200, s48, v200\n"
"v_add_u32 v201, s48, v201\n"
"v_mfma_f32_16x16x32_bf16 v[28:31], v[148:151], v[140:143], v[28:31]\n"
"v_mfma_f32_16x16x32_bf16 v[28:31], v[152:155], v[144:147], v[28:31]\n"
"s_waitcnt lgkmcnt(4)\n"
"v_mfma_f32_16x16x32_bf16 v[32:35], v[156:159], a[192:195], v[80:83]\n"
"v_mfma_f32_16x16x32_bf16 v[32:35], v[160:163], a[196:199], v[32:35]\n"
"v_mfma_f32_16x16x32_bf16 v[36:39], v[156:159], a[216:219], v[84:87]\n"
"v_mfma_f32_16x16x32_bf16 v[36:39], v[160:163], a[220:223], v[36:39]\n"
"s_waitcnt vmcnt(12) lgkmcnt(0)\n"
"s_barrier\n"
"v_mfma_f32_16x16x32_bf16 v[40:43], v[156:159], a[240:243], v[88:91]\n"
"s_add_u32 s43, s42, 24576\n"
"v_mfma_f32_16x16x32_bf16 v[40:43], v[160:163], a[244:247], v[40:43]\n"
"v_mfma_f32_16x16x32_bf16 v[44:47], v[156:159], v[124:127], v[92:95]\n"
"v_exp_f32_e32 v16, v16\n"
"v_mfma_f32_16x16x32_bf16 v[44:47], v[160:163], v[128:131], v[44:47]\n"
"ds_read_b64_tr_b16 v[148:149], v198 offset:24576\n"
"ds_read_b64_tr_b16 v[150:151], v198 offset:30720\n"
"v_mfma_f32_16x16x32_bf16 v[32:35], v[164:167], a[200:203], v[32:35]\n"
"s_mov_b32 m0, s42\n"
"v_exp_f32_e32 v17, v17\n"
"v_mfma_f32_16x16x32_bf16 v[32:35], v[168:171], a[204:207], v[32:35]\n"
"buffer_load_dwordx4 v202, s[20:23], s44 offen offset:0 nt lds\n"
"v_cvt_pk_bf16_f32 v64, v16, v17\n"
"v_mfma_f32_16x16x32_bf16 v[36:39], v[164:167], a[224:227], v[36:39]\n"
"v_mfma_f32_16x16x32_bf16 v[36:39], v[168:171], a[228:231], v[36:39]\n"
"ds_read_b64_tr_b16 v[152:153], v199 offset:24576\n"
"ds_read_b64_tr_b16 v[154:155], v199 offset:30720\n"
"v_mfma_f32_16x16x32_bf16 v[40:43], v[164:167], a[248:251], v[40:43]\n"
"v_exp_f32_e32 v18, v18\n"
"v_mfma_f32_16x16x32_bf16 v[40:43], v[168:171], a[252:255], v[40:43]\n"
"v_exp_f32_e32 v19, v19\n"
"v_mfma_f32_16x16x32_bf16 v[44:47], v[164:167], v[132:135], v[44:47]\n"
"v_cvt_pk_bf16_f32 v65, v18, v19\n"
"v_mfma_f32_16x16x32_bf16 v[44:47], v[168:171], v[136:139], v[44:47]\n"
"ds_read_b64_tr_b16 v[156:157], v200 offset:24576\n"
"ds_read_b64_tr_b16 v[158:159], v200 offset:30720\n"
"v_mfma_f32_16x16x32_bf16 v[32:35], v[172:175], a[208:211], v[32:35]\n"
"buffer_load_dwordx4 v203, s[20:23], s44 offen offset:1024 nt lds\n"
"v_mfma_f32_16x16x32_bf16 v[32:35], v[176:179], a[212:215], v[32:35]\n"
"v_exp_f32_e32 v20, v20\n"
"v_mfma_f32_16x16x32_bf16 v[36:39], v[172:175], a[232:235], v[36:39]\n"
"v_exp_f32_e32 v21, v21\n"
"v_mfma_f32_16x16x32_bf16 v[36:39], v[176:179], a[236:239], v[36:39]\n"
"v_cvt_pk_bf16_f32 v68, v20, v21\n"
"v_mfma_f32_16x16x32_bf16 v[40:43], v[172:175], v[116:119], v[40:43]\n"
"v_mfma_f32_16x16x32_bf16 v[40:43], v[176:179], v[120:123], v[40:43]\n"
"v_exp_f32_e32 v22, v22\n"
"v_mfma_f32_16x16x32_bf16 v[44:47], v[172:175], v[140:143], v[44:47]\n"
"v_mfma_f32_16x16x32_bf16 v[44:47], v[176:179], v[144:147], v[44:47]\n"
"s_waitcnt lgkmcnt(2)\n"
"v_exp_f32_e32 v23, v23\n"
"s_mov_b32 s41, 14\n"
".p2align 6\n"
"L_ata%=:\n"
"v_mfma_f32_16x16x32_bf16 a[0:3], v[148:151], v[48:51], a[0:3]\n"
"ds_read_b64_tr_b16 v[160:161], v201 offset:24576\n"
"ds_read_b64_tr_b16 v[162:163], v201 offset:30720\n"
"v_mfma_f32_16x16x32_bf16 a[4:7], v[148:151], v[52:55], a[4:7]\n"
"buffer_load_dwordx4 v204, s[20:23], s44 offen offset:2048 nt lds\n"
"v_cvt_pk_bf16_f32 v69, v22, v23\n"
"v_mfma_f32_16x16x32_bf16 a[8:11], v[148:151], v[56:59], a[8:11]\n"
"v_exp_f32_e32 v24, v24\n"
"v_mfma_f32_16x16x32_bf16 a[12:15], v[148:151], v[60:63], a[12:15]\n"
"v_mfma_f32_16x16x32_bf16 a[16:19], v[152:155], v[48:51], a[16:19]\n"
"ds_read_b64_tr_b16 v[164:165], v198 offset:24704\n"
"ds_read_b64_tr_b16 v[166:167], v198 offset:30848\n"
"v_mfma_f32_16x16x32_bf16 a[20:23], v[152:155], v[52:55], a[20:23]\n"
"v_exp_f32_e32 v25, v25\n"
"v_mfma_f32_16x16x32_bf16 a[24:27], v[152:155], v[56:59], a[24:27]\n"
"v_cvt_pk_bf16_f32 v72, v24, v25\n"
"v_mfma_f32_16x16x32_bf16 a[28:31], v[152:155], v[60:63], a[28:31]\n"
"s_waitcnt lgkmcnt(2)\n"
"v_exp_f32_e32 v26, v26\n"
"v_mfma_f32_16x16x32_bf16 a[32:35], v[156:159], v[48:51], a[32:35]\n"
"ds_read_b64_tr_b16 v[168:169], v199 offset:24704\n"
"ds_read_b64_tr_b16 v[170:171], v199 offset:30848\n"
"v_mfma_f32_16x16x32_bf16 a[36:39], v[156:159], v[52:55], a[36:39]\n"
"v_exp_f32_e32 v27, v27\n"
"v_mfma_f32_16x16x32_bf16 a[40:43], v[156:159], v[56:59], a[40:43]\n"
"buffer_load_dwordx4 v205, s[20:23], s44 offen offset:3072 nt lds\n"
"v_cvt_pk_bf16_f32 v73, v26, v27\n"
"v_mfma_f32_16x16x32_bf16 a[44:47], v[156:159], v[60:63], a[44:47]\n"
"v_mfma_f32_16x16x32_bf16 a[48:51], v[160:163], v[48:51], a[48:51]\n"
"ds_read_b64_tr_b16 v[172:173], v200 offset:24704\n"
"ds_read_b64_tr_b16 v[174:175], v200 offset:30848\n"
"v_mfma_f32_16x16x32_bf16 a[52:55], v[160:163], v[52:55], a[52:55]\n"
"v_exp_f32_e32 v28, v28\n"
"v_mfma_f32_16x16x32_bf16 a[56:59], v[160:163], v[56:59], a[56:59]\n"
"v_exp_f32_e32 v29, v29\n"
"v_mfma_f32_16x16x32_bf16 a[60:63], v[160:163], v[60:63], a[60:63]\n"
"s_waitcnt lgkmcnt(2)\n"
"v_cvt_pk_bf16_f32 v76, v28, v29\n"
"v_mfma_f32_16x16x32_bf16 a[64:67], v[164:167], v[48:51], a[64:67]\n"
"ds_read_b64_tr_b16 v[176:177], v201 offset:24704\n"
"ds_read_b64_tr_b16 v[178:179], v201 offset:30848\n"
"v_mfma_f32_16x16x32_bf16 a[68:71], v[164:167], v[52:55], a[68:71]\n"
"v_exp_f32_e32 v30, v30\n"
"v_mfma_f32_16x16x32_bf16 a[72:75], v[164:167], v[56:59], a[72:75]\n"
"s_add_u32 m0, s42, 4096\n"
"v_mfma_f32_16x16x32_bf16 a[76:79], v[164:167], v[60:63], a[76:79]\n"
"buffer_load_dwordx4 v206, s[20:23], s44 offen offset:0 nt lds\n"
"v_mfma_f32_16x16x32_bf16 a[80:83], v[168:171], v[48:51], a[80:83]\n"
"ds_read_b64_tr_b16 v[148:149], v198 offset:24832\n"
"ds_read_b64_tr_b16 v[150:151], v198 offset:30976\n"
"v_mfma_f32_16x16x32_bf16 a[84:87], v[168:171], v[52:55], a[84:87]\n"
"v_exp_f32_e32 v31, v31\n"
"v_mfma_f32_16x16x32_bf16 a[88:91], v[168:171], v[56:59], a[88:91]\n"
"v_exp_f32_e32 v32, v32\n"
"v_mfma_f32_16x16x32_bf16 a[92:95], v[168:171], v[60:63], a[92:95]\n"
"s_waitcnt lgkmcnt(2)\n"
"v_cvt_pk_bf16_f32 v77, v30, v31\n"
"v_mfma_f32_16x16x32_bf16 a[96:99], v[172:175], v[48:51], a[96:99]\n"
"ds_read_b64_tr_b16 v[152:153], v199 offset:24832\n"
"ds_read_b64_tr_b16 v[154:155], v199 offset:30976\n"
"v_mfma_f32_16x16x32_bf16 a[100:103], v[172:175], v[52:55], a[100:103]\n"
"v_exp_f32_e32 v33, v33\n"
"v_mfma_f32_16x16x32_bf16 a[104:107], v[172:175], v[56:59], a[104:107]\n"
"v_cvt_pk_bf16_f32 v66, v32, v33\n"
"v_mfma_f32_16x16x32_bf16 a[108:111], v[172:175], v[60:63], a[108:111]\n"
"v_exp_f32_e32 v34, v34\n"
"v_mfma_f32_16x16x32_bf16 a[112:115], v[176:179], v[48:51], a[112:115]\n"
"ds_read_b64_tr_b16 v[156:157], v200 offset:24832\n"
"ds_read_b64_tr_b16 v[158:159], v200 offset:30976\n"
"v_mfma_f32_16x16x32_bf16 a[116:119], v[176:179], v[52:55], a[116:119]\n"
"buffer_load_dwordx4 v207, s[20:23], s44 offen offset:1024 nt lds\n"
"v_mfma_f32_16x16x32_bf16 a[120:123], v[176:179], v[56:59], a[120:123]\n"
"v_exp_f32_e32 v35, v35\n"
"v_mfma_f32_16x16x32_bf16 a[124:127], v[176:179], v[60:63], a[124:127]\n"
"s_waitcnt lgkmcnt(2)\n"
"v_cvt_pk_bf16_f32 v67, v34, v35\n"
"v_mfma_f32_16x16x32_bf16 a[128:131], v[148:151], v[48:51], a[128:131]\n"
"ds_read_b64_tr_b16 v[160:161], v201 offset:24832\n"
"ds_read_b64_tr_b16 v[162:163], v201 offset:30976\n"
"v_mfma_f32_16x16x32_bf16 a[132:135], v[148:151], v[52:55], a[132:135]\n"
"v_exp_f32_e32 v36, v36\n"
"v_mfma_f32_16x16x32_bf16 a[136:139], v[148:151], v[56:59], a[136:139]\n"
"v_mfma_f32_16x16x32_bf16 a[140:143], v[148:151], v[60:63], a[140:143]\n"
"v_exp_f32_e32 v37, v37\n"
"v_mfma_f32_16x16x32_bf16 a[144:147], v[152:155], v[48:51], a[144:147]\n"
"s_mov_b32 m0, s43\n"
"v_cvt_pk_bf16_f32 v70, v36, v37\n"
"v_mfma_f32_16x16x32_bf16 a[148:151], v[152:155], v[52:55], a[148:151]\n"
"buffer_load_dwordx4 v202, s[24:27], s45 offen offset:0 nt lds\n"
"v_mfma_f32_16x16x32_bf16 a[152:155], v[152:155], v[56:59], a[152:155]\n"
"v_exp_f32_e32 v38, v38\n"
"v_mfma_f32_16x16x32_bf16 a[156:159], v[152:155], v[60:63], a[156:159]\n"
"s_waitcnt lgkmcnt(0)\n"
"v_mfma_f32_16x16x32_bf16 a[160:163], v[156:159], v[48:51], a[160:163]\n"
"ds_read_b128 v[164:167], v196 offset:0\n"
"ds_read_b128 v[168:171], v197 offset:0\n"
"v_mfma_f32_16x16x32_bf16 a[164:167], v[156:159], v[52:55], a[164:167]\n"
"v_exp_f32_e32 v39, v39\n"
"v_mfma_f32_16x16x32_bf16 a[168:171], v[156:159], v[56:59], a[168:171]\n"
"v_cvt_pk_bf16_f32 v71, v38, v39\n"
"v_mfma_f32_16x16x32_bf16 a[172:175], v[156:159], v[60:63], a[172:175]\n"
"v_exp_f32_e32 v40, v40\n"
"v_mfma_f32_16x16x32_bf16 a[176:179], v[160:163], v[48:51], a[176:179]\n"
"v_mfma_f32_16x16x32_bf16 a[180:183], v[160:163], v[52:55], a[180:183]\n"
"v_mfma_f32_16x16x32_bf16 a[184:187], v[160:163], v[56:59], a[184:187]\n"
"buffer_load_dwordx4 v203, s[24:27], s45 offen offset:1024 nt lds\n"
"v_mfma_f32_16x16x32_bf16 a[188:191], v[160:163], v[60:63], a[188:191]\n"
"v_exp_f32_e32 v41, v41\n"
"v_mfma_f32_16x16x32_bf16 v[96:99], v[112:115], v[48:51], v[96:99]\n"
"ds_read_b128 v[172:175], v196 offset:128\n"
"ds_read_b128 v[176:179], v197 offset:128\n"
"v_mfma_f32_16x16x32_bf16 v[100:103], v[112:115], v[52:55], v[100:103]\n"
"v_exp_f32_e32 v42, v42\n"
"v_mfma_f32_16x16x32_bf16 v[104:107], v[112:115], v[56:59], v[104:107]\n"
"v_cvt_pk_bf16_f32 v74, v40, v41\n"
"v_mfma_f32_16x16x32_bf16 v[108:111], v[112:115], v[60:63], v[108:111]\n"
"s_waitcnt lgkmcnt(2)\n"
"v_exp_f32_e32 v43, v43\n"
"v_mfma_f32_16x16x32_bf16 v[16:19], v[164:167], a[192:195], v[80:83]\n"
"v_cvt_pk_bf16_f32 v75, v42, v43\n"
"v_mfma_f32_16x16x32_bf16 v[16:19], v[168:171], a[196:199], v[16:19]\n"
"v_mfma_f32_16x16x32_bf16 v[20:23], v[164:167], a[216:219], v[84:87]\n"
"v_exp_f32_e32 v44, v44\n"
"v_mfma_f32_16x16x32_bf16 v[20:23], v[168:171], a[220:223], v[20:23]\n"
"buffer_load_dwordx4 v204, s[24:27], s45 offen offset:2048 nt lds\n"
"v_mfma_f32_16x16x32_bf16 v[24:27], v[164:167], a[240:243], v[88:91]\n"
"ds_read_b128 v[148:151], v196 offset:256\n"
"ds_read_b128 v[152:155], v197 offset:256\n"
"v_mfma_f32_16x16x32_bf16 v[24:27], v[168:171], a[244:247], v[24:27]\n"
"v_exp_f32_e32 v45, v45\n"
"v_mfma_f32_16x16x32_bf16 v[28:31], v[164:167], v[124:127], v[92:95]\n"
"v_cvt_pk_bf16_f32 v78, v44, v45\n"
"v_mfma_f32_16x16x32_bf16 v[28:31], v[168:171], v[128:131], v[28:31]\n"
"s_waitcnt lgkmcnt(2)\n"
"v_exp_f32_e32 v46, v46\n"
"v_mfma_f32_16x16x32_bf16 v[16:19], v[172:175], a[200:203], v[16:19]\n"
"v_mfma_f32_16x16x32_bf16 v[16:19], v[176:179], a[204:207], v[16:19]\n"
"v_mfma_f32_16x16x32_bf16 v[20:23], v[172:175], a[224:227], v[20:23]\n"
"v_exp_f32_e32 v47, v47\n"
"v_mfma_f32_16x16x32_bf16 v[20:23], v[176:179], a[228:231], v[20:23]\n"
"v_cvt_pk_bf16_f32 v79, v46, v47\n"
"v_mfma_f32_16x16x32_bf16 v[24:27], v[172:175], a[248:251], v[24:27]\n"
"ds_read_b128 v[156:159], v196 offset:6144\n"
"ds_read_b128 v[160:163], v197 offset:6144\n"
"v_mfma_f32_16x16x32_bf16 v[24:27], v[176:179], a[252:255], v[24:27]\n"
"buffer_load_dwordx4 v205, s[24:27], s45 offen offset:3072 nt lds\n"
"v_mfma_f32_16x16x32_bf16 v[28:31], v[172:175], v[132:135], v[28:31]\n"
"v_mfma_f32_16x16x32_bf16 v[28:31], v[176:179], v[136:139], v[28:31]\n"
"s_waitcnt lgkmcnt(2)\n"
"v_mfma_f32_16x16x32_bf16 v[16:19], v[148:151], a[208:211], v[16:19]\n"
"v_mfma_f32_16x16x32_bf16 v[16:19], v[152:155], a[212:215], v[16:19]\n"
"v_mfma_f32_16x16x32_bf16 v[20:23], v[148:151], a[232:235], v[20:23]\n"
"v_mfma_f32_16x16x32_bf16 v[20:23], v[152:155], a[236:239], v[20:23]\n"
"v_mfma_f32_16x16x32_bf16 v[24:27], v[148:151], v[116:119], v[24:27]\n"
"s_add_u32 m0, s43, 4096\n"
"ds_read_b128 v[164:167], v196 offset:6272\n"
"ds_read_b128 v[168:171], v197 offset:6272\n"
"v_mfma_f32_16x16x32_bf16 v[24:27], v[152:155], v[120:123], v[24:27]\n"
"buffer_load_dwordx4 v206, s[24:27], s45 offen offset:0 nt lds\n"
"v_mfma_f32_16x16x32_bf16 v[28:31], v[148:151], v[140:143], v[28:31]\n"
"v_mfma_f32_16x16x32_bf16 v[28:31], v[152:155], v[144:147], v[28:31]\n"
"s_waitcnt lgkmcnt(2)\n"
"v_mfma_f32_16x16x32_bf16 v[32:35], v[156:159], a[192:195], v[80:83]\n"
"v_mfma_f32_16x16x32_bf16 v[32:35], v[160:163], a[196:199], v[32:35]\n"
"v_mfma_f32_16x16x32_bf16 v[36:39], v[156:159], a[216:219], v[84:87]\n"
"v_mfma_f32_16x16x32_bf16 v[36:39], v[160:163], a[220:223], v[36:39]\n"
"v_mfma_f32_16x16x32_bf16 v[40:43], v[156:159], a[240:243], v[88:91]\n"
"ds_read_b128 v[172:175], v196 offset:6400\n"
"ds_read_b128 v[176:179], v197 offset:6400\n"
"v_mfma_f32_16x16x32_bf16 v[40:43], v[160:163], a[244:247], v[40:43]\n"
"v_mfma_f32_16x16x32_bf16 v[44:47], v[156:159], v[124:127], v[92:95]\n"
"buffer_load_dwordx4 v207, s[24:27], s45 offen offset:1024 nt lds\n"
"v_mfma_f32_16x16x32_bf16 v[44:47], v[160:163], v[128:131], v[44:47]\n"
"s_waitcnt lgkmcnt(2)\n"
"s_mov_b32 s45, s44\n"
"v_mfma_f32_16x16x32_bf16 v[32:35], v[164:167], a[200:203], v[32:35]\n"
"s_add_u32 s90, s90, 1\n"
"v_exp_f32_e32 v16, v16\n"
"v_mfma_f32_16x16x32_bf16 v[32:35], v[168:171], a[204:207], v[32:35]\n"
"s_add_u32 s44, s88, s90\n"
"v_exp_f32_e32 v17, v17\n"
"v_mfma_f32_16x16x32_bf16 v[36:39], v[164:167], a[224:227], v[36:39]\n"
"s_and_b32 s44, s44, 15\n"
"v_cvt_pk_bf16_f32 v48, v16, v17\n"
"v_mfma_f32_16x16x32_bf16 v[36:39], v[168:171], a[228:231], v[36:39]\n"
"s_mul_i32 s44, s44, 24576\n"
"v_exp_f32_e32 v18, v18\n"
"v_mfma_f32_16x16x32_bf16 v[40:43], v[164:167], a[248:251], v[40:43]\n"
"s_add_u32 s44, s44, s89\n"
"ds_read_b64_tr_b16 v[148:149], v198 offset:36864\n"
"ds_read_b64_tr_b16 v[150:151], v198 offset:43008\n"
"v_mfma_f32_16x16x32_bf16 v[40:43], v[168:171], a[252:255], v[40:43]\n"
"s_cmp_ge_u32 s90, 16\n"
"s_cselect_b32 s22, 0, 393216\n"
"v_mfma_f32_16x16x32_bf16 v[44:47], v[164:167], v[132:135], v[44:47]\n"
"s_cmp_ge_u32 s90, 17\n"
"s_cselect_b32 s26, 0, 393216\n"
"v_mfma_f32_16x16x32_bf16 v[44:47], v[168:171], v[136:139], v[44:47]\n"
"s_waitcnt lgkmcnt(2)\n"
"s_add_u32 s42, s42, s48\n"
"v_mfma_f32_16x16x32_bf16 v[32:35], v[172:175], a[208:211], v[32:35]\n"
"s_add_u32 s49, s49, 1\n"
"ds_read_b64_tr_b16 v[152:153], v199 offset:36864\n"
"ds_read_b64_tr_b16 v[154:155], v199 offset:43008\n"
"v_mfma_f32_16x16x32_bf16 v[32:35], v[176:179], a[212:215], v[32:35]\n"
"s_cmp_eq_u32 s49, 3\n"
"s_cselect_b32 s49, 0, s49\n"
"v_mfma_f32_16x16x32_bf16 v[36:39], v[172:175], a[232:235], v[36:39]\n"
"s_cmp_eq_u32 s49, 2\n"
"s_cselect_b32 s48, s69, s68\n"
"v_mfma_f32_16x16x32_bf16 v[36:39], v[176:179], a[236:239], v[36:39]\n"
"v_exp_f32_e32 v19, v19\n"
"v_mfma_f32_16x16x32_bf16 v[40:43], v[172:175], v[116:119], v[40:43]\n"
"ds_read_b64_tr_b16 v[156:157], v200 offset:36864\n"
"ds_read_b64_tr_b16 v[158:159], v200 offset:43008\n"
"v_mfma_f32_16x16x32_bf16 v[40:43], v[176:179], v[120:123], v[40:43]\n"
"v_exp_f32_e32 v20, v20\n"
"v_mfma_f32_16x16x32_bf16 v[44:47], v[172:175], v[140:143], v[44:47]\n"
"v_exp_f32_e32 v21, v21\n"
"v_mfma_f32_16x16x32_bf16 v[44:47], v[176:179], v[144:147], v[44:47]\n"
"s_waitcnt lgkmcnt(2)\n"
"v_exp_f32_e32 v22, v22\n"
"v_mfma_f32_16x16x32_bf16 a[0:3], v[148:151], v[64:67], a[0:3]\n"
"ds_read_b64_tr_b16 v[160:161], v201 offset:36864\n"
"ds_read_b64_tr_b16 v[162:163], v201 offset:43008\n"
"v_mfma_f32_16x16x32_bf16 a[4:7], v[148:151], v[68:71], a[4:7]\n"
"v_exp_f32_e32 v23, v23\n"
"v_mfma_f32_16x16x32_bf16 a[8:11], v[148:151], v[72:75], a[8:11]\n"
"v_exp_f32_e32 v24, v24\n"
"v_mfma_f32_16x16x32_bf16 a[12:15], v[148:151], v[76:79], a[12:15]\n"
"v_cvt_pk_bf16_f32 v49, v18, v19\n"
"v_cvt_pk_bf16_f32 v52, v20, v21\n"
"v_mfma_f32_16x16x32_bf16 a[16:19], v[152:155], v[64:67], a[16:19]\n"
"ds_read_b64_tr_b16 v[164:165], v198 offset:36992\n"
"ds_read_b64_tr_b16 v[166:167], v198 offset:43136\n"
"v_mfma_f32_16x16x32_bf16 a[20:23], v[152:155], v[68:71], a[20:23]\n"
"v_exp_f32_e32 v25, v25\n"
"v_mfma_f32_16x16x32_bf16 a[24:27], v[152:155], v[72:75], a[24:27]\n"
"v_cvt_pk_bf16_f32 v53, v22, v23\n"
"v_cvt_pk_bf16_f32 v56, v24, v25\n"
"v_mfma_f32_16x16x32_bf16 a[28:31], v[152:155], v[76:79], a[28:31]\n"
"s_waitcnt lgkmcnt(2)\n"
"v_exp_f32_e32 v26, v26\n"
"v_mfma_f32_16x16x32_bf16 a[32:35], v[156:159], v[64:67], a[32:35]\n"
"ds_read_b64_tr_b16 v[168:169], v199 offset:36992\n"
"ds_read_b64_tr_b16 v[170:171], v199 offset:43136\n"
"v_mfma_f32_16x16x32_bf16 a[36:39], v[156:159], v[68:71], a[36:39]\n"
"v_exp_f32_e32 v27, v27\n"
"v_mfma_f32_16x16x32_bf16 a[40:43], v[156:159], v[72:75], a[40:43]\n"
"v_cvt_pk_bf16_f32 v57, v26, v27\n"
"v_mfma_f32_16x16x32_bf16 a[44:47], v[156:159], v[76:79], a[44:47]\n"
"v_mfma_f32_16x16x32_bf16 a[48:51], v[160:163], v[64:67], a[48:51]\n"
"ds_read_b64_tr_b16 v[172:173], v200 offset:36992\n"
"ds_read_b64_tr_b16 v[174:175], v200 offset:43136\n"
"v_mfma_f32_16x16x32_bf16 a[52:55], v[160:163], v[68:71], a[52:55]\n"
"v_exp_f32_e32 v28, v28\n"
"v_mfma_f32_16x16x32_bf16 a[56:59], v[160:163], v[72:75], a[56:59]\n"
"v_exp_f32_e32 v29, v29\n"
"v_mfma_f32_16x16x32_bf16 a[60:63], v[160:163], v[76:79], a[60:63]\n"
"s_waitcnt lgkmcnt(2)\n"
"v_cvt_pk_bf16_f32 v60, v28, v29\n"
"v_mfma_f32_16x16x32_bf16 a[64:67], v[164:167], v[64:67], a[64:67]\n"
"ds_read_b64_tr_b16 v[176:177], v201 offset:36992\n"
"ds_read_b64_tr_b16 v[178:179], v201 offset:43136\n"
"v_mfma_f32_16x16x32_bf16 a[68:71], v[164:167], v[68:71], a[68:71]\n"
"v_exp_f32_e32 v30, v30\n"
"v_mfma_f32_16x16x32_bf16 a[72:75], v[164:167], v[72:75], a[72:75]\n"
"v_mfma_f32_16x16x32_bf16 a[76:79], v[164:167], v[76:79], a[76:79]\n"
"v_mfma_f32_16x16x32_bf16 a[80:83], v[168:171], v[64:67], a[80:83]\n"
"ds_read_b64_tr_b16 v[148:149], v198 offset:37120\n"
"ds_read_b64_tr_b16 v[150:151], v198 offset:43264\n"
"v_mfma_f32_16x16x32_bf16 a[84:87], v[168:171], v[68:71], a[84:87]\n"
"v_exp_f32_e32 v31, v31\n"
"v_mfma_f32_16x16x32_bf16 a[88:91], v[168:171], v[72:75], a[88:91]\n"
"v_exp_f32_e32 v32, v32\n"
"v_mfma_f32_16x16x32_bf16 a[92:95], v[168:171], v[76:79], a[92:95]\n"
"s_waitcnt lgkmcnt(2)\n"
"v_cvt_pk_bf16_f32 v61, v30, v31\n"
"v_mfma_f32_16x16x32_bf16 a[96:99], v[172:175], v[64:67], a[96:99]\n"
"ds_read_b64_tr_b16 v[152:153], v199 offset:37120\n"
"ds_read_b64_tr_b16 v[154:155], v199 offset:43264\n"
"v_mfma_f32_16x16x32_bf16 a[100:103], v[172:175], v[68:71], a[100:103]\n"
"v_exp_f32_e32 v33, v33\n"
"v_mfma_f32_16x16x32_bf16 a[104:107], v[172:175], v[72:75], a[104:107]\n"
"v_cvt_pk_bf16_f32 v50, v32, v33\n"
"v_mfma_f32_16x16x32_bf16 a[108:111], v[172:175], v[76:79], a[108:111]\n"
"v_exp_f32_e32 v34, v34\n"
"v_mfma_f32_16x16x32_bf16 a[112:115], v[176:179], v[64:67], a[112:115]\n"
"ds_read_b64_tr_b16 v[156:157], v200 offset:37120\n"
"ds_read_b64_tr_b16 v[158:159], v200 offset:43264\n"
"v_mfma_f32_16x16x32_bf16 a[116:119], v[176:179], v[68:71], a[116:119]\n"
"v_mfma_f32_16x16x32_bf16 a[120:123], v[176:179], v[72:75], a[120:123]\n"
"v_exp_f32_e32 v35, v35\n"
"v_mfma_f32_16x16x32_bf16 a[124:127], v[176:179], v[76:79], a[124:127]\n"
"s_waitcnt lgkmcnt(2)\n"
"v_cvt_pk_bf16_f32 v51, v34, v35\n"
"v_mfma_f32_16x16x32_bf16 a[128:131], v[148:151], v[64:67], a[128:131]\n"
"ds_read_b64_tr_b16 v[160:161], v201 offset:37120\n"
"ds_read_b64_tr_b16 v[162:163], v201 offset:43264\n"
"v_mfma_f32_16x16x32_bf16 a[132:135], v[148:151], v[68:71], a[132:135]\n"
"v_exp_f32_e32 v36, v36\n"
"v_mfma_f32_16x16x32_bf16 a[136:139], v[148:151], v[72:75], a[136:139]\n"
"v_mfma_f32_16x16x32_bf16 a[140:143], v[148:151], v[76:79], a[140:143]\n"
"v_exp_f32_e32 v37, v37\n"
"v_mfma_f32_16x16x32_bf16 a[144:147], v[152:155], v[64:67], a[144:147]\n"
"v_cvt_pk_bf16_f32 v54, v36, v37\n"
"v_mfma_f32_16x16x32_bf16 a[148:151], v[152:155], v[68:71], a[148:151]\n"
"v_mfma_f32_16x16x32_bf16 a[152:155], v[152:155], v[72:75], a[152:155]\n"
"v_exp_f32_e32 v38, v38\n"
"v_mfma_f32_16x16x32_bf16 a[156:159], v[152:155], v[76:79], a[156:159]\n"
"s_waitcnt lgkmcnt(0)\n"
"v_mfma_f32_16x16x32_bf16 a[160:163], v[156:159], v[64:67], a[160:163]\n"
"ds_read_b128 v[164:167], v196 offset:12288\n"
"ds_read_b128 v[168:171], v197 offset:12288\n"
"v_mfma_f32_16x16x32_bf16 a[164:167], v[156:159], v[68:71], a[164:167]\n"
"v_exp_f32_e32 v39, v39\n"
"v_mfma_f32_16x16x32_bf16 a[168:171], v[156:159], v[72:75], a[168:171]\n"
"v_cvt_pk_bf16_f32 v55, v38, v39\n"
"v_mfma_f32_16x16x32_bf16 a[172:175], v[156:159], v[76:79], a[172:175]\n"
"v_exp_f32_e32 v40, v40\n"
"v_mfma_f32_16x16x32_bf16 a[176:179], v[160:163], v[64:67], a[176:179]\n"
"v_mfma_f32_16x16x32_bf16 a[180:183], v[160:163], v[68:71], a[180:183]\n"
"v_mfma_f32_16x16x32_bf16 a[184:187], v[160:163], v[72:75], a[184:187]\n"
"v_exp_f32_e32 v41, v41\n"
"v_mfma_f32_16x16x32_bf16 a[188:191], v[160:163], v[76:79], a[188:191]\n"
"v_cvt_pk_bf16_f32 v58, v40, v41\n"
"v_mfma_f32_16x16x32_bf16 v[96:99], v[112:115], v[64:67], v[96:99]\n"
"ds_read_b128 v[172:175], v196 offset:12416\n"
"ds_read_b128 v[176:179], v197 offset:12416\n"
"v_mfma_f32_16x16x32_bf16 v[100:103], v[112:115], v[68:71], v[100:103]\n"
"v_exp_f32_e32 v42, v42\n"
"v_mfma_f32_16x16x32_bf16 v[104:107], v[112:115], v[72:75], v[104:107]\n"
"v_mfma_f32_16x16x32_bf16 v[108:111], v[112:115], v[76:79], v[108:111]\n"
"s_waitcnt lgkmcnt(2)\n"
"v_exp_f32_e32 v43, v43\n"
"v_mfma_f32_16x16x32_bf16 v[16:19], v[164:167], a[192:195], v[80:83]\n"
"ds_read_b128 v[148:151], v196 offset:12544\n"
"v_cvt_pk_bf16_f32 v59, v42, v43\n"
"v_mfma_f32_16x16x32_bf16 v[16:19], v[168:171], a[196:199], v[16:19]\n"
"ds_read_b128 v[152:155], v197 offset:12544\n"
"v_mfma_f32_16x16x32_bf16 v[20:23], v[164:167], a[216:219], v[84:87]\n"
"ds_read_b128 v[156:159], v196 offset:18432\n"
"v_mfma_f32_16x16x32_bf16 v[20:23], v[168:171], a[220:223], v[20:23]\n"
"ds_read_b128 v[160:163], v197 offset:18432\n"
"v_mfma_f32_16x16x32_bf16 v[24:27], v[164:167], a[240:243], v[88:91]\n"
"v_exp_f32_e32 v44, v44\n"
"v_mfma_f32_16x16x32_bf16 v[24:27], v[168:171], a[244:247], v[24:27]\n"
"v_exp_f32_e32 v45, v45\n"
"v_mfma_f32_16x16x32_bf16 v[28:31], v[164:167], v[124:127], v[92:95]\n"
"v_cvt_pk_bf16_f32 v62, v44, v45\n"
"v_mfma_f32_16x16x32_bf16 v[28:31], v[168:171], v[128:131], v[28:31]\n"
"s_waitcnt lgkmcnt(4)\n"
"v_exp_f32_e32 v46, v46\n"
"v_mfma_f32_16x16x32_bf16 v[16:19], v[172:175], a[200:203], v[16:19]\n"
"ds_read_b128 v[164:167], v196 offset:18560\n"
"v_mfma_f32_16x16x32_bf16 v[16:19], v[176:179], a[204:207], v[16:19]\n"
"ds_read_b128 v[168:171], v197 offset:18560\n"
"v_mfma_f32_16x16x32_bf16 v[20:23], v[172:175], a[224:227], v[20:23]\n"
"v_exp_f32_e32 v47, v47\n"
"v_mfma_f32_16x16x32_bf16 v[20:23], v[176:179], a[228:231], v[20:23]\n"
"v_cvt_pk_bf16_f32 v63, v46, v47\n"
"v_mfma_f32_16x16x32_bf16 v[24:27], v[172:175], a[248:251], v[24:27]\n"
"v_mfma_f32_16x16x32_bf16 v[24:27], v[176:179], a[252:255], v[24:27]\n"
"v_mfma_f32_16x16x32_bf16 v[28:31], v[172:175], v[132:135], v[28:31]\n"
"v_mfma_f32_16x16x32_bf16 v[28:31], v[176:179], v[136:139], v[28:31]\n"
"s_waitcnt lgkmcnt(4)\n"
"v_mfma_f32_16x16x32_bf16 v[16:19], v[148:151], a[208:211], v[16:19]\n"
"ds_read_b128 v[172:175], v196 offset:18688\n"
"v_mfma_f32_16x16x32_bf16 v[16:19], v[152:155], a[212:215], v[16:19]\n"
"ds_read_b128 v[176:179], v197 offset:18688\n"
"v_mfma_f32_16x16x32_bf16 v[20:23], v[148:151], a[232:235], v[20:23]\n"
"v_mfma_f32_16x16x32_bf16 v[20:23], v[152:155], a[236:239], v[20:23]\n"
"v_add_u32 v196, s48, v196\n"
"v_add_u32 v197, s48, v197\n"
"v_mfma_f32_16x16x32_bf16 v[24:27], v[148:151], v[116:119], v[24:27]\n"
"v_add_u32 v198, s48, v198\n"
"v_add_u32 v199, s48, v199\n"
"v_mfma_f32_16x16x32_bf16 v[24:27], v[152:155], v[120:123], v[24:27]\n"
"v_add_u32 v200, s48, v200\n"
"v_add_u32 v201, s48, v201\n"
"v_mfma_f32_16x16x32_bf16 v[28:31], v[148:151], v[140:143], v[28:31]\n"
"v_mfma_f32_16x16x32_bf16 v[28:31], v[152:155], v[144:147], v[28:31]\n"
"s_waitcnt lgkmcnt(4)\n"
"v_mfma_f32_16x16x32_bf16 v[32:35], v[156:159], a[192:195], v[80:83]\n"
"v_mfma_f32_16x16x32_bf16 v[32:35], v[160:163], a[196:199], v[32:35]\n"
"v_mfma_f32_16x16x32_bf16 v[36:39], v[156:159], a[216:219], v[84:87]\n"
"v_mfma_f32_16x16x32_bf16 v[36:39], v[160:163], a[220:223], v[36:39]\n"
"s_waitcnt vmcnt(12) lgkmcnt(0)\n"
"s_barrier\n"
"v_mfma_f32_16x16x32_bf16 v[40:43], v[156:159], a[240:243], v[88:91]\n"
"s_add_u32 s43, s42, 24576\n"
"v_mfma_f32_16x16x32_bf16 v[40:43], v[160:163], a[244:247], v[40:43]\n"
"v_mfma_f32_16x16x32_bf16 v[44:47], v[156:159], v[124:127], v[92:95]\n"
"v_exp_f32_e32 v16, v16\n"
"v_mfma_f32_16x16x32_bf16 v[44:47], v[160:163], v[128:131], v[44:47]\n"
"ds_read_b64_tr_b16 v[148:149], v198 offset:24576\n"
"ds_read_b64_tr_b16 v[150:151], v198 offset:30720\n"
"v_mfma_f32_16x16x32_bf16 v[32:35], v[164:167], a[200:203], v[32:35]\n"
"s_mov_b32 m0, s42\n"
"v_exp_f32_e32 v17, v17\n"
"v_mfma_f32_16x16x32_bf16 v[32:35], v[168:171], a[204:207], v[32:35]\n"
"buffer_load_dwordx4 v202, s[20:23], s44 offen offset:0 nt lds\n"
"v_cvt_pk_bf16_f32 v64, v16, v17\n"
"v_mfma_f32_16x16x32_bf16 v[36:39], v[164:167], a[224:227], v[36:39]\n"
"v_mfma_f32_16x16x32_bf16 v[36:39], v[168:171], a[228:231], v[36:39]\n"
"ds_read_b64_tr_b16 v[152:153], v199 offset:24576\n"
"ds_read_b64_tr_b16 v[154:155], v199 offset:30720\n"
"v_mfma_f32_16x16x32_bf16 v[40:43], v[164:167], a[248:251], v[40:43]\n"
"v_exp_f32_e32 v18, v18\n"
"v_mfma_f32_16x16x32_bf16 v[40:43], v[168:171], a[252:255], v[40:43]\n"
"v_exp_f32_e32 v19, v19\n"
"v_mfma_f32_16x16x32_bf16 v[44:47], v[164:167], v[132:135], v[44:47]\n"
"v_cvt_pk_bf16_f32 v65, v18, v19\n"
"v_mfma_f32_16x16x32_bf16 v[44:47], v[168:171], v[136:139], v[44:47]\n"
"ds_read_b64_tr_b16 v[156:157], v200 offset:24576\n"
"ds_read_b64_tr_b16 v[158:159], v200 offset:30720\n"
"v_mfma_f32_16x16x32_bf16 v[32:35], v[172:175], a[208:211], v[32:35]\n"
"buffer_load_dwordx4 v203, s[20:23], s44 offen offset:1024 nt lds\n"
"v_mfma_f32_16x16x32_bf16 v[32:35], v[176:179], a[212:215], v[32:35]\n"
"v_exp_f32_e32 v20, v20\n"
"v_mfma_f32_16x16x32_bf16 v[36:39], v[172:175], a[232:235], v[36:39]\n"
"v_exp_f32_e32 v21, v21\n"
"v_mfma_f32_16x16x32_bf16 v[36:39], v[176:179], a[236:239], v[36:39]\n"
"v_cvt_pk_bf16_f32 v68, v20, v21\n"
"v_mfma_f32_16x16x32_bf16 v[40:43], v[172:175], v[116:119], v[40:43]\n"
"v_mfma_f32_16x16x32_bf16 v[40:43], v[176:179], v[120:123], v[40:43]\n"
"v_exp_f32_e32 v22, v22\n"
"v_mfma_f32_16x16x32_bf16 v[44:47], v[172:175], v[140:143], v[44:47]\n"
"v_mfma_f32_16x16x32_bf16 v[44:47], v[176:179], v[144:147], v[44:47]\n"
"s_waitcnt lgkmcnt(2)\n"
"v_exp_f32_e32 v23, v23\n"
"s_sub_u32 s41, s41, 1\n"
"s_cmp_lg_u32 s41, 0\n"
"s_cbranch_scc1 L_ata%=\n"
"v_mfma_f32_16x16x32_bf16 a[0:3], v[148:151], v[48:51], a[0:3]\n"
"ds_read_b64_tr_b16 v[160:161], v201 offset:24576\n"
"ds_read_b64_tr_b16 v[162:163], v201 offset:30720\n"
"v_mfma_f32_16x16x32_bf16 a[4:7], v[148:151], v[52:55], a[4:7]\n"
"v_cvt_pk_bf16_f32 v69, v22, v23\n"
"v_mfma_f32_16x16x32_bf16 a[8:11], v[148:151], v[56:59], a[8:11]\n"
"v_exp_f32_e32 v24, v24\n"
"v_mfma_f32_16x16x32_bf16 a[12:15], v[148:151], v[60:63], a[12:15]\n"
"v_mfma_f32_16x16x32_bf16 a[16:19], v[152:155], v[48:51], a[16:19]\n"
"ds_read_b64_tr_b16 v[164:165], v198 offset:24704\n"
"ds_read_b64_tr_b16 v[166:167], v198 offset:30848\n"
"v_mfma_f32_16x16x32_bf16 a[20:23], v[152:155], v[52:55], a[20:23]\n"
"v_exp_f32_e32 v25, v25\n"
"v_mfma_f32_16x16x32_bf16 a[24:27], v[152:155], v[56:59], a[24:27]\n"
"v_cvt_pk_bf16_f32 v72, v24, v25\n"
"v_mfma_f32_16x16x32_bf16 a[28:31], v[152:155], v[60:63], a[28:31]\n"
"s_waitcnt lgkmcnt(2)\n"
"v_exp_f32_e32 v26, v26\n"
"v_mfma_f32_16x16x32_bf16 a[32:35], v[156:159], v[48:51], a[32:35]\n"
"ds_read_b64_tr_b16 v[168:169], v199 offset:24704\n"
"ds_read_b64_tr_b16 v[170:171], v199 offset:30848\n"
"v_mfma_f32_16x16x32_bf16 a[36:39], v[156:159], v[52:55], a[36:39]\n"
"v_exp_f32_e32 v27, v27\n"
"v_mfma_f32_16x16x32_bf16 a[40:43], v[156:159], v[56:59], a[40:43]\n"
"v_cvt_pk_bf16_f32 v73, v26, v27\n"
"v_mfma_f32_16x16x32_bf16 a[44:47], v[156:159], v[60:63], a[44:47]\n"
"v_mfma_f32_16x16x32_bf16 a[48:51], v[160:163], v[48:51], a[48:51]\n"
"ds_read_b64_tr_b16 v[172:173], v200 offset:24704\n"
"ds_read_b64_tr_b16 v[174:175], v200 offset:30848\n"
"v_mfma_f32_16x16x32_bf16 a[52:55], v[160:163], v[52:55], a[52:55]\n"
"v_exp_f32_e32 v28, v28\n"
"v_mfma_f32_16x16x32_bf16 a[56:59], v[160:163], v[56:59], a[56:59]\n"
"v_exp_f32_e32 v29, v29\n"
"v_mfma_f32_16x16x32_bf16 a[60:63], v[160:163], v[60:63], a[60:63]\n"
"s_waitcnt lgkmcnt(2)\n"
"v_cvt_pk_bf16_f32 v76, v28, v29\n"
"v_mfma_f32_16x16x32_bf16 a[64:67], v[164:167], v[48:51], a[64:67]\n"
"ds_read_b64_tr_b16 v[176:177], v201 offset:24704\n"
"ds_read_b64_tr_b16 v[178:179], v201 offset:30848\n"
"v_mfma_f32_16x16x32_bf16 a[68:71], v[164:167], v[52:55], a[68:71]\n"
"v_exp_f32_e32 v30, v30\n"
"v_mfma_f32_16x16x32_bf16 a[72:75], v[164:167], v[56:59], a[72:75]\n"
"v_mfma_f32_16x16x32_bf16 a[76:79], v[164:167], v[60:63], a[76:79]\n"
"v_mfma_f32_16x16x32_bf16 a[80:83], v[168:171], v[48:51], a[80:83]\n"
"ds_read_b64_tr_b16 v[148:149], v198 offset:24832\n"
"ds_read_b64_tr_b16 v[150:151], v198 offset:30976\n"
"v_mfma_f32_16x16x32_bf16 a[84:87], v[168:171], v[52:55], a[84:87]\n"
"v_exp_f32_e32 v31, v31\n"
"v_mfma_f32_16x16x32_bf16 a[88:91], v[168:171], v[56:59], a[88:91]\n"
"v_exp_f32_e32 v32, v32\n"
"v_mfma_f32_16x16x32_bf16 a[92:95], v[168:171], v[60:63], a[92:95]\n"
"s_waitcnt lgkmcnt(2)\n"
"v_cvt_pk_bf16_f32 v77, v30, v31\n"
"v_mfma_f32_16x16x32_bf16 a[96:99], v[172:175], v[48:51], a[96:99]\n"
"ds_read_b64_tr_b16 v[152:153], v199 offset:24832\n"
"ds_read_b64_tr_b16 v[154:155], v199 offset:30976\n"
"v_mfma_f32_16x16x32_bf16 a[100:103], v[172:175], v[52:55], a[100:103]\n"
"v_exp_f32_e32 v33, v33\n"
"v_mfma_f32_16x16x32_bf16 a[104:107], v[172:175], v[56:59], a[104:107]\n"
"v_cvt_pk_bf16_f32 v66, v32, v33\n"
"v_mfma_f32_16x16x32_bf16 a[108:111], v[172:175], v[60:63], a[108:111]\n"
"v_exp_f32_e32 v34, v34\n"
"v_mfma_f32_16x16x32_bf16 a[112:115], v[176:179], v[48:51], a[112:115]\n"
"ds_read_b64_tr_b16 v[156:157], v200 offset:24832\n"
"ds_read_b64_tr_b16 v[158:159], v200 offset:30976\n"
"v_mfma_f32_16x16x32_bf16 a[116:119], v[176:179], v[52:55], a[116:119]\n"
"v_mfma_f32_16x16x32_bf16 a[120:123], v[176:179], v[56:59], a[120:123]\n"
"v_exp_f32_e32 v35, v35\n"
"v_mfma_f32_16x16x32_bf16 a[124:127], v[176:179], v[60:63], a[124:127]\n"
"s_waitcnt lgkmcnt(2)\n"
"v_cvt_pk_bf16_f32 v67, v34, v35\n"
"v_mfma_f32_16x16x32_bf16 a[128:131], v[148:151], v[48:51], a[128:131]\n"
"ds_read_b64_tr_b16 v[160:161], v201 offset:24832\n"
"ds_read_b64_tr_b16 v[162:163], v201 offset:30976\n"
"v_mfma_f32_16x16x32_bf16 a[132:135], v[148:151], v[52:55], a[132:135]\n"
"v_exp_f32_e32 v36, v36\n"
"v_mfma_f32_16x16x32_bf16 a[136:139], v[148:151], v[56:59], a[136:139]\n"
"v_mfma_f32_16x16x32_bf16 a[140:143], v[148:151], v[60:63], a[140:143]\n"
"v_exp_f32_e32 v37, v37\n"
"v_mfma_f32_16x16x32_bf16 a[144:147], v[152:155], v[48:51], a[144:147]\n"
"v_cvt_pk_bf16_f32 v70, v36, v37\n"
"v_mfma_f32_16x16x32_bf16 a[148:151], v[152:155], v[52:55], a[148:151]\n"
"v_mfma_f32_16x16x32_bf16 a[152:155], v[152:155], v[56:59], a[152:155]\n"
"v_exp_f32_e32 v38, v38\n"
"v_mfma_f32_16x16x32_bf16 a[156:159], v[152:155], v[60:63], a[156:159]\n"
"s_waitcnt lgkmcnt(0)\n"
"v_mfma_f32_16x16x32_bf16 a[160:163], v[156:159], v[48:51], a[160:163]\n"
"v_mfma_f32_16x16x32_bf16 a[164:167], v[156:159], v[52:55], a[164:167]\n"
"v_exp_f32_e32 v39, v39\n"
"v_mfma_f32_16x16x32_bf16 a[168:171], v[156:159], v[56:59], a[168:171]\n"
"v_cvt_pk_bf16_f32 v71, v38, v39\n"
"v_mfma_f32_16x16x32_bf16 a[172:175], v[156:159], v[60:63], a[172:175]\n"
"v_exp_f32_e32 v40, v40\n"
"v_mfma_f32_16x16x32_bf16 a[176:179], v[160:163], v[48:51], a[176:179]\n"
"v_mfma_f32_16x16x32_bf16 a[180:183], v[160:163], v[52:55], a[180:183]\n"
"v_mfma_f32_16x16x32_bf16 a[184:187], v[160:163], v[56:59], a[184:187]\n"
"v_mfma_f32_16x16x32_bf16 a[188:191], v[160:163], v[60:63], a[188:191]\n"
"v_exp_f32_e32 v41, v41\n"
"v_mfma_f32_16x16x32_bf16 v[96:99], v[112:115], v[48:51], v[96:99]\n"
"v_mfma_f32_16x16x32_bf16 v[100:103], v[112:115], v[52:55], v[100:103]\n"
"v_exp_f32_e32 v42, v42\n"
"v_mfma_f32_16x16x32_bf16 v[104:107], v[112:115], v[56:59], v[104:107]\n"
"v_cvt_pk_bf16_f32 v74, v40, v41\n"
"v_mfma_f32_16x16x32_bf16 v[108:111], v[112:115], v[60:63], v[108:111]\n"
"v_exp_f32_e32 v43, v43\n"
"ds_read_b64_tr_b16 v[148:149], v198 offset:36864\n"
"ds_read_b64_tr_b16 v[150:151], v198 offset:43008\n"
"ds_read_b64_tr_b16 v[152:153], v199 offset:36864\n"
"ds_read_b64_tr_b16 v[154:155], v199 offset:43008\n"
"ds_read_b64_tr_b16 v[156:157], v200 offset:36864\n"
"ds_read_b64_tr_b16 v[158:159], v200 offset:43008\n"
"v_cvt_pk_bf16_f32 v75, v42, v43\n"
"s_nop 0\n"
"v_exp_f32_e32 v44, v44\n"
"s_nop 0\n"
"v_exp_f32_e32 v45, v45\n"
"s_nop 0\n"
"v_cvt_pk_bf16_f32 v78, v44, v45\n"
"s_nop 0\n"
"v_exp_f32_e32 v46, v46\n"
"s_nop 0\n"
"v_exp_f32_e32 v47, v47\n"
"s_nop 0\n"
"v_cvt_pk_bf16_f32 v79, v46, v47\n"
"s_nop 0\n"
"s_nop 1\n"
"s_waitcnt lgkmcnt(2)\n"
"v_mfma_f32_16x16x32_bf16 a[0:3], v[148:151], v[64:67], a[0:3]\n"
"ds_read_b64_tr_b16 v[160:161], v201 offset:36864\n"
"ds_read_b64_tr_b16 v[162:163], v201 offset:43008\n"
"v_mfma_f32_16x16x32_bf16 a[4:7], v[148:151], v[68:71], a[4:7]\n"
"v_mfma_f32_16x16x32_bf16 a[8:11], v[148:151], v[72:75], a[8:11]\n"
"v_mfma_f32_16x16x32_bf16 a[12:15], v[148:151], v[76:79], a[12:15]\n"
"v_mfma_f32_16x16x32_bf16 a[16:19], v[152:155], v[64:67], a[16:19]\n"
"ds_read_b64_tr_b16 v[164:165], v198 offset:36992\n"
"ds_read_b64_tr_b16 v[166:167], v198 offset:43136\n"
"v_mfma_f32_16x16x32_bf16 a[20:23], v[152:155], v[68:71], a[20:23]\n"
"v_mfma_f32_16x16x32_bf16 a[24:27], v[152:155], v[72:75], a[24:27]\n"
"v_mfma_f32_16x16x32_bf16 a[28:31], v[152:155], v[76:79], a[28:31]\n"
"s_waitcnt lgkmcnt(2)\n"
"v_mfma_f32_16x16x32_bf16 a[32:35], v[156:159], v[64:67], a[32:35]\n"
"ds_read_b64_tr_b16 v[168:169], v199 offset:36992\n"
"ds_read_b64_tr_b16 v[170:171], v199 offset:43136\n"
"v_mfma_f32_16x16x32_bf16 a[36:39], v[156:159], v[68:71], a[36:39]\n"
"v_mfma_f32_16x16x32_bf16 a[40:43], v[156:159], v[72:75], a[40:43]\n"
"v_mfma_f32_16x16x32_bf16 a[44:47], v[156:159], v[76:79], a[44:47]\n"
"v_mfma_f32_16x16x32_bf16 a[48:51], v[160:163], v[64:67], a[48:51]\n"
"ds_read_b64_tr_b16 v[172:173], v200 offset:36992\n"
"ds_read_b64_tr_b16 v[174:175], v200 offset:43136\n"
"v_mfma_f32_16x16x32_bf16 a[52:55], v[160:163], v[68:71], a[52:55]\n"
"v_mfma_f32_16x16x32_bf16 a[56:59], v[160:163], v[72:75], a[56:59]\n"
"v_mfma_f32_16x16x32_bf16 a[60:63], v[160:163], v[76:79], a[60:63]\n"
"s_waitcnt lgkmcnt(2)\n"
"v_mfma_f32_16x16x32_bf16 a[64:67], v[164:167], v[64:67], a[64:67]\n"
"ds_read_b64_tr_b16 v[176:177], v201 offset:36992\n"
"ds_read_b64_tr_b16 v[178:179], v201 offset:43136\n"
"v_mfma_f32_16x16x32_bf16 a[68:71], v[164:167], v[68:71], a[68:71]\n"
"v_mfma_f32_16x16x32_bf16 a[72:75], v[164:167], v[72:75], a[72:75]\n"
"v_mfma_f32_16x16x32_bf16 a[76:79], v[164:167], v[76:79], a[76:79]\n"
"v_mfma_f32_16x16x32_bf16 a[80:83], v[168:171], v[64:67], a[80:83]\n"
"ds_read_b64_tr_b16 v[148:149], v198 offset:37120\n"
"ds_read_b64_tr_b16 v[150:151], v198 offset:43264\n"
"v_mfma_f32_16x16x32_bf16 a[84:87], v[168:171], v[68:71], a[84:87]\n"
"v_mfma_f32_16x16x32_bf16 a[88:91], v[168:171], v[72:75], a[88:91]\n"
"v_mfma_f32_16x16x32_bf16 a[92:95], v[168:171], v[76:79], a[92:95]\n"
"s_waitcnt lgkmcnt(2)\n"
"v_mfma_f32_16x16x32_bf16 a[96:99], v[172:175], v[64:67], a[96:99]\n"
"ds_read_b64_tr_b16 v[152:153], v199 offset:37120\n"
"ds_read_b64_tr_b16 v[154:155], v199 offset:43264\n"
"v_mfma_f32_16x16x32_bf16 a[100:103], v[172:175], v[68:71], a[100:103]\n"
"v_mfma_f32_16x16x32_bf16 a[104:107], v[172:175], v[72:75], a[104:107]\n"
"v_mfma_f32_16x16x32_bf16 a[108:111], v[172:175], v[76:79], a[108:111]\n"
"v_mfma_f32_16x16x32_bf16 a[112:115], v[176:179], v[64:67], a[112:115]\n"
"ds_read_b64_tr_b16 v[156:157], v200 offset:37120\n"
"ds_read_b64_tr_b16 v[158:159], v200 offset:43264\n"
"v_mfma_f32_16x16x32_bf16 a[116:119], v[176:179], v[68:71], a[116:119]\n"
"v_mfma_f32_16x16x32_bf16 a[120:123], v[176:179], v[72:75], a[120:123]\n"
"v_mfma_f32_16x16x32_bf16 a[124:127], v[176:179], v[76:79], a[124:127]\n"
"s_waitcnt lgkmcnt(2)\n"
"v_mfma_f32_16x16x32_bf16 a[128:131], v[148:151], v[64:67], a[128:131]\n"
"ds_read_b64_tr_b16 v[160:161], v201 offset:37120\n"
"ds_read_b64_tr_b16 v[162:163], v201 offset:43264\n"
"v_mfma_f32_16x16x32_bf16 a[132:135], v[148:151], v[68:71], a[132:135]\n"
"v_mfma_f32_16x16x32_bf16 a[136:139], v[148:151], v[72:75], a[136:139]\n"
"v_mfma_f32_16x16x32_bf16 a[140:143], v[148:151], v[76:79], a[140:143]\n"
"v_mfma_f32_16x16x32_bf16 a[144:147], v[152:155], v[64:67], a[144:147]\n"
"v_mfma_f32_16x16x32_bf16 a[148:151], v[152:155], v[68:71], a[148:151]\n"
"v_mfma_f32_16x16x32_bf16 a[152:155], v[152:155], v[72:75], a[152:155]\n"
"v_mfma_f32_16x16x32_bf16 a[156:159], v[152:155], v[76:79], a[156:159]\n"
"s_waitcnt lgkmcnt(0)\n"
"v_mfma_f32_16x16x32_bf16 a[160:163], v[156:159], v[64:67], a[160:163]\n"
"v_mfma_f32_16x16x32_bf16 a[164:167], v[156:159], v[68:71], a[164:167]\n"
"v_mfma_f32_16x16x32_bf16 a[168:171], v[156:159], v[72:75], a[168:171]\n"
"v_mfma_f32_16x16x32_bf16 a[172:175], v[156:159], v[76:79], a[172:175]\n"
"v_mfma_f32_16x16x32_bf16 a[176:179], v[160:163], v[64:67], a[176:179]\n"
"v_mfma_f32_16x16x32_bf16 a[180:183], v[160:163], v[68:71], a[180:183]\n"
"v_mfma_f32_16x16x32_bf16 a[184:187], v[160:163], v[72:75], a[184:187]\n"
"v_mfma_f32_16x16x32_bf16 a[188:191], v[160:163], v[76:79], a[188:191]\n"
"v_mfma_f32_16x16x32_bf16 v[96:99], v[112:115], v[64:67], v[96:99]\n"
"v_mfma_f32_16x16x32_bf16 v[100:103], v[112:115], v[68:71], v[100:103]\n"
"v_mfma_f32_16x16x32_bf16 v[104:107], v[112:115], v[72:75], v[104:107]\n"
"v_mfma_f32_16x16x32_bf16 v[108:111], v[112:115], v[76:79], v[108:111]\n"
"L_epia%=:\n"
"s_barrier\n"
"s_cmp_ge_u32 %[blk], 240\n"
"s_cbranch_scc1 L_nowoa%=\n"
"s_and_b32 s60, %[blk], 7\n"
"s_lshr_b32 s61, %[blk], 3\n"
"s_mul_i32 s62, s61, 52\n"
"s_lshr_b32 s62, s62, 8\n"
"s_mul_i32 s63, s62, 5\n"
"s_sub_u32 s51, s61, s63\n"
"s_and_b32 s52, s60, 3\n"
"s_lshr_b32 s64, s60, 2\n"
"s_mul_i32 s64, s64, 6\n"
"s_add_u32 s50, s64, s62\n"
"s_mov_b64 s[32:33], %[wo]\n"
"s_and_b32 s33, s33, 0xffff\n"
"s_mov_b32 s34, 7864320\n"
"s_mov_b32 s35, 0x20000\n"
"v_lshrrev_b32 v5, 4, v4\n"
"v_and_b32 v6, 15, v4\n"
"v_add_u32 v7, 0, v5\n"
"v_lshlrev_b32 v7, 1, v7\n"
"v_xor_b32 v7, v7, v6\n"
"v_lshlrev_b32 v7, 4, v7\n"
"v_add_u32 v8, 0, v5\n"
"v_mul_u32_u24 v8, 1280, v8\n"
"v_add_u32 v7, v7, v8\n"
"v_subrev_u32 v170, 0, v7\n"
"v_add_u32 v7, 4, v5\n"
"v_lshlrev_b32 v7, 1, v7\n"
"v_xor_b32 v7, v7, v6\n"
"v_lshlrev_b32 v7, 4, v7\n"
"v_add_u32 v8, 4, v5\n"
"v_mul_u32_u24 v8, 1280, v8\n"
"v_add_u32 v7, v7, v8\n"
"v_subrev_u32 v171, 1024, v7\n"
"v_add_u32 v7, 0, v5\n"
"v_lshlrev_b32 v7, 1, v7\n"
"v_xor_b32 v7, v7, v6\n"
"v_lshlrev_b32 v7, 4, v7\n"
"v_add_u32 v8, 8, v5\n"
"v_mul_u32_u24 v8, 1280, v8\n"
"v_add_u32 v7, v7, v8\n"
"v_subrev_u32 v172, 2048, v7\n"
"v_add_u32 v7, 4, v5\n"
"v_lshlrev_b32 v7, 1, v7\n"
"v_xor_b32 v7, v7, v6\n"
"v_lshlrev_b32 v7, 4, v7\n"
"v_add_u32 v8, 12, v5\n"
"v_mul_u32_u24 v8, 1280, v8\n"
"v_add_u32 v7, v7, v8\n"
"v_subrev_u32 v173, 3072, v7\n"
"s_mul_i32 s43, s52, 1966080\n"
"s_mul_i32 s60, s40, 20480\n"
"s_add_u32 s43, s43, s60\n"
"s_lshl_b32 s60, s51, 8\n"
"s_add_u32 s43, s43, s60\n"
"s_lshl_b32 s46, s40, 12\n"
"s_add_u32 s46, s46, 20480\n"
"s_mov_b32 s55, 0\n"
"s_add_u32 m0, s46, 0\n"
"s_add_u32 s60, s43, 0\n"
"buffer_load_dwordx4 v170, s[32:35], s60 offen offset:0 lds\n"
"buffer_load_dwordx4 v171, s[32:35], s60 offen offset:1024 lds\n"
"buffer_load_dwordx4 v172, s[32:35], s60 offen offset:2048 lds\n"
"buffer_load_dwordx4 v173, s[32:35], s60 offen offset:3072 lds\n"
"s_add_u32 m0, s46, 36864\n"
"s_add_u32 s60, s43, 81920\n"
"buffer_load_dwordx4 v170, s[32:35], s60 offen offset:0 lds\n"
"buffer_load_dwordx4 v171, s[32:35], s60 offen offset:1024 lds\n"
"buffer_load_dwordx4 v172, s[32:35], s60 offen offset:2048 lds\n"
"buffer_load_dwordx4 v173, s[32:35], s60 offen offset:3072 lds\n"
"s_add_u32 m0, s46, 73728\n"
"s_add_u32 s60, s43, 163840\n"
"buffer_load_dwordx4 v170, s[32:35], s60 offen offset:0 lds\n"
"buffer_load_dwordx4 v171, s[32:35], s60 offen offset:1024 lds\n"
"buffer_load_dwordx4 v172, s[32:35], s60 offen offset:2048 lds\n"
"buffer_load_dwordx4 v173, s[32:35], s60 offen offset:3072 lds\n"
"s_add_u32 m0, s46, 110592\n"
"s_add_u32 s60, s43, 245760\n"
"buffer_load_dwordx4 v170, s[32:35], s60 offen offset:0 lds\n"
"buffer_load_dwordx4 v171, s[32:35], s60 offen offset:1024 lds\n"
"buffer_load_dwordx4 v172, s[32:35], s60 offen offset:2048 lds\n"
"buffer_load_dwordx4 v173, s[32:35], s60 offen offset:3072 lds\n"
"s_add_u32 s43, s43, 327680\n"
"L_nowoa%=:\n"
"s_nop 7\n"
"v_rcp_f32 v230, v96\n"
"v_rcp_f32 v232, v100\n"
"v_rcp_f32 v234, v104\n"
"v_rcp_f32 v236, v108\n"
"s_mov_b32 s60, 0x53800000\n"
"s_mov_b64 s[58:59], 0\n"
"v_cmp_nge_f32 vcc, s60, v96\n"
"s_nop 1\n"
"s_or_b64 s[58:59], s[58:59], vcc\n"
"v_cmp_nge_f32 vcc, s60, v100\n"
"s_nop 1\n"
"s_or_b64 s[58:59], s[58:59], vcc\n"
"v_cmp_nge_f32 vcc, s60, v104\n"
"s_nop 1\n"
"s_or_b64 s[58:59], s[58:59], vcc\n"
"v_cmp_nge_f32 vcc, s60, v108\n"
"s_nop 1\n"
"s_or_b64 s[58:59], s[58:59], vcc\n"
"v_mov_b32 v231, v230\n"
"v_mov_b32 v233, v232\n"
"v_mov_b32 v235, v234\n"
"v_mov_b32 v237, v236\n"
"v_lshlrev_b32 v5, 4, v4\n"
"s_mul_i32 s60, s40, 786432\n"
"s_mul_i32 s61, s66, 6144\n"
"s_add_u32 s60, s60, s61\n"
"v_mov_b32 v112, 0\n"
"v_mov_b32 v80, 0\n"
"v_mov_b32 v113, 0\n"
"v_mov_b32 v81, 0\n"
"v_mov_b32 v114, 0\n"
"v_mov_b32 v82, 0\n"
"v_mov_b32 v115, 0\n"
"v_mov_b32 v83, 0\n"
"s_add_u32 s62, s60, 0\n"
"s_add_u32 s63, s62, 4096\n"
"v_accvgpr_read_b32 v16, a0\n"
"v_accvgpr_read_b32 v17, a1\n"
"v_accvgpr_read_b32 v18, a2\n"
"v_accvgpr_read_b32 v19, a3\n"
"v_accvgpr_read_b32 v20, a16\n"
"v_accvgpr_read_b32 v21, a17\n"
"v_accvgpr_read_b32 v22, a18\n"
"v_accvgpr_read_b32 v23, a19\n"
"v_pk_mul_f32 v[16:17], v[16:17], v[230:231]\n"
"v_pk_mul_f32 v[18:19], v[18:19], v[230:231]\n"
"v_pk_mul_f32 v[20:21], v[20:21], v[230:231]\n"
"v_pk_mul_f32 v[22:23], v[22:23], v[230:231]\n"
"v_cvt_pk_bf16_f32 v24, v16, v17\n"
"v_cvt_pk_bf16_f32 v25, v18, v19\n"
"v_cvt_pk_bf16_f32 v26, v20, v21\n"
"v_cvt_pk_bf16_f32 v27, v22, v23\n"
"buffer_store_dwordx4 v[24:27], v5, s[36:39], s62 offen offset:0 sc1\n"
"v_accvgpr_read_b32 v28, a32\n"
"v_accvgpr_read_b32 v29, a33\n"
"v_accvgpr_read_b32 v30, a34\n"
"v_accvgpr_read_b32 v31, a35\n"
"v_accvgpr_read_b32 v32, a48\n"
"v_accvgpr_read_b32 v33, a49\n"
"v_accvgpr_read_b32 v34, a50\n"
"v_accvgpr_read_b32 v35, a51\n"
"v_mfma_f32_16x16x32_bf16 v[80:83], v[112:115], v[24:27], v[80:83]\n"
"v_pk_mul_f32 v[28:29], v[28:29], v[230:231]\n"
"v_pk_mul_f32 v[30:31], v[30:31], v[230:231]\n"
"v_pk_mul_f32 v[32:33], v[32:33], v[230:231]\n"
"v_pk_mul_f32 v[34:35], v[34:35], v[230:231]\n"
"v_cvt_pk_bf16_f32 v36, v28, v29\n"
"v_cvt_pk_bf16_f32 v37, v30, v31\n"
"v_cvt_pk_bf16_f32 v38, v32, v33\n"
"v_cvt_pk_bf16_f32 v39, v34, v35\n"
"buffer_store_dwordx4 v[36:39], v5, s[36:39], s62 offen offset:1024 sc1\n"
"v_accvgpr_read_b32 v40, a64\n"
"v_accvgpr_read_b32 v41, a65\n"
"v_accvgpr_read_b32 v42, a66\n"
"v_accvgpr_read_b32 v43, a67\n"
"v_accvgpr_read_b32 v44, a80\n"
"v_accvgpr_read_b32 v45, a81\n"
"v_accvgpr_read_b32 v46, a82\n"
"v_accvgpr_read_b32 v47, a83\n"
"v_mfma_f32_16x16x32_bf16 v[80:83], v[112:115], v[36:39], v[80:83]\n"
"v_pk_mul_f32 v[40:41], v[40:41], v[230:231]\n"
"v_pk_mul_f32 v[42:43], v[42:43], v[230:231]\n"
"v_pk_mul_f32 v[44:45], v[44:45], v[230:231]\n"
"v_pk_mul_f32 v[46:47], v[46:47], v[230:231]\n"
"v_cvt_pk_bf16_f32 v48, v40, v41\n"
"v_cvt_pk_bf16_f32 v49, v42, v43\n"
"v_cvt_pk_bf16_f32 v50, v44, v45\n"
"v_cvt_pk_bf16_f32 v51, v46, v47\n"
"buffer_store_dwordx4 v[48:51], v5, s[36:39], s62 offen offset:2048 sc1\n"
"v_accvgpr_read_b32 v52, a96\n"
"v_accvgpr_read_b32 v53, a97\n"
"v_accvgpr_read_b32 v54, a98\n"
"v_accvgpr_read_b32 v55, a99\n"
"v_accvgpr_read_b32 v56, a112\n"
"v_accvgpr_read_b32 v57, a113\n"
"v_accvgpr_read_b32 v58, a114\n"
"v_accvgpr_read_b32 v59, a115\n"
"v_mfma_f32_16x16x32_bf16 v[80:83], v[112:115], v[48:51], v[80:83]\n"
"v_pk_mul_f32 v[52:53], v[52:53], v[230:231]\n"
"v_pk_mul_f32 v[54:55], v[54:55], v[230:231]\n"
"v_pk_mul_f32 v[56:57], v[56:57], v[230:231]\n"
"v_pk_mul_f32 v[58:59], v[58:59], v[230:231]\n"
"v_cvt_pk_bf16_f32 v60, v52, v53\n"
"v_cvt_pk_bf16_f32 v61, v54, v55\n"
"v_cvt_pk_bf16_f32 v62, v56, v57\n"
"v_cvt_pk_bf16_f32 v63, v58, v59\n"
"buffer_store_dwordx4 v[60:63], v5, s[36:39], s62 offen offset:3072 sc1\n"
"v_accvgpr_read_b32 v16, a128\n"
"v_accvgpr_read_b32 v17, a129\n"
"v_accvgpr_read_b32 v18, a130\n"
"v_accvgpr_read_b32 v19, a131\n"
"v_accvgpr_read_b32 v20, a144\n"
"v_accvgpr_read_b32 v21, a145\n"
"v_accvgpr_read_b32 v22, a146\n"
"v_accvgpr_read_b32 v23, a147\n"
"v_mfma_f32_16x16x32_bf16 v[80:83], v[112:115], v[60:63], v[80:83]\n"
"v_pk_mul_f32 v[16:17], v[16:17], v[230:231]\n"
"v_pk_mul_f32 v[18:19], v[18:19], v[230:231]\n"
"v_pk_mul_f32 v[20:21], v[20:21], v[230:231]\n"
"v_pk_mul_f32 v[22:23], v[22:23], v[230:231]\n"
"v_cvt_pk_bf16_f32 v24, v16, v17\n"
"v_cvt_pk_bf16_f32 v25, v18, v19\n"
"v_cvt_pk_bf16_f32 v26, v20, v21\n"
"v_cvt_pk_bf16_f32 v27, v22, v23\n"
"buffer_store_dwordx4 v[24:27], v5, s[36:39], s63 offen offset:0 sc1\n"
"v_accvgpr_read_b32 v28, a160\n"
"v_accvgpr_read_b32 v29, a161\n"
"v_accvgpr_read_b32 v30, a162\n"
"v_accvgpr_read_b32 v31, a163\n"
"v_accvgpr_read_b32 v32, a176\n"
"v_accvgpr_read_b32 v33, a177\n"
"v_accvgpr_read_b32 v34, a178\n"
"v_accvgpr_read_b32 v35, a179\n"
"v_mfma_f32_16x16x32_bf16 v[80:83], v[112:115], v[24:27], v[80:83]\n"
"v_pk_mul_f32 v[28:29], v[28:29], v[230:231]\n"
"v_pk_mul_f32 v[30:31], v[30:31], v[230:231]\n"
"v_pk_mul_f32 v[32:33], v[32:33], v[230:231]\n"
"v_pk_mul_f32 v[34:35], v[34:35], v[230:231]\n"
"v_cvt_pk_bf16_f32 v36, v28, v29\n"
"v_cvt_pk_bf16_f32 v37, v30, v31\n"
"v_cvt_pk_bf16_f32 v38, v32, v33\n"
"v_cvt_pk_bf16_f32 v39, v34, v35\n"
"buffer_store_dwordx4 v[36:39], v5, s[36:39], s63 offen offset:1024 sc1\n"
"s_add_u32 s62, s60, 196608\n"
"s_add_u32 s63, s62, 4096\n"
"v_accvgpr_read_b32 v40, a4\n"
"v_accvgpr_read_b32 v41, a5\n"
"v_accvgpr_read_b32 v42, a6\n"
"v_accvgpr_read_b32 v43, a7\n"
"v_accvgpr_read_b32 v44, a20\n"
"v_accvgpr_read_b32 v45, a21\n"
"v_accvgpr_read_b32 v46, a22\n"
"v_accvgpr_read_b32 v47, a23\n"
"v_mfma_f32_16x16x32_bf16 v[80:83], v[112:115], v[36:39], v[80:83]\n"
"v_pk_mul_f32 v[40:41], v[40:41], v[232:233]\n"
"v_pk_mul_f32 v[42:43], v[42:43], v[232:233]\n"
"v_pk_mul_f32 v[44:45], v[44:45], v[232:233]\n"
"v_pk_mul_f32 v[46:47], v[46:47], v[232:233]\n"
"v_cvt_pk_bf16_f32 v48, v40, v41\n"
"v_cvt_pk_bf16_f32 v49, v42, v43\n"
"v_cvt_pk_bf16_f32 v50, v44, v45\n"
"v_cvt_pk_bf16_f32 v51, v46, v47\n"
"buffer_store_dwordx4 v[48:51], v5, s[36:39], s62 offen offset:0 sc1\n"
"v_accvgpr_read_b32 v52, a36\n"
"v_accvgpr_read_b32 v53, a37\n"
"v_accvgpr_read_b32 v54, a38\n"
"v_accvgpr_read_b32 v55, a39\n"
"v_accvgpr_read_b32 v56, a52\n"
"v_accvgpr_read_b32 v57, a53\n"
"v_accvgpr_read_b32 v58, a54\n"
"v_accvgpr_read_b32 v59, a55\n"
"v_mfma_f32_16x16x32_bf16 v[80:83], v[112:115], v[48:51], v[80:83]\n"
"v_pk_mul_f32 v[52:53], v[52:53], v[232:233]\n"
"v_pk_mul_f32 v[54:55], v[54:55], v[232:233]\n"
"v_pk_mul_f32 v[56:57], v[56:57], v[232:233]\n"
"v_pk_mul_f32 v[58:59], v[58:59], v[232:233]\n"
"v_cvt_pk_bf16_f32 v60, v52, v53\n"
"v_cvt_pk_bf16_f32 v61, v54, v55\n"
"v_cvt_pk_bf16_f32 v62, v56, v57\n"
"v_cvt_pk_bf16_f32 v63, v58, v59\n"
"buffer_store_dwordx4 v[60:63], v5, s[36:39], s62 offen offset:1024 sc1\n"
"v_accvgpr_read_b32 v16, a68\n"
"v_accvgpr_read_b32 v17, a69\n"
"v_accvgpr_read_b32 v18, a70\n"
"v_accvgpr_read_b32 v19, a71\n"
"v_accvgpr_read_b32 v20, a84\n"
"v_accvgpr_read_b32 v21, a85\n"
"v_accvgpr_read_b32 v22, a86\n"
"v_accvgpr_read_b32 v23, a87\n"
"v_mfma_f32_16x16x32_bf16 v[80:83], v[112:115], v[60:63], v[80:83]\n"
"v_pk_mul_f32 v[16:17], v[16:17], v[232:233]\n"
"v_pk_mul_f32 v[18:19], v[18:19], v[232:233]\n"
"v_pk_mul_f32 v[20:21], v[20:21], v[232:233]\n"
"v_pk_mul_f32 v[22:23], v[22:23], v[232:233]\n"
"v_cvt_pk_bf16_f32 v24, v16, v17\n"
"v_cvt_pk_bf16_f32 v25, v18, v19\n"
"v_cvt_pk_bf16_f32 v26, v20, v21\n"
"v_cvt_pk_bf16_f32 v27, v22, v23\n"
"buffer_store_dwordx4 v[24:27], v5, s[36:39], s62 offen offset:2048 sc1\n"
"v_accvgpr_read_b32 v28, a100\n"
"v_accvgpr_read_b32 v29, a101\n"
"v_accvgpr_read_b32 v30, a102\n"
"v_accvgpr_read_b32 v31, a103\n"
"v_accvgpr_read_b32 v32, a116\n"
"v_accvgpr_read_b32 v33, a117\n"
"v_accvgpr_read_b32 v34, a118\n"
"v_accvgpr_read_b32 v35, a119\n"
"v_mfma_f32_16x16x32_bf16 v[80:83], v[112:115], v[24:27], v[80:83]\n"
"v_pk_mul_f32 v[28:29], v[28:29], v[232:233]\n"
"v_pk_mul_f32 v[30:31], v[30:31], v[232:233]\n"
"v_pk_mul_f32 v[32:33], v[32:33], v[232:233]\n"
"v_pk_mul_f32 v[34:35], v[34:35], v[232:233]\n"
"v_cvt_pk_bf16_f32 v36, v28, v29\n"
"v_cvt_pk_bf16_f32 v37, v30, v31\n"
"v_cvt_pk_bf16_f32 v38, v32, v33\n"
"v_cvt_pk_bf16_f32 v39, v34, v35\n"
"buffer_store_dwordx4 v[36:39], v5, s[36:39], s62 offen offset:3072 sc1\n"
"v_accvgpr_read_b32 v40, a132\n"
"v_accvgpr_read_b32 v41, a133\n"
"v_accvgpr_read_b32 v42, a134\n"
"v_accvgpr_read_b32 v43, a135\n"
"v_accvgpr_read_b32 v44, a148\n"
"v_accvgpr_read_b32 v45, a149\n"
"v_accvgpr_read_b32 v46, a150\n"
"v_accvgpr_read_b32 v47, a151\n"
"v_mfma_f32_16x16x32_bf16 v[80:83], v[112:115], v[36:39], v[80:83]\n"
"v_pk_mul_f32 v[40:41], v[40:41], v[232:233]\n"
"v_pk_mul_f32 v[42:43], v[42:43], v[232:233]\n"
"v_pk_mul_f32 v[44:45], v[44:45], v[232:233]\n"
"v_pk_mul_f32 v[46:47], v[46:47], v[232:233]\n"
"v_cvt_pk_bf16_f32 v48, v40, v41\n"
"v_cvt_pk_bf16_f32 v49, v42, v43\n"
"v_cvt_pk_bf16_f32 v50, v44, v45\n"
"v_cvt_pk_bf16_f32 v51, v46, v47\n"
"buffer_store_dwordx4 v[48:51], v5, s[36:39], s63 offen offset:0 sc1\n"
"v_accvgpr_read_b32 v52, a164\n"
"v_accvgpr_read_b32 v53, a165\n"
"v_accvgpr_read_b32 v54, a166\n"
"v_accvgpr_read_b32 v55, a167\n"
"v_accvgpr_read_b32 v56, a180\n"
"v_accvgpr_read_b32 v57, a181\n"
"v_accvgpr_read_b32 v58, a182\n"
"v_accvgpr_read_b32 v59, a183\n"
"v_mfma_f32_16x16x32_bf16 v[80:83], v[112:115], v[48:51], v[80:83]\n"
"v_pk_mul_f32 v[52:53], v[52:53], v[232:233]\n"
"v_pk_mul_f32 v[54:55], v[54:55], v[232:233]\n"
"v_pk_mul_f32 v[56:57], v[56:57], v[232:233]\n"
"v_pk_mul_f32 v[58:59], v[58:59], v[232:233]\n"
"v_cvt_pk_bf16_f32 v60, v52, v53\n"
"v_cvt_pk_bf16_f32 v61, v54, v55\n"
"v_cvt_pk_bf16_f32 v62, v56, v57\n"
"v_cvt_pk_bf16_f32 v63, v58, v59\n"
"buffer_store_dwordx4 v[60:63], v5, s[36:39], s63 offen offset:1024 sc1\n"
"s_add_u32 s62, s60, 393216\n"
"s_add_u32 s63, s62, 4096\n"
"v_accvgpr_read_b32 v16, a8\n"
"v_accvgpr_read_b32 v17, a9\n"
"v_accvgpr_read_b32 v18, a10\n"
"v_accvgpr_read_b32 v19, a11\n"
"v_accvgpr_read_b32 v20, a24\n"
"v_accvgpr_read_b32 v21, a25\n"
"v_accvgpr_read_b32 v22, a26\n"
"v_accvgpr_read_b32 v23, a27\n"
"v_mfma_f32_16x16x32_bf16 v[80:83], v[112:115], v[60:63], v[80:83]\n"
"v_pk_mul_f32 v[16:17], v[16:17], v[234:235]\n"
"v_pk_mul_f32 v[18:19], v[18:19], v[234:235]\n"
"v_pk_mul_f32 v[20:21], v[20:21], v[234:235]\n"
"v_pk_mul_f32 v[22:23], v[22:23], v[234:235]\n"
"v_cvt_pk_bf16_f32 v24, v16, v17\n"
"v_cvt_pk_bf16_f32 v25, v18, v19\n"
"v_cvt_pk_bf16_f32 v26, v20, v21\n"
"v_cvt_pk_bf16_f32 v27, v22, v23\n"
"buffer_store_dwordx4 v[24:27], v5, s[36:39], s62 offen offset:0 sc1\n"
"v_accvgpr_read_b32 v28, a40\n"
"v_accvgpr_read_b32 v29, a41\n"
"v_accvgpr_read_b32 v30, a42\n"
"v_accvgpr_read_b32 v31, a43\n"
"v_accvgpr_read_b32 v32, a56\n"
"v_accvgpr_read_b32 v33, a57\n"
"v_accvgpr_read_b32 v34, a58\n"
"v_accvgpr_read_b32 v35, a59\n"
"v_mfma_f32_16x16x32_bf16 v[80:83], v[112:115], v[24:27], v[80:83]\n"
"v_pk_mul_f32 v[28:29], v[28:29], v[234:235]\n"
"v_pk_mul_f32 v[30:31], v[30:31], v[234:235]\n"
"v_pk_mul_f32 v[32:33], v[32:33], v[234:235]\n"
"v_pk_mul_f32 v[34:35], v[34:35], v[234:235]\n"
"v_cvt_pk_bf16_f32 v36, v28, v29\n"
"v_cvt_pk_bf16_f32 v37, v30, v31\n"
"v_cvt_pk_bf16_f32 v38, v32, v33\n"
"v_cvt_pk_bf16_f32 v39, v34, v35\n"
"buffer_store_dwordx4 v[36:39], v5, s[36:39], s62 offen offset:1024 sc1\n"
"v_accvgpr_read_b32 v40, a72\n"
"v_accvgpr_read_b32 v41, a73\n"
"v_accvgpr_read_b32 v42, a74\n"
"v_accvgpr_read_b32 v43, a75\n"
"v_accvgpr_read_b32 v44, a88\n"
"v_accvgpr_read_b32 v45, a89\n"
"v_accvgpr_read_b32 v46, a90\n"
"v_accvgpr_read_b32 v47, a91\n"
"v_mfma_f32_16x16x32_bf16 v[80:83], v[112:115], v[36:39], v[80:83]\n"
"v_pk_mul_f32 v[40:41], v[40:41], v[234:235]\n"
"v_pk_mul_f32 v[42:43], v[42:43], v[234:235]\n"
"v_pk_mul_f32 v[44:45], v[44:45], v[234:235]\n"
"v_pk_mul_f32 v[46:47], v[46:47], v[234:235]\n"
"v_cvt_pk_bf16_f32 v48, v40, v41\n"
"v_cvt_pk_bf16_f32 v49, v42, v43\n"
"v_cvt_pk_bf16_f32 v50, v44, v45\n"
"v_cvt_pk_bf16_f32 v51, v46, v47\n"
"buffer_store_dwordx4 v[48:51], v5, s[36:39], s62 offen offset:2048 sc1\n"
"v_accvgpr_read_b32 v52, a104\n"
"v_accvgpr_read_b32 v53, a105\n"
"v_accvgpr_read_b32 v54, a106\n"
"v_accvgpr_read_b32 v55, a107\n"
"v_accvgpr_read_b32 v56, a120\n"
"v_accvgpr_read_b32 v57, a121\n"
"v_accvgpr_read_b32 v58, a122\n"
"v_accvgpr_read_b32 v59, a123\n"
"v_mfma_f32_16x16x32_bf16 v[80:83], v[112:115], v[48:51], v[80:83]\n"
"v_pk_mul_f32 v[52:53], v[52:53], v[234:235]\n"
"v_pk_mul_f32 v[54:55], v[54:55], v[234:235]\n"
"v_pk_mul_f32 v[56:57], v[56:57], v[234:235]\n"
"v_pk_mul_f32 v[58:59], v[58:59], v[234:235]\n"
"v_cvt_pk_bf16_f32 v60, v52, v53\n"
"v_cvt_pk_bf16_f32 v61, v54, v55\n"
"v_cvt_pk_bf16_f32 v62, v56, v57\n"
"v_cvt_pk_bf16_f32 v63, v58, v59\n"
"buffer_store_dwordx4 v[60:63], v5, s[36:39], s62 offen offset:3072 sc1\n"
"v_accvgpr_read_b32 v16, a136\n"
"v_accvgpr_read_b32 v17, a137\n"
"v_accvgpr_read_b32 v18, a138\n"
"v_accvgpr_read_b32 v19, a139\n"
"v_accvgpr_read_b32 v20, a152\n"
"v_accvgpr_read_b32 v21, a153\n"
"v_accvgpr_read_b32 v22, a154\n"
"v_accvgpr_read_b32 v23, a155\n"
"v_mfma_f32_16x16x32_bf16 v[80:83], v[112:115], v[60:63], v[80:83]\n"
"v_pk_mul_f32 v[16:17], v[16:17], v[234:235]\n"
"v_pk_mul_f32 v[18:19], v[18:19], v[234:235]\n"
"v_pk_mul_f32 v[20:21], v[20:21], v[234:235]\n"
"v_pk_mul_f32 v[22:23], v[22:23], v[234:235]\n"
"v_cvt_pk_bf16_f32 v24, v16, v17\n"
"v_cvt_pk_bf16_f32 v25, v18, v19\n"
"v_cvt_pk_bf16_f32 v26, v20, v21\n"
"v_cvt_pk_bf16_f32 v27, v22, v23\n"
"buffer_store_dwordx4 v[24:27], v5, s[36:39], s63 offen offset:0 sc1\n"
"v_accvgpr_read_b32 v28, a168\n"
"v_accvgpr_read_b32 v29, a169\n"
"v_accvgpr_read_b32 v30, a170\n"
"v_accvgpr_read_b32 v31, a171\n"
"v_accvgpr_read_b32 v32, a184\n"
"v_accvgpr_read_b32 v33, a185\n"
"v_accvgpr_read_b32 v34, a186\n"
"v_accvgpr_read_b32 v35, a187\n"
"v_mfma_f32_16x16x32_bf16 v[80:83], v[112:115], v[24:27], v[80:83]\n"
"v_pk_mul_f32 v[28:29], v[28:29], v[234:235]\n"
"v_pk_mul_f32 v[30:31], v[30:31], v[234:235]\n"
"v_pk_mul_f32 v[32:33], v[32:33], v[234:235]\n"
"v_pk_mul_f32 v[34:35], v[34:35], v[234:235]\n"
"v_cvt_pk_bf16_f32 v36, v28, v29\n"
"v_cvt_pk_bf16_f32 v37, v30, v31\n"
"v_cvt_pk_bf16_f32 v38, v32, v33\n"
"v_cvt_pk_bf16_f32 v39, v34, v35\n"
"buffer_store_dwordx4 v[36:39], v5, s[36:39], s63 offen offset:1024 sc1\n"
"s_add_u32 s62, s60, 589824\n"
"s_add_u32 s63, s62, 4096\n"
"v_accvgpr_read_b32 v40, a12\n"
"v_accvgpr_read_b32 v41, a13\n"
"v_accvgpr_read_b32 v42, a14\n"
"v_accvgpr_read_b32 v43, a15\n"
"v_accvgpr_read_b32 v44, a28\n"
"v_accvgpr_read_b32 v45, a29\n"
"v_accvgpr_read_b32 v46, a30\n"
"v_accvgpr_read_b32 v47, a31\n"
"v_mfma_f32_16x16x32_bf16 v[80:83], v[112:115], v[36:39], v[80:83]\n"
"v_pk_mul_f32 v[40:41], v[40:41], v[236:237]\n"
"v_pk_mul_f32 v[42:43], v[42:43], v[236:237]\n"
"v_pk_mul_f32 v[44:45], v[44:45], v[236:237]\n"
"v_pk_mul_f32 v[46:47], v[46:47], v[236:237]\n"
"v_cvt_pk_bf16_f32 v48, v40, v41\n"
"v_cvt_pk_bf16_f32 v49, v42, v43\n"
"v_cvt_pk_bf16_f32 v50, v44, v45\n"
"v_cvt_pk_bf16_f32 v51, v46, v47\n"
"buffer_store_dwordx4 v[48:51], v5, s[36:39], s62 offen offset:0 sc1\n"
"v_accvgpr_read_b32 v52, a44\n"
"v_accvgpr_read_b32 v53, a45\n"
"v_accvgpr_read_b32 v54, a46\n"
"v_accvgpr_read_b32 v55, a47\n"
"v_accvgpr_read_b32 v56, a60\n"
"v_accvgpr_read_b32 v57, a61\n"
"v_accvgpr_read_b32 v58, a62\n"
"v_accvgpr_read_b32 v59, a63\n"
"v_mfma_f32_16x16x32_bf16 v[80:83], v[112:115], v[48:51], v[80:83]\n"
"v_pk_mul_f32 v[52:53], v[52:53], v[236:237]\n"
"v_pk_mul_f32 v[54:55], v[54:55], v[236:237]\n"
"v_pk_mul_f32 v[56:57], v[56:57], v[236:237]\n"
"v_pk_mul_f32 v[58:59], v[58:59], v[236:237]\n"
"v_cvt_pk_bf16_f32 v60, v52, v53\n"
"v_cvt_pk_bf16_f32 v61, v54, v55\n"
"v_cvt_pk_bf16_f32 v62, v56, v57\n"
"v_cvt_pk_bf16_f32 v63, v58, v59\n"
"buffer_store_dwordx4 v[60:63], v5, s[36:39], s62 offen offset:1024 sc1\n"
"v_accvgpr_read_b32 v16, a76\n"
"v_accvgpr_read_b32 v17, a77\n"
"v_accvgpr_read_b32 v18, a78\n"
"v_accvgpr_read_b32 v19, a79\n"
"v_accvgpr_read_b32 v20, a92\n"
"v_accvgpr_read_b32 v21, a93\n"
"v_accvgpr_read_b32 v22, a94\n"
"v_accvgpr_read_b32 v23, a95\n"
"v_mfma_f32_16x16x32_bf16 v[80:83], v[112:115], v[60:63], v[80:83]\n"
"v_pk_mul_f32 v[16:17], v[16:17], v[236:237]\n"
"v_pk_mul_f32 v[18:19], v[18:19], v[236:237]\n"
"v_pk_mul_f32 v[20:21], v[20:21], v[236:237]\n"
"v_pk_mul_f32 v[22:23], v[22:23], v[236:237]\n"
"v_cvt_pk_bf16_f32 v24, v16, v17\n"
"v_cvt_pk_bf16_f32 v25, v18, v19\n"
"v_cvt_pk_bf16_f32 v26, v20, v21\n"
"v_cvt_pk_bf16_f32 v27, v22, v23\n"
"buffer_store_dwordx4 v[24:27], v5, s[36:39], s62 offen offset:2048 sc1\n"
"v_accvgpr_read_b32 v28, a108\n"
"v_accvgpr_read_b32 v29, a109\n"
"v_accvgpr_read_b32 v30, a110\n"
"v_accvgpr_read_b32 v31, a111\n"
"v_accvgpr_read_b32 v32, a124\n"
"v_accvgpr_read_b32 v33, a125\n"
"v_accvgpr_read_b32 v34, a126\n"
"v_accvgpr_read_b32 v35, a127\n"
"v_mfma_f32_16x16x32_bf16 v[80:83], v[112:115], v[24:27], v[80:83]\n"
"v_pk_mul_f32 v[28:29], v[28:29], v[236:237]\n"
"v_pk_mul_f32 v[30:31], v[30:31], v[236:237]\n"
"v_pk_mul_f32 v[32:33], v[32:33], v[236:237]\n"
"v_pk_mul_f32 v[34:35], v[34:35], v[236:237]\n"
"v_cvt_pk_bf16_f32 v36, v28, v29\n"
"v_cvt_pk_bf16_f32 v37, v30, v31\n"
"v_cvt_pk_bf16_f32 v38, v32, v33\n"
"v_cvt_pk_bf16_f32 v39, v34, v35\n"
"buffer_store_dwordx4 v[36:39], v5, s[36:39], s62 offen offset:3072 sc1\n"
"v_accvgpr_read_b32 v40, a140\n"
"v_accvgpr_read_b32 v41, a141\n"
"v_accvgpr_read_b32 v42, a142\n"
"v_accvgpr_read_b32 v43, a143\n"
"v_accvgpr_read_b32 v44, a156\n"
"v_accvgpr_read_b32 v45, a157\n"
"v_accvgpr_read_b32 v46, a158\n"
"v_accvgpr_read_b32 v47, a159\n"
"v_mfma_f32_16x16x32_bf16 v[80:83], v[112:115], v[36:39], v[80:83]\n"
"v_pk_mul_f32 v[40:41], v[40:41], v[236:237]\n"
"v_pk_mul_f32 v[42:43], v[42:43], v[236:237]\n"
"v_pk_mul_f32 v[44:45], v[44:45], v[236:237]\n"
"v_pk_mul_f32 v[46:47], v[46:47], v[236:237]\n"
"v_cvt_pk_bf16_f32 v48, v40, v41\n"
"v_cvt_pk_bf16_f32 v49, v42, v43\n"
"v_cvt_pk_bf16_f32 v50, v44, v45\n"
"v_cvt_pk_bf16_f32 v51, v46, v47\n"
"buffer_store_dwordx4 v[48:51], v5, s[36:39], s63 offen offset:0 sc1\n"
"v_accvgpr_read_b32 v52, a172\n"
"v_accvgpr_read_b32 v53, a173\n"
"v_accvgpr_read_b32 v54, a174\n"
"v_accvgpr_read_b32 v55, a175\n"
"v_accvgpr_read_b32 v56, a188\n"
"v_accvgpr_read_b32 v57, a189\n"
"v_accvgpr_read_b32 v58, a190\n"
"v_accvgpr_read_b32 v59, a191\n"
"v_mfma_f32_16x16x32_bf16 v[80:83], v[112:115], v[48:51], v[80:83]\n"
"v_pk_mul_f32 v[52:53], v[52:53], v[236:237]\n"
"v_pk_mul_f32 v[54:55], v[54:55], v[236:237]\n"
"v_pk_mul_f32 v[56:57], v[56:57], v[236:237]\n"
"v_pk_mul_f32 v[58:59], v[58:59], v[236:237]\n"
"v_cvt_pk_bf16_f32 v60, v52, v53\n"
"v_cvt_pk_bf16_f32 v61, v54, v55\n"
"v_cvt_pk_bf16_f32 v62, v56, v57\n"
"v_cvt_pk_bf16_f32 v63, v58, v59\n"
"buffer_store_dwordx4 v[60:63], v5, s[36:39], s63 offen offset:1024 sc1\n"
"s_nop 1\n"
"v_mfma_f32_16x16x32_bf16 v[80:83], v[112:115], v[60:63], v[80:83]\n"
"s_nop 7\n"
"v_cmp_u_f32 vcc, v80, v80\n"
"s_nop 1\n"
"s_or_b64 s[58:59], s[58:59], vcc\n"
"s_or_b32 s58, s58, s59\n"
"L_post%=:\n"
"s_lshl_b32 s60, s40, 2\n"
"s_add_u32 s60, s60, 163824\n"
"v_mov_b32 v5, s60\n"
"v_mov_b32 v6, s58\n"
"ds_write_b32 v5, v6\n"
"s_waitcnt lgkmcnt(0)\n"
"s_barrier\n"
"v_mov_b32 v5, 163824\n"
"ds_read_b128 v[8:11], v5\n"
"s_waitcnt lgkmcnt(0)\n"
"v_or_b32 v8, v8, v9\n"
"v_or_b32 v10, v10, v11\n"
"v_or_b32 v8, v8, v10\n"
"s_nop 0\n"
"v_readfirstlane_b32 s60, v8\n"
"s_nop 3\n"
"s_cmp_eq_u32 s60, 0\n"
"s_cbranch_scc1 L_ok%=\n"
"s_cmp_lg_u32 s91, 0\n"
"s_cbranch_scc1 L_ok%=\n"
"s_waitcnt vmcnt(0)\n"
"s_mov_b32 s91, 1\n"
"s_mul_i32 s89, s40, 6144\n"
"s_mov_b32 s22, 393216\n"
"s_mov_b32 s26, 393216\n"
"v_and_b32 v5, 15, v4\n"
"v_lshrrev_b32 v6, 4, v4\n"
"v_bfe_u32 v8, v5, 2, 1\n"
"v_bfe_u32 v9, v5, 3, 1\n"
"v_xor_b32 v8, v8, v9\n"
"v_lshl_or_b32 v9, v8, 2, v9\n"
"v_bfe_u32 v8, v5, 1, 1\n"
"v_lshl_or_b32 v7, v8, 1, v9\n"
"v_mul_u32_u24 v10, 384, v5\n"
"v_lshlrev_b32 v11, 7, v5\n"
"v_add_u32 v8, 0, v6\n"
"v_xor_b32 v8, v8, v7\n"
"v_lshl_add_u32 v196, v8, 4, v10\n"
"v_add_u32 v8, 4, v6\n"
"v_xor_b32 v8, v8, v7\n"
"v_lshl_add_u32 v197, v8, 4, v10\n"
"v_bfe_u32 v8, v4, 2, 2\n"
"v_and_b32 v9, 3, v4\n"
"v_lshl_add_u32 v10, v6, 2, v8\n"
"v_bfe_u32 v11, v10, 2, 1\n"
"v_bfe_u32 v12, v10, 3, 1\n"
"v_xor_b32 v11, v11, v12\n"
"v_lshl_or_b32 v12, v11, 2, v12\n"
"v_bfe_u32 v11, v10, 1, 1\n"
"v_lshl_or_b32 v7, v11, 1, v12\n"
"v_mul_u32_u24 v10, 384, v10\n"
"v_and_b32 v11, 1, v9\n"
"v_lshl_add_u32 v10, v11, 3, v10\n"
"v_lshrrev_b32 v11, 1, v9\n"
"v_add_u32 v12, 0, v11\n"
"v_xor_b32 v12, v12, v7\n"
"v_lshl_add_u32 v198, v12, 4, v10\n"
"v_add_u32 v12, 2, v11\n"
"v_xor_b32 v12, v12, v7\n"
"v_lshl_add_u32 v199, v12, 4, v10\n"
"v_add_u32 v12, 4, v11\n"
"v_xor_b32 v12, v12, v7\n"
"v_lshl_add_u32 v200, v12, 4, v10\n"
"v_add_u32 v12, 6, v11\n"
"v_xor_b32 v12, v12, v7\n"
"v_lshl_add_u32 v201, v12, 4, v10\n"
"v_mov_b32 v5, 0\n"
"v_accvgpr_write_b32 a0, v5\n"
"v_accvgpr_write_b32 a1, v5\n"
"v_accvgpr_write_b32 a2, v5\n"
"v_accvgpr_write_b32 a3, v5\n"
"v_accvgpr_write_b32 a4, v5\n"
"v_accvgpr_write_b32 a5, v5\n"
"v_accvgpr_write_b32 a6, v5\n"
"v_accvgpr_write_b32 a7, v5\n"
"v_accvgpr_write_b32 a8, v5\n"
"v_accvgpr_write_b32 a9, v5\n"
"v_accvgpr_write_b32 a10, v5\n"
"v_accvgpr_write_b32 a11, v5\n"
"v_accvgpr_write_b32 a12, v5\n"
"v_accvgpr_write_b32 a13, v5\n"
"v_accvgpr_write_b32 a14, v5\n"
"v_accvgpr_write_b32 a15, v5\n"
"v_accvgpr_write_b32 a16, v5\n"
"v_accvgpr_write_b32 a17, v5\n"
"v_accvgpr_write_b32 a18, v5\n"
"v_accvgpr_write_b32 a19, v5\n"
"v_accvgpr_write_b32 a20, v5\n"
"v_accvgpr_write_b32 a21, v5\n"
"v_accvgpr_write_b32 a22, v5\n"
"v_accvgpr_write_b32 a23, v5\n"
"v_accvgpr_write_b32 a24, v5\n"
"v_accvgpr_write_b32 a25, v5\n"
"v_accvgpr_write_b32 a26, v5\n"
"v_accvgpr_write_b32 a27, v5\n"
"v_accvgpr_write_b32 a28, v5\n"
"v_accvgpr_write_b32 a29, v5\n"
"v_accvgpr_write_b32 a30, v5\n"
"v_accvgpr_write_b32 a31, v5\n"
"v_accvgpr_write_b32 a32, v5\n"
"v_accvgpr_write_b32 a33, v5\n"
"v_accvgpr_write_b32 a34, v5\n"
"v_accvgpr_write_b32 a35, v5\n"
"v_accvgpr_write_b32 a36, v5\n"
"v_accvgpr_write_b32 a37, v5\n"
"v_accvgpr_write_b32 a38, v5\n"
"v_accvgpr_write_b32 a39, v5\n"
"v_accvgpr_write_b32 a40, v5\n"
"v_accvgpr_write_b32 a41, v5\n"
"v_accvgpr_write_b32 a42, v5\n"
"v_accvgpr_write_b32 a43, v5\n"
"v_accvgpr_write_b32 a44, v5\n"
"v_accvgpr_write_b32 a45, v5\n"
"v_accvgpr_write_b32 a46, v5\n"
"v_accvgpr_write_b32 a47, v5\n"
"v_accvgpr_write_b32 a48, v5\n"
"v_accvgpr_write_b32 a49, v5\n"
"v_accvgpr_write_b32 a50, v5\n"
"v_accvgpr_write_b32 a51, v5\n"
"v_accvgpr_write_b32 a52, v5\n"
"v_accvgpr_write_b32 a53, v5\n"
"v_accvgpr_write_b32 a54, v5\n"
"v_accvgpr_write_b32 a55, v5\n"
"v_accvgpr_write_b32 a56, v5\n"
"v_accvgpr_write_b32 a57, v5\n"
"v_accvgpr_write_b32 a58, v5\n"
"v_accvgpr_write_b32 a59, v5\n"
"v_accvgpr_write_b32 a60, v5\n"
"v_accvgpr_write_b32 a61, v5\n"
"v_accvgpr_write_b32 a62, v5\n"
"v_accvgpr_write_b32 a63, v5\n"
"v_accvgpr_write_b32 a64, v5\n"
"v_accvgpr_write_b32 a65, v5\n"
"v_accvgpr_write_b32 a66, v5\n"
"v_accvgpr_write_b32 a67, v5\n"
"v_accvgpr_write_b32 a68, v5\n"
"v_accvgpr_write_b32 a69, v5\n"
"v_accvgpr_write_b32 a70, v5\n"
"v_accvgpr_write_b32 a71, v5\n"
"v_accvgpr_write_b32 a72, v5\n"
"v_accvgpr_write_b32 a73, v5\n"
"v_accvgpr_write_b32 a74, v5\n"
"v_accvgpr_write_b32 a75, v5\n"
"v_accvgpr_write_b32 a76, v5\n"
"v_accvgpr_write_b32 a77, v5\n"
"v_accvgpr_write_b32 a78, v5\n"
"v_accvgpr_write_b32 a79, v5\n"
"v_accvgpr_write_b32 a80, v5\n"
"v_accvgpr_write_b32 a81, v5\n"
"v_accvgpr_write_b32 a82, v5\n"
"v_accvgpr_write_b32 a83, v5\n"
"v_accvgpr_write_b32 a84, v5\n"
"v_accvgpr_write_b32 a85, v5\n"
"v_accvgpr_write_b32 a86, v5\n"
"v_accvgpr_write_b32 a87, v5\n"
"v_accvgpr_write_b32 a88, v5\n"
"v_accvgpr_write_b32 a89, v5\n"
"v_accvgpr_write_b32 a90, v5\n"
"v_accvgpr_write_b32 a91, v5\n"
"v_accvgpr_write_b32 a92, v5\n"
"v_accvgpr_write_b32 a93, v5\n"
"v_accvgpr_write_b32 a94, v5\n"
"v_accvgpr_write_b32 a95, v5\n"
"v_accvgpr_write_b32 a96, v5\n"
"v_accvgpr_write_b32 a97, v5\n"
"v_accvgpr_write_b32 a98, v5\n"
"v_accvgpr_write_b32 a99, v5\n"
"v_accvgpr_write_b32 a100, v5\n"
"v_accvgpr_write_b32 a101, v5\n"
"v_accvgpr_write_b32 a102, v5\n"
"v_accvgpr_write_b32 a103, v5\n"
"v_accvgpr_write_b32 a104, v5\n"
"v_accvgpr_write_b32 a105, v5\n"
"v_accvgpr_write_b32 a106, v5\n"
"v_accvgpr_write_b32 a107, v5\n"
"v_accvgpr_write_b32 a108, v5\n"
"v_accvgpr_write_b32 a109, v5\n"
"v_accvgpr_write_b32 a110, v5\n"
"v_accvgpr_write_b32 a111, v5\n"
"v_accvgpr_write_b32 a112, v5\n"
"v_accvgpr_write_b32 a113, v5\n"
"v_accvgpr_write_b32 a114, v5\n"
"v_accvgpr_write_b32 a115, v5\n"
"v_accvgpr_write_b32 a116, v5\n"
"v_accvgpr_write_b32 a117, v5\n"
"v_accvgpr_write_b32 a118, v5\n"
"v_accvgpr_write_b32 a119, v5\n"
"v_accvgpr_write_b32 a120, v5\n"
"v_accvgpr_write_b32 a121, v5\n"
"v_accvgpr_write_b32 a122, v5\n"
"v_accvgpr_write_b32 a123, v5\n"
"v_accvgpr_write_b32 a124, v5\n"
"v_accvgpr_write_b32 a125, v5\n"
"v_accvgpr_write_b32 a126, v5\n"
"v_accvgpr_write_b32 a127, v5\n"
"v_accvgpr_write_b32 a128, v5\n"
"v_accvgpr_write_b32 a129, v5\n"
"v_accvgpr_write_b32 a130, v5\n"
"v_accvgpr_write_b32 a131, v5\n"
"v_accvgpr_write_b32 a132, v5\n"
"v_accvgpr_write_b32 a133, v5\n"
"v_accvgpr_write_b32 a134, v5\n"
"v_accvgpr_write_b32 a135, v5\n"
"v_accvgpr_write_b32 a136, v5\n"
"v_accvgpr_write_b32 a137, v5\n"
"v_accvgpr_write_b32 a138, v5\n"
"v_accvgpr_write_b32 a139, v5\n"
"v_accvgpr_write_b32 a140, v5\n"
"v_accvgpr_write_b32 a141, v5\n"
"v_accvgpr_write_b32 a142, v5\n"
"v_accvgpr_write_b32 a143, v5\n"
"v_accvgpr_write_b32 a144, v5\n"
"v_accvgpr_write_b32 a145, v5\n"
"v_accvgpr_write_b32 a146, v5\n"
"v_accvgpr_write_b32 a147, v5\n"
"v_accvgpr_write_b32 a148, v5\n"
"v_accvgpr_write_b32 a149, v5\n"
"v_accvgpr_write_b32 a150, v5\n"
"v_accvgpr_write_b32 a151, v5\n"
"v_accvgpr_write_b32 a152, v5\n"
"v_accvgpr_write_b32 a153, v5\n"
"v_accvgpr_write_b32 a154, v5\n"
"v_accvgpr_write_b32 a155, v5\n"
"v_accvgpr_write_b32 a156, v5\n"
"v_accvgpr_write_b32 a157, v5\n"
"v_accvgpr_write_b32 a158, v5\n"
"v_accvgpr_write_b32 a159, v5\n"
"v_accvgpr_write_b32 a160, v5\n"
"v_accvgpr_write_b32 a161, v5\n"
"v_accvgpr_write_b32 a162, v5\n"
"v_accvgpr_write_b32 a163, v5\n"
"v_accvgpr_write_b32 a164, v5\n"
"v_accvgpr_write_b32 a165, v5\n"
"v_accvgpr_write_b32 a166, v5\n"
"v_accvgpr_write_b32 a167, v5\n"
"v_accvgpr_write_b32 a168, v5\n"
"v_accvgpr_write_b32 a169, v5\n"
"v_accvgpr_write_b32 a170, v5\n"
"v_accvgpr_write_b32 a171, v5\n"
"v_accvgpr_write_b32 a172, v5\n"
"v_accvgpr_write_b32 a173, v5\n"
"v_accvgpr_write_b32 a174, v5\n"
"v_accvgpr_write_b32 a175, v5\n"
"v_accvgpr_write_b32 a176, v5\n"
"v_accvgpr_write_b32 a177, v5\n"
"v_accvgpr_write_b32 a178, v5\n"
"v_accvgpr_write_b32 a179, v5\n"
"v_accvgpr_write_b32 a180, v5\n"
"v_accvgpr_write_b32 a181, v5\n"
"v_accvgpr_write_b32 a182, v5\n"
"v_accvgpr_write_b32 a183, v5\n"
"v_accvgpr_write_b32 a184, v5\n"
"v_accvgpr_write_b32 a185, v5\n"
"v_accvgpr_write_b32 a186, v5\n"
"v_accvgpr_write_b32 a187, v5\n"
"v_accvgpr_write_b32 a188, v5\n"
"v_accvgpr_write_b32 a189, v5\n"
"v_accvgpr_write_b32 a190, v5\n"
"v_accvgpr_write_b32 a191, v5\n"
"v_mov_b32 v232, 0xff800000\n"
"v_mov_b32 v96, 0\n"
"v_mov_b32 v97, 0\n"
"v_mov_b32 v98, 0\n"
"v_mov_b32 v99, 0\n"
"v_mov_b32 v233, 0xff800000\n"
"v_mov_b32 v100, 0\n"
"v_mov_b32 v101, 0\n"
"v_mov_b32 v102, 0\n"
"v_mov_b32 v103, 0\n"
"v_mov_b32 v234, 0xff800000\n"
"v_mov_b32 v104, 0\n"
"v_mov_b32 v105, 0\n"
"v_mov_b32 v106, 0\n"
"v_mov_b32 v107, 0\n"
"v_mov_b32 v235, 0xff800000\n"
"v_mov_b32 v108, 0\n"
"v_mov_b32 v109, 0\n"
"v_mov_b32 v110, 0\n"
"v_mov_b32 v111, 0\n"
"v_mov_b32 v112, 0x3f803f80\n"
"v_mov_b32 v113, 0x3f803f80\n"
"v_mov_b32 v114, 0x3f803f80\n"
"v_mov_b32 v115, 0x3f803f80\n"
"s_mov_b32 s41, 0\n"
"L_sl%=:\n"
"s_and_b32 s60, s41, 1\n"
"s_cmp_lg_u32 s60, 0\n"
"s_cbranch_scc1 L_sl_noload%=\n"
"s_barrier\n"
"s_lshr_b32 s44, s41, 1\n"
"s_mul_i32 s44, s44, 24576\n"
"s_add_u32 s44, s44, s89\n"
"s_add_u32 s61, s89, 24576\n"
"s_mov_b32 m0, s89\n"
"s_nop 0\n"
"buffer_load_dwordx4 v202, s[20:23], s44 offen offset:0 lds\n"
"buffer_load_dwordx4 v203, s[20:23], s44 offen offset:1024 lds\n"
"buffer_load_dwordx4 v204, s[20:23], s44 offen offset:2048 lds\n"
"buffer_load_dwordx4 v205, s[20:23], s44 offen offset:3072 lds\n"
"s_add_u32 m0, s89, 4096\n"
"s_nop 0\n"
"buffer_load_dwordx4 v206, s[20:23], s44 offen offset:0 lds\n"
"buffer_load_dwordx4 v207, s[20:23], s44 offen offset:1024 lds\n"
"s_mov_b32 m0, s61\n"
"s_nop 0\n"
"buffer_load_dwordx4 v202, s[24:27], s44 offen offset:0 lds\n"
"buffer_load_dwordx4 v203, s[24:27], s44 offen offset:1024 lds\n"
"buffer_load_dwordx4 v204, s[24:27], s44 offen offset:2048 lds\n"
"buffer_load_dwordx4 v205, s[24:27], s44 offen offset:3072 lds\n"
"s_add_u32 m0, s61, 4096\n"
"s_nop 0\n"
"buffer_load_dwordx4 v206, s[24:27], s44 offen offset:0 lds\n"
"buffer_load_dwordx4 v207, s[24:27], s44 offen offset:1024 lds\n"
"s_waitcnt vmcnt(0)\n"
"s_barrier\n"
"L_sl_noload%=:\n"
"s_mul_i32 s60, s60, 12288\n"
"v_add_u32 v208, s60, v196\n"
"v_add_u32 v209, s60, v197\n"
"v_add_u32 v210, s60, v198\n"
"v_add_u32 v211, s60, v199\n"
"v_add_u32 v212, s60, v200\n"
"v_add_u32 v213, s60, v201\n"
"ds_read_b128 v[148:151], v208 offset:0\n"
"ds_read_b128 v[152:155], v209 offset:0\n"
"ds_read_b128 v[156:159], v208 offset:128\n"
"ds_read_b128 v[160:163], v209 offset:128\n"
"ds_read_b128 v[164:167], v208 offset:256\n"
"ds_read_b128 v[168:171], v209 offset:256\n"
"ds_read_b128 v[172:175], v208 offset:6144\n"
"ds_read_b128 v[176:179], v209 offset:6144\n"
"ds_read_b128 v[180:183], v208 offset:6272\n"
"ds_read_b128 v[184:187], v209 offset:6272\n"
"ds_read_b128 v[188:191], v208 offset:6400\n"
"ds_read_b128 v[192:195], v209 offset:6400\n"
"s_waitcnt lgkmcnt(0)\n"
"v_mfma_f32_16x16x32_bf16 v[16:19], v[148:151], a[192:195], 0\n"
"v_mfma_f32_16x16x32_bf16 v[20:23], v[148:151], a[216:219], 0\n"
"v_mfma_f32_16x16x32_bf16 v[24:27], v[148:151], a[240:243], 0\n"
"v_mfma_f32_16x16x32_bf16 v[28:31], v[148:151], v[124:127], 0\n"
"v_mfma_f32_16x16x32_bf16 v[16:19], v[152:155], a[196:199], v[16:19]\n"
"v_mfma_f32_16x16x32_bf16 v[20:23], v[152:155], a[220:223], v[20:23]\n"
"v_mfma_f32_16x16x32_bf16 v[24:27], v[152:155], a[244:247], v[24:27]\n"
"v_mfma_f32_16x16x32_bf16 v[28:31], v[152:155], v[128:131], v[28:31]\n"
"v_mfma_f32_16x16x32_bf16 v[16:19], v[156:159], a[200:203], v[16:19]\n"
"v_mfma_f32_16x16x32_bf16 v[20:23], v[156:159], a[224:227], v[20:23]\n"
"v_mfma_f32_16x16x32_bf16 v[24:27], v[156:159], a[248:251], v[24:27]\n"
"v_mfma_f32_16x16x32_bf16 v[28:31], v[156:159], v[132:135], v[28:31]\n"
"v_mfma_f32_16x16x32_bf16 v[16:19], v[160:163], a[204:207], v[16:19]\n"
"v_mfma_f32_16x16x32_bf16 v[20:23], v[160:163], a[228:231], v[20:23]\n"
"v_mfma_f32_16x16x32_bf16 v[24:27], v[160:163], a[252:255], v[24:27]\n"
"v_mfma_f32_16x16x32_bf16 v[28:31], v[160:163], v[136:139], v[28:31]\n"
"v_mfma_f32_16x16x32_bf16 v[16:19], v[164:167], a[208:211], v[16:19]\n"
"v_mfma_f32_16x16x32_bf16 v[20:23], v[164:167], a[232:235], v[20:23]\n"
"v_mfma_f32_16x16x32_bf16 v[24:27], v[164:167], v[116:119], v[24:27]\n"
"v_mfma_f32_16x16x32_bf16 v[28:31], v[164:167], v[140:143], v[28:31]\n"
"v_mfma_f32_16x16x32_bf16 v[16:19], v[168:171], a[212:215], v[16:19]\n"
"v_mfma_f32_16x16x32_bf16 v[20:23], v[168:171], a[236:239], v[20:23]\n"
"v_mfma_f32_16x16x32_bf16 v[24:27], v[168:171], v[120:123], v[24:27]\n"
"v_mfma_f32_16x16x32_bf16 v[28:31], v[168:171], v[144:147], v[28:31]\n"
"v_mfma_f32_16x16x32_bf16 v[32:35], v[172:175], a[192:195], 0\n"
"v_mfma_f32_16x16x32_bf16 v[36:39], v[172:175], a[216:219], 0\n"
"v_mfma_f32_16x16x32_bf16 v[40:43], v[172:175], a[240:243], 0\n"
"v_mfma_f32_16x16x32_bf16 v[44:47], v[172:175], v[124:127], 0\n"
"v_mfma_f32_16x16x32_bf16 v[32:35], v[176:179], a[196:199], v[32:35]\n"
"v_mfma_f32_16x16x32_bf16 v[36:39], v[176:179], a[220:223], v[36:39]\n"
"v_mfma_f32_16x16x32_bf16 v[40:43], v[176:179], a[244:247], v[40:43]\n"
"v_mfma_f32_16x16x32_bf16 v[44:47], v[176:179], v[128:131], v[44:47]\n"
"v_mfma_f32_16x16x32_bf16 v[32:35], v[180:183], a[200:203], v[32:35]\n"
"v_mfma_f32_16x16x32_bf16 v[36:39], v[180:183], a[224:227], v[36:39]\n"
"v_mfma_f32_16x16x32_bf16 v[40:43], v[180:183], a[248:251], v[40:43]\n"
"v_mfma_f32_16x16x32_bf16 v[44:47], v[180:183], v[132:135], v[44:47]\n"
"v_mfma_f32_16x16x32_bf16 v[32:35], v[184:187], a[204:207], v[32:35]\n"
"v_mfma_f32_16x16x32_bf16 v[36:39], v[184:187], a[228:231], v[36:39]\n"
"v_mfma_f32_16x16x32_bf16 v[40:43], v[184:187], a[252:255], v[40:43]\n"
"v_mfma_f32_16x16x32_bf16 v[44:47], v[184:187], v[136:139], v[44:47]\n"
"v_mfma_f32_16x16x32_bf16 v[32:35], v[188:191], a[208:211], v[32:35]\n"
"v_mfma_f32_16x16x32_bf16 v[36:39], v[188:191], a[232:235], v[36:39]\n"
"v_mfma_f32_16x16x32_bf16 v[40:43], v[188:191], v[116:119], v[40:43]\n"
"v_mfma_f32_16x16x32_bf16 v[44:47], v[188:191], v[140:143], v[44:47]\n"
"v_mfma_f32_16x16x32_bf16 v[32:35], v[192:195], a[212:215], v[32:35]\n"
"v_mfma_f32_16x16x32_bf16 v[36:39], v[192:195], a[236:239], v[36:39]\n"
"v_mfma_f32_16x16x32_bf16 v[40:43], v[192:195], v[120:123], v[40:43]\n"
"v_mfma_f32_16x16x32_bf16 v[44:47], v[192:195], v[144:147], v[44:47]\n"
"s_nop 7\n"
"v_max3_f32 v240, v16, v17, v18\n"
"v_max3_f32 v240, v240, v19, v32\n"
"v_max3_f32 v240, v240, v33, v34\n"
"v_max_f32 v240, v240, v35\n"
"v_max3_f32 v241, v20, v21, v22\n"
"v_max3_f32 v241, v241, v23, v36\n"
"v_max3_f32 v241, v241, v37, v38\n"
"v_max_f32 v241, v241, v39\n"
"v_max3_f32 v242, v24, v25, v26\n"
"v_max3_f32 v242, v242, v27, v40\n"
"v_max3_f32 v242, v242, v41, v42\n"
"v_max_f32 v242, v242, v43\n"
"v_max3_f32 v243, v28, v29, v30\n"
"v_max3_f32 v243, v243, v31, v44\n"
"v_max3_f32 v243, v243, v45, v46\n"
"v_max_f32 v243, v243, v47\n"
"ds_swizzle_b32 v244, v240 offset:0x401F\n"
"ds_swizzle_b32 v245, v241 offset:0x401F\n"
"ds_swizzle_b32 v246, v242 offset:0x401F\n"
"ds_swizzle_b32 v247, v243 offset:0x401F\n"
"s_waitcnt lgkmcnt(0)\n"
"v_max_f32 v240, v240, v244\n"
"v_mov_b32 v244, v240\n"
"v_max_f32 v241, v241, v245\n"
"v_mov_b32 v245, v241\n"
"v_max_f32 v242, v242, v246\n"
"v_mov_b32 v246, v242\n"
"v_max_f32 v243, v243, v247\n"
"v_mov_b32 v247, v243\n"
"s_nop 1\n"
"v_permlane32_swap_b32 v240, v244\n"
"v_permlane32_swap_b32 v241, v245\n"
"v_permlane32_swap_b32 v242, v246\n"
"v_permlane32_swap_b32 v243, v247\n"
"v_max_f32 v240, v240, v244\n"
"v_max_f32 v240, v240, v232\n"
"v_sub_f32 v236, v232, v240\n"
"v_exp_f32_e32 v236, v236\n"
"v_mov_b32 v232, v240\n"
"v_max_f32 v241, v241, v245\n"
"v_max_f32 v241, v241, v233\n"
"v_sub_f32 v237, v233, v241\n"
"v_exp_f32_e32 v237, v237\n"
"v_mov_b32 v233, v241\n"
"v_max_f32 v242, v242, v246\n"
"v_max_f32 v242, v242, v234\n"
"v_sub_f32 v238, v234, v242\n"
"v_exp_f32_e32 v238, v238\n"
"v_mov_b32 v234, v242\n"
"v_max_f32 v243, v243, v247\n"
"v_max_f32 v243, v243, v235\n"
"v_sub_f32 v239, v235, v243\n"
"v_exp_f32_e32 v239, v239\n"
"v_mov_b32 v235, v243\n"
"v_sub_f32 v16, v16, v232\n"
"v_sub_f32 v17, v17, v232\n"
"v_sub_f32 v18, v18, v232\n"
"v_sub_f32 v19, v19, v232\n"
"v_sub_f32 v32, v32, v232\n"
"v_sub_f32 v33, v33, v232\n"
"v_sub_f32 v34, v34, v232\n"
"v_sub_f32 v35, v35, v232\n"
"v_sub_f32 v20, v20, v233\n"
"v_sub_f32 v21, v21, v233\n"
"v_sub_f32 v22, v22, v233\n"
"v_sub_f32 v23, v23, v233\n"
"v_sub_f32 v36, v36, v233\n"
"v_sub_f32 v37, v37, v233\n"
"v_sub_f32 v38, v38, v233\n"
"v_sub_f32 v39, v39, v233\n"
"v_sub_f32 v24, v24, v234\n"
"v_sub_f32 v25, v25, v234\n"
"v_sub_f32 v26, v26, v234\n"
"v_sub_f32 v27, v27, v234\n"
"v_sub_f32 v40, v40, v234\n"
"v_sub_f32 v41, v41, v234\n"
"v_sub_f32 v42, v42, v234\n"
"v_sub_f32 v43, v43, v234\n"
"v_sub_f32 v28, v28, v235\n"
"v_sub_f32 v29, v29, v235\n"
"v_sub_f32 v30, v30, v235\n"
"v_sub_f32 v31, v31, v235\n"
"v_sub_f32 v44, v44, v235\n"
"v_sub_f32 v45, v45, v235\n"
"v_sub_f32 v46, v46, v235\n"
"v_sub_f32 v47, v47, v235\n"
"v_exp_f32_e32 v16, v16\n"
"v_exp_f32_e32 v17, v17\n"
"v_exp_f32_e32 v18, v18\n"
"v_exp_f32_e32 v19, v19\n"
"v_exp_f32_e32 v32, v32\n"
"v_exp_f32_e32 v33, v33\n"
"v_exp_f32_e32 v34, v34\n"
"v_exp_f32_e32 v35, v35\n"
"v_exp_f32_e32 v20, v20\n"
"v_exp_f32_e32 v21, v21\n"
"v_exp_f32_e32 v22, v22\n"
"v_exp_f32_e32 v23, v23\n"
"v_exp_f32_e32 v36, v36\n"
"v_exp_f32_e32 v37, v37\n"
"v_exp_f32_e32 v38, v38\n"
"v_exp_f32_e32 v39, v39\n"
"v_exp_f32_e32 v24, v24\n"
"v_exp_f32_e32 v25, v25\n"
"v_exp_f32_e32 v26, v26\n"
"v_exp_f32_e32 v27, v27\n"
"v_exp_f32_e32 v40, v40\n"
"v_exp_f32_e32 v41, v41\n"
"v_exp_f32_e32 v42, v42\n"
"v_exp_f32_e32 v43, v43\n"
"v_exp_f32_e32 v28, v28\n"
"v_exp_f32_e32 v29, v29\n"
"v_exp_f32_e32 v30, v30\n"
"v_exp_f32_e32 v31, v31\n"
"v_exp_f32_e32 v44, v44\n"
"v_exp_f32_e32 v45, v45\n"
"v_exp_f32_e32 v46, v46\n"
"v_exp_f32_e32 v47, v47\n"
"s_nop 0\n"
"v_cvt_pk_bf16_f32 v48, v16, v17\n"
"v_cvt_pk_bf16_f32 v49, v18, v19\n"
"v_cvt_pk_bf16_f32 v50, v32, v33\n"
"v_cvt_pk_bf16_f32 v51, v34, v35\n"
"v_cvt_pk_bf16_f32 v52, v20, v21\n"
"v_cvt_pk_bf16_f32 v53, v22, v23\n"
"v_cvt_pk_bf16_f32 v54, v36, v37\n"
"v_cvt_pk_bf16_f32 v55, v38, v39\n"
"v_cvt_pk_bf16_f32 v56, v24, v25\n"
"v_cvt_pk_bf16_f32 v57, v26, v27\n"
"v_cvt_pk_bf16_f32 v58, v40, v41\n"
"v_cvt_pk_bf16_f32 v59, v42, v43\n"
"v_cvt_pk_bf16_f32 v60, v28, v29\n"
"v_cvt_pk_bf16_f32 v61, v30, v31\n"
"v_cvt_pk_bf16_f32 v62, v44, v45\n"
"v_cvt_pk_bf16_f32 v63, v46, v47\n"
"v_accvgpr_read_b32 v240, a0\n"
"v_accvgpr_read_b32 v241, a1\n"
"v_accvgpr_read_b32 v242, a2\n"
"v_accvgpr_read_b32 v243, a3\n"
"v_mul_f32 v240, v240, v236\n"
"v_mul_f32 v241, v241, v236\n"
"v_mul_f32 v242, v242, v236\n"
"v_mul_f32 v243, v243, v236\n"
"v_accvgpr_write_b32 a0, v240\n"
"v_accvgpr_write_b32 a1, v241\n"
"v_accvgpr_write_b32 a2, v242\n"
"v_accvgpr_write_b32 a3, v243\n"
"v_accvgpr_read_b32 v240, a4\n"
"v_accvgpr_read_b32 v241, a5\n"
"v_accvgpr_read_b32 v242, a6\n"
"v_accvgpr_read_b32 v243, a7\n"
"v_mul_f32 v240, v240, v237\n"
"v_mul_f32 v241, v241, v237\n"
"v_mul_f32 v242, v242, v237\n"
"v_mul_f32 v243, v243, v237\n"
"v_accvgpr_write_b32 a4, v240\n"
"v_accvgpr_write_b32 a5, v241\n"
"v_accvgpr_write_b32 a6, v242\n"
"v_accvgpr_write_b32 a7, v243\n"
"v_accvgpr_read_b32 v240, a8\n"
"v_accvgpr_read_b32 v241, a9\n"
"v_accvgpr_read_b32 v242, a10\n"
"v_accvgpr_read_b32 v243, a11\n"
"v_mul_f32 v240, v240, v238\n"
"v_mul_f32 v241, v241, v238\n"
"v_mul_f32 v242, v242, v238\n"
"v_mul_f32 v243, v243, v238\n"
"v_accvgpr_write_b32 a8, v240\n"
"v_accvgpr_write_b32 a9, v241\n"
"v_accvgpr_write_b32 a10, v242\n"
"v_accvgpr_write_b32 a11, v243\n"
"v_accvgpr_read_b32 v240, a12\n"
"v_accvgpr_read_b32 v241, a13\n"
"v_accvgpr_read_b32 v242, a14\n"
"v_accvgpr_read_b32 v243, a15\n"
"v_mul_f32 v240, v240, v239\n"
"v_mul_f32 v241, v241, v239\n"
"v_mul_f32 v242, v242, v239\n"
"v_mul_f32 v243, v243, v239\n"
"v_accvgpr_write_b32 a12, v240\n"
"v_accvgpr_write_b32 a13, v241\n"
"v_accvgpr_write_b32 a14, v242\n"
"v_accvgpr_write_b32 a15, v243\n"
"v_accvgpr_read_b32 v240, a16\n"
"v_accvgpr_read_b32 v241, a17\n"
"v_accvgpr_read_b32 v242, a18\n"
"v_accvgpr_read_b32 v243, a19\n"
"v_mul_f32 v240, v240, v236\n"
"v_mul_f32 v241, v241, v236\n"
"v_mul_f32 v242, v242, v236\n"
"v_mul_f32 v243, v243, v236\n"
"v_accvgpr_write_b32 a16, v240\n"
"v_accvgpr_write_b32 a17, v241\n"
"v_accvgpr_write_b32 a18, v242\n"
"v_accvgpr_write_b32 a19, v243\n"
"v_accvgpr_read_b32 v240, a20\n"
"v_accvgpr_read_b32 v241, a21\n"
"v_accvgpr_read_b32 v242, a22\n"
"v_accvgpr_read_b32 v243, a23\n"
"v_mul_f32 v240, v240, v237\n"
"v_mul_f32 v241, v241, v237\n"
"v_mul_f32 v242, v242, v237\n"
"v_mul_f32 v243, v243, v237\n"
"v_accvgpr_write_b32 a20, v240\n"
"v_accvgpr_write_b32 a21, v241\n"
"v_accvgpr_write_b32 a22, v242\n"
"v_accvgpr_write_b32 a23, v243\n"
"v_accvgpr_read_b32 v240, a24\n"
"v_accvgpr_read_b32 v241, a25\n"
"v_accvgpr_read_b32 v242, a26\n"
"v_accvgpr_read_b32 v243, a27\n"
"v_mul_f32 v240, v240, v238\n"
"v_mul_f32 v241, v241, v238\n"
"v_mul_f32 v242, v242, v238\n"
"v_mul_f32 v243, v243, v238\n"
"v_accvgpr_write_b32 a24, v240\n"
"v_accvgpr_write_b32 a25, v241\n"
"v_accvgpr_write_b32 a26, v242\n"
"v_accvgpr_write_b32 a27, v243\n"
"v_accvgpr_read_b32 v240, a28\n"
"v_accvgpr_read_b32 v241, a29\n"
"v_accvgpr_read_b32 v242, a30\n"
"v_accvgpr_read_b32 v243, a31\n"
"v_mul_f32 v240, v240, v239\n"
"v_mul_f32 v241, v241, v239\n"
"v_mul_f32 v242, v242, v239\n"
"v_mul_f32 v243, v243, v239\n"
"v_accvgpr_write_b32 a28, v240\n"
"v_accvgpr_write_b32 a29, v241\n"
"v_accvgpr_write_b32 a30, v242\n"
"v_accvgpr_write_b32 a31, v243\n"
"v_accvgpr_read_b32 v240, a32\n"
"v_accvgpr_read_b32 v241, a33\n"
"v_accvgpr_read_b32 v242, a34\n"
"v_accvgpr_read_b32 v243, a35\n"
"v_mul_f32 v240, v240, v236\n"
"v_mul_f32 v241, v241, v236\n"
"v_mul_f32 v242, v242, v236\n"
"v_mul_f32 v243, v243, v236\n"
"v_accvgpr_write_b32 a32, v240\n"
"v_accvgpr_write_b32 a33, v241\n"
"v_accvgpr_write_b32 a34, v242\n"
"v_accvgpr_write_b32 a35, v243\n"
"v_accvgpr_read_b32 v240, a36\n"
"v_accvgpr_read_b32 v241, a37\n"
"v_accvgpr_read_b32 v242, a38\n"
"v_accvgpr_read_b32 v243, a39\n"
"v_mul_f32 v240, v240, v237\n"
"v_mul_f32 v241, v241, v237\n"
"v_mul_f32 v242, v242, v237\n"
"v_mul_f32 v243, v243, v237\n"
"v_accvgpr_write_b32 a36, v240\n"
"v_accvgpr_write_b32 a37, v241\n"
"v_accvgpr_write_b32 a38, v242\n"
"v_accvgpr_write_b32 a39, v243\n"
"v_accvgpr_read_b32 v240, a40\n"
"v_accvgpr_read_b32 v241, a41\n"
"v_accvgpr_read_b32 v242, a42\n"
"v_accvgpr_read_b32 v243, a43\n"
"v_mul_f32 v240, v240, v238\n"
"v_mul_f32 v241, v241, v238\n"
"v_mul_f32 v242, v242, v238\n"
"v_mul_f32 v243, v243, v238\n"
"v_accvgpr_write_b32 a40, v240\n"
"v_accvgpr_write_b32 a41, v241\n"
"v_accvgpr_write_b32 a42, v242\n"
"v_accvgpr_write_b32 a43, v243\n"
"v_accvgpr_read_b32 v240, a44\n"
"v_accvgpr_read_b32 v241, a45\n"
"v_accvgpr_read_b32 v242, a46\n"
"v_accvgpr_read_b32 v243, a47\n"
"v_mul_f32 v240, v240, v239\n"
"v_mul_f32 v241, v241, v239\n"
"v_mul_f32 v242, v242, v239\n"
"v_mul_f32 v243, v243, v239\n"
"v_accvgpr_write_b32 a44, v240\n"
"v_accvgpr_write_b32 a45, v241\n"
"v_accvgpr_write_b32 a46, v242\n"
"v_accvgpr_write_b32 a47, v243\n"
"v_accvgpr_read_b32 v240, a48\n"
"v_accvgpr_read_b32 v241, a49\n"
"v_accvgpr_read_b32 v242, a50\n"
"v_accvgpr_read_b32 v243, a51\n"
"v_mul_f32 v240, v240, v236\n"
"v_mul_f32 v241, v241, v236\n"
"v_mul_f32 v242, v242, v236\n"
"v_mul_f32 v243, v243, v236\n"
"v_accvgpr_write_b32 a48, v240\n"
"v_accvgpr_write_b32 a49, v241\n"
"v_accvgpr_write_b32 a50, v242\n"
"v_accvgpr_write_b32 a51, v243\n"
"v_accvgpr_read_b32 v240, a52\n"
"v_accvgpr_read_b32 v241, a53\n"
"v_accvgpr_read_b32 v242, a54\n"
"v_accvgpr_read_b32 v243, a55\n"
"v_mul_f32 v240, v240, v237\n"
"v_mul_f32 v241, v241, v237\n"
"v_mul_f32 v242, v242, v237\n"
"v_mul_f32 v243, v243, v237\n"
"v_accvgpr_write_b32 a52, v240\n"
"v_accvgpr_write_b32 a53, v241\n"
"v_accvgpr_write_b32 a54, v242\n"
"v_accvgpr_write_b32 a55, v243\n"
"v_accvgpr_read_b32 v240, a56\n"
"v_accvgpr_read_b32 v241, a57\n"
"v_accvgpr_read_b32 v242, a58\n"
"v_accvgpr_read_b32 v243, a59\n"
"v_mul_f32 v240, v240, v238\n"
"v_mul_f32 v241, v241, v238\n"
"v_mul_f32 v242, v242, v238\n"
"v_mul_f32 v243, v243, v238\n"
"v_accvgpr_write_b32 a56, v240\n"
"v_accvgpr_write_b32 a57, v241\n"
"v_accvgpr_write_b32 a58, v242\n"
"v_accvgpr_write_b32 a59, v243\n"
"v_accvgpr_read_b32 v240, a60\n"
"v_accvgpr_read_b32 v241, a61\n"
"v_accvgpr_read_b32 v242, a62\n"
"v_accvgpr_read_b32 v243, a63\n"
"v_mul_f32 v240, v240, v239\n"
"v_mul_f32 v241, v241, v239\n"
"v_mul_f32 v242, v242, v239\n"
"v_mul_f32 v243, v243, v239\n"
"v_accvgpr_write_b32 a60, v240\n"
"v_accvgpr_write_b32 a61, v241\n"
"v_accvgpr_write_b32 a62, v242\n"
"v_accvgpr_write_b32 a63, v243\n"
"v_accvgpr_read_b32 v240, a64\n"
"v_accvgpr_read_b32 v241, a65\n"
"v_accvgpr_read_b32 v242, a66\n"
"v_accvgpr_read_b32 v243, a67\n"
"v_mul_f32 v240, v240, v236\n"
"v_mul_f32 v241, v241, v236\n"
"v_mul_f32 v242, v242, v236\n"
"v_mul_f32 v243, v243, v236\n"
"v_accvgpr_write_b32 a64, v240\n"
"v_accvgpr_write_b32 a65, v241\n"
"v_accvgpr_write_b32 a66, v242\n"
"v_accvgpr_write_b32 a67, v243\n"
"v_accvgpr_read_b32 v240, a68\n"
"v_accvgpr_read_b32 v241, a69\n"
"v_accvgpr_read_b32 v242, a70\n"
"v_accvgpr_read_b32 v243, a71\n"
"v_mul_f32 v240, v240, v237\n"
"v_mul_f32 v241, v241, v237\n"
"v_mul_f32 v242, v242, v237\n"
"v_mul_f32 v243, v243, v237\n"
"v_accvgpr_write_b32 a68, v240\n"
"v_accvgpr_write_b32 a69, v241\n"
"v_accvgpr_write_b32 a70, v242\n"
"v_accvgpr_write_b32 a71, v243\n"
"v_accvgpr_read_b32 v240, a72\n"
"v_accvgpr_read_b32 v241, a73\n"
"v_accvgpr_read_b32 v242, a74\n"
"v_accvgpr_read_b32 v243, a75\n"
"v_mul_f32 v240, v240, v238\n"
"v_mul_f32 v241, v241, v238\n"
"v_mul_f32 v242, v242, v238\n"
"v_mul_f32 v243, v243, v238\n"
"v_accvgpr_write_b32 a72, v240\n"
"v_accvgpr_write_b32 a73, v241\n"
"v_accvgpr_write_b32 a74, v242\n"
"v_accvgpr_write_b32 a75, v243\n"
"v_accvgpr_read_b32 v240, a76\n"
"v_accvgpr_read_b32 v241, a77\n"
"v_accvgpr_read_b32 v242, a78\n"
"v_accvgpr_read_b32 v243, a79\n"
"v_mul_f32 v240, v240, v239\n"
"v_mul_f32 v241, v241, v239\n"
"v_mul_f32 v242, v242, v239\n"
"v_mul_f32 v243, v243, v239\n"
"v_accvgpr_write_b32 a76, v240\n"
"v_accvgpr_write_b32 a77, v241\n"
"v_accvgpr_write_b32 a78, v242\n"
"v_accvgpr_write_b32 a79, v243\n"
"v_accvgpr_read_b32 v240, a80\n"
"v_accvgpr_read_b32 v241, a81\n"
"v_accvgpr_read_b32 v242, a82\n"
"v_accvgpr_read_b32 v243, a83\n"
"v_mul_f32 v240, v240, v236\n"
"v_mul_f32 v241, v241, v236\n"
"v_mul_f32 v242, v242, v236\n"
"v_mul_f32 v243, v243, v236\n"
"v_accvgpr_write_b32 a80, v240\n"
"v_accvgpr_write_b32 a81, v241\n"
"v_accvgpr_write_b32 a82, v242\n"
"v_accvgpr_write_b32 a83, v243\n"
"v_accvgpr_read_b32 v240, a84\n"
"v_accvgpr_read_b32 v241, a85\n"
"v_accvgpr_read_b32 v242, a86\n"
"v_accvgpr_read_b32 v243, a87\n"
"v_mul_f32 v240, v240, v237\n"
"v_mul_f32 v241, v241, v237\n"
"v_mul_f32 v242, v242, v237\n"
"v_mul_f32 v243, v243, v237\n"
"v_accvgpr_write_b32 a84, v240\n"
"v_accvgpr_write_b32 a85, v241\n"
"v_accvgpr_write_b32 a86, v242\n"
"v_accvgpr_write_b32 a87, v243\n"
"v_accvgpr_read_b32 v240, a88\n"
"v_accvgpr_read_b32 v241, a89\n"
"v_accvgpr_read_b32 v242, a90\n"
"v_accvgpr_read_b32 v243, a91\n"
"v_mul_f32 v240, v240, v238\n"
"v_mul_f32 v241, v241, v238\n"
"v_mul_f32 v242, v242, v238\n"
"v_mul_f32 v243, v243, v238\n"
"v_accvgpr_write_b32 a88, v240\n"
"v_accvgpr_write_b32 a89, v241\n"
"v_accvgpr_write_b32 a90, v242\n"
"v_accvgpr_write_b32 a91, v243\n"
"v_accvgpr_read_b32 v240, a92\n"
"v_accvgpr_read_b32 v241, a93\n"
"v_accvgpr_read_b32 v242, a94\n"
"v_accvgpr_read_b32 v243, a95\n"
"v_mul_f32 v240, v240, v239\n"
"v_mul_f32 v241, v241, v239\n"
"v_mul_f32 v242, v242, v239\n"
"v_mul_f32 v243, v243, v239\n"
"v_accvgpr_write_b32 a92, v240\n"
"v_accvgpr_write_b32 a93, v241\n"
"v_accvgpr_write_b32 a94, v242\n"
"v_accvgpr_write_b32 a95, v243\n"
"v_accvgpr_read_b32 v240, a96\n"
"v_accvgpr_read_b32 v241, a97\n"
"v_accvgpr_read_b32 v242, a98\n"
"v_accvgpr_read_b32 v243, a99\n"
"v_mul_f32 v240, v240, v236\n"
"v_mul_f32 v241, v241, v236\n"
"v_mul_f32 v242, v242, v236\n"
"v_mul_f32 v243, v243, v236\n"
"v_accvgpr_write_b32 a96, v240\n"
"v_accvgpr_write_b32 a97, v241\n"
"v_accvgpr_write_b32 a98, v242\n"
"v_accvgpr_write_b32 a99, v243\n"
"v_accvgpr_read_b32 v240, a100\n"
"v_accvgpr_read_b32 v241, a101\n"
"v_accvgpr_read_b32 v242, a102\n"
"v_accvgpr_read_b32 v243, a103\n"
"v_mul_f32 v240, v240, v237\n"
"v_mul_f32 v241, v241, v237\n"
"v_mul_f32 v242, v242, v237\n"
"v_mul_f32 v243, v243, v237\n"
"v_accvgpr_write_b32 a100, v240\n"
"v_accvgpr_write_b32 a101, v241\n"
"v_accvgpr_write_b32 a102, v242\n"
"v_accvgpr_write_b32 a103, v243\n"
"v_accvgpr_read_b32 v240, a104\n"
"v_accvgpr_read_b32 v241, a105\n"
"v_accvgpr_read_b32 v242, a106\n"
"v_accvgpr_read_b32 v243, a107\n"
"v_mul_f32 v240, v240, v238\n"
"v_mul_f32 v241, v241, v238\n"
"v_mul_f32 v242, v242, v238\n"
"v_mul_f32 v243, v243, v238\n"
"v_accvgpr_write_b32 a104, v240\n"
"v_accvgpr_write_b32 a105, v241\n"
"v_accvgpr_write_b32 a106, v242\n"
"v_accvgpr_write_b32 a107, v243\n"
"v_accvgpr_read_b32 v240, a108\n"
"v_accvgpr_read_b32 v241, a109\n"
"v_accvgpr_read_b32 v242, a110\n"
"v_accvgpr_read_b32 v243, a111\n"
"v_mul_f32 v240, v240, v239\n"
"v_mul_f32 v241, v241, v239\n"
"v_mul_f32 v242, v242, v239\n"
"v_mul_f32 v243, v243, v239\n"
"v_accvgpr_write_b32 a108, v240\n"
"v_accvgpr_write_b32 a109, v241\n"
"v_accvgpr_write_b32 a110, v242\n"
"v_accvgpr_write_b32 a111, v243\n"
"v_accvgpr_read_b32 v240, a112\n"
"v_accvgpr_read_b32 v241, a113\n"
"v_accvgpr_read_b32 v242, a114\n"
"v_accvgpr_read_b32 v243, a115\n"
"v_mul_f32 v240, v240, v236\n"
"v_mul_f32 v241, v241, v236\n"
"v_mul_f32 v242, v242, v236\n"
"v_mul_f32 v243, v243, v236\n"
"v_accvgpr_write_b32 a112, v240\n"
"v_accvgpr_write_b32 a113, v241\n"
"v_accvgpr_write_b32 a114, v242\n"
"v_accvgpr_write_b32 a115, v243\n"
"v_accvgpr_read_b32 v240, a116\n"
"v_accvgpr_read_b32 v241, a117\n"
"v_accvgpr_read_b32 v242, a118\n"
"v_accvgpr_read_b32 v243, a119\n"
"v_mul_f32 v240, v240, v237\n"
"v_mul_f32 v241, v241, v237\n"
"v_mul_f32 v242, v242, v237\n"
"v_mul_f32 v243, v243, v237\n"
"v_accvgpr_write_b32 a116, v240\n"
"v_accvgpr_write_b32 a117, v241\n"
"v_accvgpr_write_b32 a118, v242\n"
"v_accvgpr_write_b32 a119, v243\n"
"v_accvgpr_read_b32 v240, a120\n"
"v_accvgpr_read_b32 v241, a121\n"
"v_accvgpr_read_b32 v242, a122\n"
"v_accvgpr_read_b32 v243, a123\n"
"v_mul_f32 v240, v240, v238\n"
"v_mul_f32 v241, v241, v238\n"
"v_mul_f32 v242, v242, v238\n"
"v_mul_f32 v243, v243, v238\n"
"v_accvgpr_write_b32 a120, v240\n"
"v_accvgpr_write_b32 a121, v241\n"
"v_accvgpr_write_b32 a122, v242\n"
"v_accvgpr_write_b32 a123, v243\n"
"v_accvgpr_read_b32 v240, a124\n"
"v_accvgpr_read_b32 v241, a125\n"
"v_accvgpr_read_b32 v242, a126\n"
"v_accvgpr_read_b32 v243, a127\n"
"v_mul_f32 v240, v240, v239\n"
"v_mul_f32 v241, v241, v239\n"
"v_mul_f32 v242, v242, v239\n"
"v_mul_f32 v243, v243, v239\n"
"v_accvgpr_write_b32 a124, v240\n"
"v_accvgpr_write_b32 a125, v241\n"
"v_accvgpr_write_b32 a126, v242\n"
"v_accvgpr_write_b32 a127, v243\n"
"v_accvgpr_read_b32 v240, a128\n"
"v_accvgpr_read_b32 v241, a129\n"
"v_accvgpr_read_b32 v242, a130\n"
"v_accvgpr_read_b32 v243, a131\n"
"v_mul_f32 v240, v240, v236\n"
"v_mul_f32 v241, v241, v236\n"
"v_mul_f32 v242, v242, v236\n"
"v_mul_f32 v243, v243, v236\n"
"v_accvgpr_write_b32 a128, v240\n"
"v_accvgpr_write_b32 a129, v241\n"
"v_accvgpr_write_b32 a130, v242\n"
"v_accvgpr_write_b32 a131, v243\n"
"v_accvgpr_read_b32 v240, a132\n"
"v_accvgpr_read_b32 v241, a133\n"
"v_accvgpr_read_b32 v242, a134\n"
"v_accvgpr_read_b32 v243, a135\n"
"v_mul_f32 v240, v240, v237\n"
"v_mul_f32 v241, v241, v237\n"
"v_mul_f32 v242, v242, v237\n"
"v_mul_f32 v243, v243, v237\n"
"v_accvgpr_write_b32 a132, v240\n"
"v_accvgpr_write_b32 a133, v241\n"
"v_accvgpr_write_b32 a134, v242\n"
"v_accvgpr_write_b32 a135, v243\n"
"v_accvgpr_read_b32 v240, a136\n"
"v_accvgpr_read_b32 v241, a137\n"
"v_accvgpr_read_b32 v242, a138\n"
"v_accvgpr_read_b32 v243, a139\n"
"v_mul_f32 v240, v240, v238\n"
"v_mul_f32 v241, v241, v238\n"
"v_mul_f32 v242, v242, v238\n"
"v_mul_f32 v243, v243, v238\n"
"v_accvgpr_write_b32 a136, v240\n"
"v_accvgpr_write_b32 a137, v241\n"
"v_accvgpr_write_b32 a138, v242\n"
"v_accvgpr_write_b32 a139, v243\n"
"v_accvgpr_read_b32 v240, a140\n"
"v_accvgpr_read_b32 v241, a141\n"
"v_accvgpr_read_b32 v242, a142\n"
"v_accvgpr_read_b32 v243, a143\n"
"v_mul_f32 v240, v240, v239\n"
"v_mul_f32 v241, v241, v239\n"
"v_mul_f32 v242, v242, v239\n"
"v_mul_f32 v243, v243, v239\n"
"v_accvgpr_write_b32 a140, v240\n"
"v_accvgpr_write_b32 a141, v241\n"
"v_accvgpr_write_b32 a142, v242\n"
"v_accvgpr_write_b32 a143, v243\n"
"v_accvgpr_read_b32 v240, a144\n"
"v_accvgpr_read_b32 v241, a145\n"
"v_accvgpr_read_b32 v242, a146\n"
"v_accvgpr_read_b32 v243, a147\n"
"v_mul_f32 v240, v240, v236\n"
"v_mul_f32 v241, v241, v236\n"
"v_mul_f32 v242, v242, v236\n"
"v_mul_f32 v243, v243, v236\n"
"v_accvgpr_write_b32 a144, v240\n"
"v_accvgpr_write_b32 a145, v241\n"
"v_accvgpr_write_b32 a146, v242\n"
"v_accvgpr_write_b32 a147, v243\n"
"v_accvgpr_read_b32 v240, a148\n"
"v_accvgpr_read_b32 v241, a149\n"
"v_accvgpr_read_b32 v242, a150\n"
"v_accvgpr_read_b32 v243, a151\n"
"v_mul_f32 v240, v240, v237\n"
"v_mul_f32 v241, v241, v237\n"
"v_mul_f32 v242, v242, v237\n"
"v_mul_f32 v243, v243, v237\n"
"v_accvgpr_write_b32 a148, v240\n"
"v_accvgpr_write_b32 a149, v241\n"
"v_accvgpr_write_b32 a150, v242\n"
"v_accvgpr_write_b32 a151, v243\n"
"v_accvgpr_read_b32 v240, a152\n"
"v_accvgpr_read_b32 v241, a153\n"
"v_accvgpr_read_b32 v242, a154\n"
"v_accvgpr_read_b32 v243, a155\n"
"v_mul_f32 v240, v240, v238\n"
"v_mul_f32 v241, v241, v238\n"
"v_mul_f32 v242, v242, v238\n"
"v_mul_f32 v243, v243, v238\n"
"v_accvgpr_write_b32 a152, v240\n"
"v_accvgpr_write_b32 a153, v241\n"
"v_accvgpr_write_b32 a154, v242\n"
"v_accvgpr_write_b32 a155, v243\n"
"v_accvgpr_read_b32 v240, a156\n"
"v_accvgpr_read_b32 v241, a157\n"
"v_accvgpr_read_b32 v242, a158\n"
"v_accvgpr_read_b32 v243, a159\n"
"v_mul_f32 v240, v240, v239\n"
"v_mul_f32 v241, v241, v239\n"
"v_mul_f32 v242, v242, v239\n"
"v_mul_f32 v243, v243, v239\n"
"v_accvgpr_write_b32 a156, v240\n"
"v_accvgpr_write_b32 a157, v241\n"
"v_accvgpr_write_b32 a158, v242\n"
"v_accvgpr_write_b32 a159, v243\n"
"v_accvgpr_read_b32 v240, a160\n"
"v_accvgpr_read_b32 v241, a161\n"
"v_accvgpr_read_b32 v242, a162\n"
"v_accvgpr_read_b32 v243, a163\n"
"v_mul_f32 v240, v240, v236\n"
"v_mul_f32 v241, v241, v236\n"
"v_mul_f32 v242, v242, v236\n"
"v_mul_f32 v243, v243, v236\n"
"v_accvgpr_write_b32 a160, v240\n"
"v_accvgpr_write_b32 a161, v241\n"
"v_accvgpr_write_b32 a162, v242\n"
"v_accvgpr_write_b32 a163, v243\n"
"v_accvgpr_read_b32 v240, a164\n"
"v_accvgpr_read_b32 v241, a165\n"
"v_accvgpr_read_b32 v242, a166\n"
"v_accvgpr_read_b32 v243, a167\n"
"v_mul_f32 v240, v240, v237\n"
"v_mul_f32 v241, v241, v237\n"
"v_mul_f32 v242, v242, v237\n"
"v_mul_f32 v243, v243, v237\n"
"v_accvgpr_write_b32 a164, v240\n"
"v_accvgpr_write_b32 a165, v241\n"
"v_accvgpr_write_b32 a166, v242\n"
"v_accvgpr_write_b32 a167, v243\n"
"v_accvgpr_read_b32 v240, a168\n"
"v_accvgpr_read_b32 v241, a169\n"
"v_accvgpr_read_b32 v242, a170\n"
"v_accvgpr_read_b32 v243, a171\n"
"v_mul_f32 v240, v240, v238\n"
"v_mul_f32 v241, v241, v238\n"
"v_mul_f32 v242, v242, v238\n"
"v_mul_f32 v243, v243, v238\n"
"v_accvgpr_write_b32 a168, v240\n"
"v_accvgpr_write_b32 a169, v241\n"
"v_accvgpr_write_b32 a170, v242\n"
"v_accvgpr_write_b32 a171, v243\n"
"v_accvgpr_read_b32 v240, a172\n"
"v_accvgpr_read_b32 v241, a173\n"
"v_accvgpr_read_b32 v242, a174\n"
"v_accvgpr_read_b32 v243, a175\n"
"v_mul_f32 v240, v240, v239\n"
"v_mul_f32 v241, v241, v239\n"
"v_mul_f32 v242, v242, v239\n"
"v_mul_f32 v243, v243, v239\n"
"v_accvgpr_write_b32 a172, v240\n"
"v_accvgpr_write_b32 a173, v241\n"
"v_accvgpr_write_b32 a174, v242\n"
"v_accvgpr_write_b32 a175, v243\n"
"v_accvgpr_read_b32 v240, a176\n"
"v_accvgpr_read_b32 v241, a177\n"
"v_accvgpr_read_b32 v242, a178\n"
"v_accvgpr_read_b32 v243, a179\n"
"v_mul_f32 v240, v240, v236\n"
"v_mul_f32 v241, v241, v236\n"
"v_mul_f32 v242, v242, v236\n"
"v_mul_f32 v243, v243, v236\n"
"v_accvgpr_write_b32 a176, v240\n"
"v_accvgpr_write_b32 a177, v241\n"
"v_accvgpr_write_b32 a178, v242\n"
"v_accvgpr_write_b32 a179, v243\n"
"v_accvgpr_read_b32 v240, a180\n"
"v_accvgpr_read_b32 v241, a181\n"
"v_accvgpr_read_b32 v242, a182\n"
"v_accvgpr_read_b32 v243, a183\n"
"v_mul_f32 v240, v240, v237\n"
"v_mul_f32 v241, v241, v237\n"
"v_mul_f32 v242, v242, v237\n"
"v_mul_f32 v243, v243, v237\n"
"v_accvgpr_write_b32 a180, v240\n"
"v_accvgpr_write_b32 a181, v241\n"
"v_accvgpr_write_b32 a182, v242\n"
"v_accvgpr_write_b32 a183, v243\n"
"v_accvgpr_read_b32 v240, a184\n"
"v_accvgpr_read_b32 v241, a185\n"
"v_accvgpr_read_b32 v242, a186\n"
"v_accvgpr_read_b32 v243, a187\n"
"v_mul_f32 v240, v240, v238\n"
"v_mul_f32 v241, v241, v238\n"
"v_mul_f32 v242, v242, v238\n"
"v_mul_f32 v243, v243, v238\n"
"v_accvgpr_write_b32 a184, v240\n"
"v_accvgpr_write_b32 a185, v241\n"
"v_accvgpr_write_b32 a186, v242\n"
"v_accvgpr_write_b32 a187, v243\n"
"v_accvgpr_read_b32 v240, a188\n"
"v_accvgpr_read_b32 v241, a189\n"
"v_accvgpr_read_b32 v242, a190\n"
"v_accvgpr_read_b32 v243, a191\n"
"v_mul_f32 v240, v240, v239\n"
"v_mul_f32 v241, v241, v239\n"
"v_mul_f32 v242, v242, v239\n"
"v_mul_f32 v243, v243, v239\n"
"v_accvgpr_write_b32 a188, v240\n"
"v_accvgpr_write_b32 a189, v241\n"
"v_accvgpr_write_b32 a190, v242\n"
"v_accvgpr_write_b32 a191, v243\n"
"v_mul_f32 v96, v96, v236\n"
"v_mul_f32 v97, v97, v236\n"
"v_mul_f32 v98, v98, v236\n"
"v_mul_f32 v99, v99, v236\n"
"v_mul_f32 v100, v100, v237\n"
"v_mul_f32 v101, v101, v237\n"
"v_mul_f32 v102, v102, v237\n"
"v_mul_f32 v103, v103, v237\n"
"v_mul_f32 v104, v104, v238\n"
"v_mul_f32 v105, v105, v238\n"
"v_mul_f32 v106, v106, v238\n"
"v_mul_f32 v107, v107, v238\n"
"v_mul_f32 v108, v108, v239\n"
"v_mul_f32 v109, v109, v239\n"
"v_mul_f32 v110, v110, v239\n"
"v_mul_f32 v111, v111, v239\n"
"ds_read_b64_tr_b16 v[148:149], v210 offset:24576\n"
"ds_read_b64_tr_b16 v[150:151], v210 offset:30720\n"
"ds_read_b64_tr_b16 v[152:153], v211 offset:24576\n"
"ds_read_b64_tr_b16 v[154:155], v211 offset:30720\n"
"ds_read_b64_tr_b16 v[156:157], v212 offset:24576\n"
"ds_read_b64_tr_b16 v[158:159], v212 offset:30720\n"
"ds_read_b64_tr_b16 v[160:161], v213 offset:24576\n"
"ds_read_b64_tr_b16 v[162:163], v213 offset:30720\n"
"ds_read_b64_tr_b16 v[164:165], v210 offset:24704\n"
"ds_read_b64_tr_b16 v[166:167], v210 offset:30848\n"
"ds_read_b64_tr_b16 v[168:169], v211 offset:24704\n"
"ds_read_b64_tr_b16 v[170:171], v211 offset:30848\n"
"s_waitcnt lgkmcnt(0)\n"
"ds_read_b64_tr_b16 v[172:173], v212 offset:24704\n"
"ds_read_b64_tr_b16 v[174:175], v212 offset:30848\n"
"ds_read_b64_tr_b16 v[176:177], v213 offset:24704\n"
"ds_read_b64_tr_b16 v[178:179], v213 offset:30848\n"
"ds_read_b64_tr_b16 v[180:181], v210 offset:24832\n"
"ds_read_b64_tr_b16 v[182:183], v210 offset:30976\n"
"ds_read_b64_tr_b16 v[184:185], v211 offset:24832\n"
"ds_read_b64_tr_b16 v[186:187], v211 offset:30976\n"
"ds_read_b64_tr_b16 v[188:189], v212 offset:24832\n"
"ds_read_b64_tr_b16 v[190:191], v212 offset:30976\n"
"ds_read_b64_tr_b16 v[192:193], v213 offset:24832\n"
"ds_read_b64_tr_b16 v[194:195], v213 offset:30976\n"
"s_waitcnt lgkmcnt(0)\n"
"s_nop 1\n"
"v_mfma_f32_16x16x32_bf16 a[0:3], v[148:151], v[48:51], a[0:3]\n"
"v_mfma_f32_16x16x32_bf16 a[4:7], v[148:151], v[52:55], a[4:7]\n"
"v_mfma_f32_16x16x32_bf16 a[8:11], v[148:151], v[56:59], a[8:11]\n"
"v_mfma_f32_16x16x32_bf16 a[12:15], v[148:151], v[60:63], a[12:15]\n"
"v_mfma_f32_16x16x32_bf16 a[16:19], v[152:155], v[48:51], a[16:19]\n"
"v_mfma_f32_16x16x32_bf16 a[20:23], v[152:155], v[52:55], a[20:23]\n"
"v_mfma_f32_16x16x32_bf16 a[24:27], v[152:155], v[56:59], a[24:27]\n"
"v_mfma_f32_16x16x32_bf16 a[28:31], v[152:155], v[60:63], a[28:31]\n"
"v_mfma_f32_16x16x32_bf16 a[32:35], v[156:159], v[48:51], a[32:35]\n"
"v_mfma_f32_16x16x32_bf16 a[36:39], v[156:159], v[52:55], a[36:39]\n"
"v_mfma_f32_16x16x32_bf16 a[40:43], v[156:159], v[56:59], a[40:43]\n"
"v_mfma_f32_16x16x32_bf16 a[44:47], v[156:159], v[60:63], a[44:47]\n"
"v_mfma_f32_16x16x32_bf16 a[48:51], v[160:163], v[48:51], a[48:51]\n"
"v_mfma_f32_16x16x32_bf16 a[52:55], v[160:163], v[52:55], a[52:55]\n"
"v_mfma_f32_16x16x32_bf16 a[56:59], v[160:163], v[56:59], a[56:59]\n"
"v_mfma_f32_16x16x32_bf16 a[60:63], v[160:163], v[60:63], a[60:63]\n"
"v_mfma_f32_16x16x32_bf16 a[64:67], v[164:167], v[48:51], a[64:67]\n"
"v_mfma_f32_16x16x32_bf16 a[68:71], v[164:167], v[52:55], a[68:71]\n"
"v_mfma_f32_16x16x32_bf16 a[72:75], v[164:167], v[56:59], a[72:75]\n"
"v_mfma_f32_16x16x32_bf16 a[76:79], v[164:167], v[60:63], a[76:79]\n"
"v_mfma_f32_16x16x32_bf16 a[80:83], v[168:171], v[48:51], a[80:83]\n"
"v_mfma_f32_16x16x32_bf16 a[84:87], v[168:171], v[52:55], a[84:87]\n"
"v_mfma_f32_16x16x32_bf16 a[88:91], v[168:171], v[56:59], a[88:91]\n"
"v_mfma_f32_16x16x32_bf16 a[92:95], v[168:171], v[60:63], a[92:95]\n"
"v_mfma_f32_16x16x32_bf16 a[96:99], v[172:175], v[48:51], a[96:99]\n"
"v_mfma_f32_16x16x32_bf16 a[100:103], v[172:175], v[52:55], a[100:103]\n"
"v_mfma_f32_16x16x32_bf16 a[104:107], v[172:175], v[56:59], a[104:107]\n"
"v_mfma_f32_16x16x32_bf16 a[108:111], v[172:175], v[60:63], a[108:111]\n"
"v_mfma_f32_16x16x32_bf16 a[112:115], v[176:179], v[48:51], a[112:115]\n"
"v_mfma_f32_16x16x32_bf16 a[116:119], v[176:179], v[52:55], a[116:119]\n"
"v_mfma_f32_16x16x32_bf16 a[120:123], v[176:179], v[56:59], a[120:123]\n"
"v_mfma_f32_16x16x32_bf16 a[124:127], v[176:179], v[60:63], a[124:127]\n"
"v_mfma_f32_16x16x32_bf16 a[128:131], v[180:183], v[48:51], a[128:131]\n"
"v_mfma_f32_16x16x32_bf16 a[132:135], v[180:183], v[52:55], a[132:135]\n"
"v_mfma_f32_16x16x32_bf16 a[136:139], v[180:183], v[56:59], a[136:139]\n"
"v_mfma_f32_16x16x32_bf16 a[140:143], v[180:183], v[60:63], a[140:143]\n"
"v_mfma_f32_16x16x32_bf16 a[144:147], v[184:187], v[48:51], a[144:147]\n"
"v_mfma_f32_16x16x32_bf16 a[148:151], v[184:187], v[52:55], a[148:151]\n"
"v_mfma_f32_16x16x32_bf16 a[152:155], v[184:187], v[56:59], a[152:155]\n"
"v_mfma_f32_16x16x32_bf16 a[156:159], v[184:187], v[60:63], a[156:159]\n"
"v_mfma_f32_16x16x32_bf16 a[160:163], v[188:191], v[48:51], a[160:163]\n"
"v_mfma_f32_16x16x32_bf16 a[164:167], v[188:191], v[52:55], a[164:167]\n"
"v_mfma_f32_16x16x32_bf16 a[168:171], v[188:191], v[56:59], a[168:171]\n"
"v_mfma_f32_16x16x32_bf16 a[172:175], v[188:191], v[60:63], a[172:175]\n"
"v_mfma_f32_16x16x32_bf16 a[176:179], v[192:195], v[48:51], a[176:179]\n"
"v_mfma_f32_16x16x32_bf16 a[180:183], v[192:195], v[52:55], a[180:183]\n"
"v_mfma_f32_16x16x32_bf16 a[184:187], v[192:195], v[56:59], a[184:187]\n"
"v_mfma_f32_16x16x32_bf16 a[188:191], v[192:195], v[60:63], a[188:191]\n"
"v_mfma_f32_16x16x32_bf16 v[96:99], v[112:115], v[48:51], v[96:99]\n"
"v_mfma_f32_16x16x32_bf16 v[100:103], v[112:115], v[52:55], v[100:103]\n"
"v_mfma_f32_16x16x32_bf16 v[104:107], v[112:115], v[56:59], v[104:107]\n"
"v_mfma_f32_16x16x32_bf16 v[108:111], v[112:115], v[60:63], v[108:111]\n"
"s_nop 7\n"
"s_add_u32 s41, s41, 1\n"
"s_cmp_lt_u32 s41, 32\n"
"s_cbranch_scc1 L_sl%=\n"
"s_branch L_epia%=\n"
"L_ok%=:\n"
"s_cmp_ge_u32 %[blk], 240\n"
"s_cbranch_scc1 L_nok2set%=\n"
"s_mov_b64 s[20:21], %[o]\n"
"s_and_b32 s21, s21, 0xffff\n"
"s_mov_b32 s22, 23592960\n"
"s_mov_b32 s23, 0x20000\n"
"s_mov_b64 s[28:29], %[out]\n"
"s_and_b32 s29, s29, 0xffff\n"
"s_mov_b32 s30, 2457600\n"
"s_mov_b32 s31, 0x20000\n"
"s_mov_b64 s[24:25], %[o]\n"
"s_add_u32 s24, s24, 33554432\n"
"s_addc_u32 s25, s25, 0\n"
"s_and_b32 s25, s25, 0xffff\n"
"s_mov_b32 s26, 19660800\n"
"s_mov_b32 s27, 0x20000\n"
"s_lshr_b32 s53, s40, 1\n"
"s_and_b32 s54, s40, 1\n"
"v_lshlrev_b32 v6, 4, v4\n"
"v_add_u32 v165, 0, v6\n"
"v_add_u32 v166, 195584, v6\n"
"v_add_u32 v167, 391168, v6\n"
"v_add_u32 v168, 586752, v6\n"
"v_add_u32 v169, 786432, v6\n"
"s_mul_i32 s60, s50, 10\n"
"s_mul_i32 s61, s54, 5\n"
"s_add_u32 s60, s60, s61\n"
"s_mul_i32 s60, s60, 196608\n"
"s_mul_i32 s61, s52, 48\n"
"s_add_u32 s61, s61, s53\n"
"s_lshl_b32 s61, s61, 10\n"
"s_add_u32 s42, s60, s61\n"
"s_mul_i32 s45, s53, 10240\n"
"s_mul_i32 s60, s54, 5120\n"
"s_add_u32 s45, s45, s60\n"
"v_lshlrev_b32 v164, 4, v4\n"
"s_mul_i32 s60, s53, 5120\n"
"v_add_u32 v164, s60, v164\n"
"v_lshrrev_b32 v5, 4, v4\n"
"v_bfe_u32 v6, v4, 2, 2\n"
"v_and_b32 v7, 3, v4\n"
"v_lshl_add_u32 v8, v5, 2, v6\n"
"v_and_b32 v9, 7, v8\n"
"v_lshlrev_b32 v9, 1, v9\n"
"v_lshlrev_b32 v8, 8, v8\n"
"v_and_b32 v10, 1, v7\n"
"v_lshl_add_u32 v8, v10, 3, v8\n"
"v_lshrrev_b32 v7, 1, v7\n"
"s_lshl_b32 s60, s54, 3\n"
"v_add_u32 v7, s60, v7\n"
"v_add_u32 v10, 0, v7\n"
"v_xor_b32 v10, v10, v9\n"
"v_lshl_add_u32 v160, v10, 4, v8\n"
"v_add_u32 v10, 2, v7\n"
"v_xor_b32 v10, v10, v9\n"
"v_lshl_add_u32 v161, v10, 4, v8\n"
"v_add_u32 v10, 4, v7\n"
"v_xor_b32 v10, v10, v9\n"
"v_lshl_add_u32 v162, v10, 4, v8\n"
"v_add_u32 v10, 6, v7\n"
"v_xor_b32 v10, v10, v9\n"
"v_lshl_add_u32 v163, v10, 4, v8\n"
"v_mov_b32 v5, 0\n"
"v_accvgpr_write_b32 a0, v5\n"
"v_accvgpr_write_b32 a1, v5\n"
"v_accvgpr_write_b32 a2, v5\n"
"v_accvgpr_write_b32 a3, v5\n"
"v_accvgpr_write_b32 a4, v5\n"
"v_accvgpr_write_b32 a5, v5\n"
"v_accvgpr_write_b32 a6, v5\n"
"v_accvgpr_write_b32 a7, v5\n"
"v_accvgpr_write_b32 a8, v5\n"
"v_accvgpr_write_b32 a9, v5\n"
"v_accvgpr_write_b32 a10, v5\n"
"v_accvgpr_write_b32 a11, v5\n"
"v_accvgpr_write_b32 a12, v5\n"
"v_accvgpr_write_b32 a13, v5\n"
"v_accvgpr_write_b32 a14, v5\n"
"v_accvgpr_write_b32 a15, v5\n"
"v_accvgpr_write_b32 a16, v5\n"
"v_accvgpr_write_b32 a17, v5\n"
"v_accvgpr_write_b32 a18, v5\n"
"v_accvgpr_write_b32 a19, v5\n"
"v_accvgpr_write_b32 a20, v5\n"
"v_accvgpr_write_b32 a21, v5\n"
"v_accvgpr_write_b32 a22, v5\n"
"v_accvgpr_write_b32 a23, v5\n"
"v_accvgpr_write_b32 a24, v5\n"
"v_accvgpr_write_b32 a25, v5\n"
"v_accvgpr_write_b32 a26, v5\n"
"v_accvgpr_write_b32 a27, v5\n"
"v_accvgpr_write_b32 a28, v5\n"
"v_accvgpr_write_b32 a29, v5\n"
"v_accvgpr_write_b32 a30, v5\n"
"v_accvgpr_write_b32 a31, v5\n"
"v_accvgpr_write_b32 a32, v5\n"
"v_accvgpr_write_b32 a33, v5\n"
"v_accvgpr_write_b32 a34, v5\n"
"v_accvgpr_write_b32 a35, v5\n"
"v_accvgpr_write_b32 a36, v5\n"
"v_accvgpr_write_b32 a37, v5\n"
"v_accvgpr_write_b32 a38, v5\n"
"v_accvgpr_write_b32 a39, v5\n"
"v_accvgpr_write_b32 a40, v5\n"
"v_accvgpr_write_b32 a41, v5\n"
"v_accvgpr_write_b32 a42, v5\n"
"v_accvgpr_write_b32 a43, v5\n"
"v_accvgpr_write_b32 a44, v5\n"
"v_accvgpr_write_b32 a45, v5\n"
"v_accvgpr_write_b32 a46, v5\n"
"v_accvgpr_write_b32 a47, v5\n"
"v_accvgpr_write_b32 a48, v5\n"
"v_accvgpr_write_b32 a49, v5\n"
"v_accvgpr_write_b32 a50, v5\n"
"v_accvgpr_write_b32 a51, v5\n"
"v_accvgpr_write_b32 a52, v5\n"
"v_accvgpr_write_b32 a53, v5\n"
"v_accvgpr_write_b32 a54, v5\n"
"v_accvgpr_write_b32 a55, v5\n"
"v_accvgpr_write_b32 a56, v5\n"
"v_accvgpr_write_b32 a57, v5\n"
"v_accvgpr_write_b32 a58, v5\n"
"v_accvgpr_write_b32 a59, v5\n"
"v_accvgpr_write_b32 a60, v5\n"
"v_accvgpr_write_b32 a61, v5\n"
"v_accvgpr_write_b32 a62, v5\n"
"v_accvgpr_write_b32 a63, v5\n"
"v_accvgpr_write_b32 a64, v5\n"
"v_accvgpr_write_b32 a65, v5\n"
"v_accvgpr_write_b32 a66, v5\n"
"v_accvgpr_write_b32 a67, v5\n"
"v_accvgpr_write_b32 a68, v5\n"
"v_accvgpr_write_b32 a69, v5\n"
"v_accvgpr_write_b32 a70, v5\n"
"v_accvgpr_write_b32 a71, v5\n"
"v_accvgpr_write_b32 a72, v5\n"
"v_accvgpr_write_b32 a73, v5\n"
"v_accvgpr_write_b32 a74, v5\n"
"v_accvgpr_write_b32 a75, v5\n"
"v_accvgpr_write_b32 a76, v5\n"
"v_accvgpr_write_b32 a77, v5\n"
"v_accvgpr_write_b32 a78, v5\n"
"v_accvgpr_write_b32 a79, v5\n"
"L_nok2set%=:\n"
"v_readfirstlane_b32 s94, v248\n"
"v_readfirstlane_b32 s95, v249\n"
"s_nop 3\n"
"s_add_u32 s94, s94, 1\n"
"s_xor_b32 s95, s95, 0x5bd1e995\n"
"s_waitcnt vmcnt(0)\n"
"s_barrier\n"
"s_cmp_lg_u32 s40, 0\n"
"s_cbranch_scc1 L_nopub%=\n"
"s_lshl_b32 s60, %[blk], 7\n"
"s_add_u32 s60, s60, 58720256\n"
"v_mov_b32 v6, s60\n"
"v_mov_b32 v10, s94\n"
"v_mov_b32 v11, s95\n"
"s_mov_b64 exec, 1\n"
"global_store_dwordx2 v6, v[10:11], %[o] sc1\n"
"s_mov_b64 exec, -1\n"
"L_nopub%=:\n"
"s_cmp_lt_u32 %[blk], 240\n"
"s_cbranch_scc1 L_k2role%=\n"
"s_cmp_lg_u32 %[blk], 255\n"
"s_cbranch_scc1 L_done%=\n"
"s_cmp_lg_u32 s40, 0\n"
"s_cbranch_scc1 L_done%=\n"
"v_add_u32 v6, 0, v4\n"
"v_lshlrev_b32 v6, 7, v6\n"
"v_add_u32 v6, 58720256, v6\n"
"v_add_u32 v7, 64, v4\n"
"v_lshlrev_b32 v7, 7, v7\n"
"v_add_u32 v7, 58720256, v7\n"
"v_add_u32 v8, 128, v4\n"
"v_lshlrev_b32 v8, 7, v8\n"
"v_add_u32 v8, 58720256, v8\n"
"v_add_u32 v9, 192, v4\n"
"v_lshlrev_b32 v9, 7, v9\n"
"v_add_u32 v9, 58720256, v9\n"
"s_mov_b32 s66, 0\n"
"L_poll_e%=:\n"
"global_load_dwordx2 v[12:13], v6, %[o] sc1\n"
"global_load_dwordx2 v[14:15], v7, %[o] sc1\n"
"global_load_dwordx2 v[16:17], v8, %[o] sc1\n"
"global_load_dwordx2 v[18:19], v9, %[o] sc1\n"
"s_waitcnt vmcnt(0)\n"
"v_cmp_ne_u64 vcc, s[94:95], v[12:13]\n"
"v_cmp_ne_u64 s[68:69], s[94:95], v[14:15]\n"
"s_nop 1\n"
"s_or_b64 vcc, vcc, s[68:69]\n"
"v_cmp_ne_u64 s[68:69], s[94:95], v[16:17]\n"
"s_nop 1\n"
"s_or_b64 vcc, vcc, s[68:69]\n"
"v_cmp_ne_u64 s[68:69], s[94:95], v[18:19]\n"
"s_nop 1\n"
"s_or_b64 vcc, vcc, s[68:69]\n"
"s_nop 1\n"
"s_add_u32 s66, s66, 1\n"
"s_cmp_eq_u64 vcc, 0\n"
"s_cbranch_scc1 L_polled_e%=\n"
"s_cmp_lt_u32 s66, 0x80000\n"
"s_cbranch_scc1 L_poll_e%=\n"
"L_polled_e%=:\n"
"v_mov_b32 v6, 59768832\n"
"v_mov_b32 v10, s94\n"
"v_mov_b32 v11, s95\n"
"s_mov_b64 exec, 1\n"
"global_store_dwordx2 v6, v[10:11], %[o] sc1\n"
"s_mov_b64 exec, -1\n"
"s_branch L_done%=\n"
"L_k2role%=:\n"
"s_cmp_lg_u32 s40, 0\n"
"s_cbranch_scc1 L_nopollo%=\n"
"s_lshr_b32 s60, %[blk], 3\n"
"s_mul_i32 s60, s60, 52\n"
"s_lshr_b32 s60, s60, 8\n"
"s_mul_i32 s61, s60, 160\n"
"s_mul_i32 s62, s61, 4370\n"
"s_lshr_b32 s62, s62, 20\n"
"s_add_u32 s61, s61, 159\n"
"s_mul_i32 s63, s61, 4370\n"
"s_lshr_b32 s63, s63, 20\n"
"v_mov_b32 v6, s62\n"
"v_mov_b32 v7, s63\n"
"v_bfe_u32 v8, v4, 3, 1\n"
"v_cmp_eq_u32 vcc, 1, v8\n"
"s_nop 1\n"
"v_cndmask_b32 v6, v6, v7, vcc\n"
"v_and_b32 v7, 7, v4\n"
"v_lshl_add_u32 v6, v7, 2, v6\n"
"s_and_b32 s60, %[blk], 7\n"
"v_lshl_add_u32 v6, v6, 3, s60\n"
"v_lshlrev_b32 v6, 7, v6\n"
"v_add_u32 v6, 58720256, v6\n"
"s_mov_b32 s66, 0\n"
"L_poll_o%=:\n"
"global_load_dwordx2 v[12:13], v6, %[o] sc1\n"
"s_waitcnt vmcnt(0)\n"
"v_cmp_ne_u64 vcc, s[94:95], v[12:13]\n"
"s_nop 1\n"
"s_add_u32 s66, s66, 1\n"
"s_cmp_eq_u64 vcc, 0\n"
"s_cbranch_scc1 L_polled_o%=\n"
"s_cmp_lt_u32 s66, 0x80000\n"
"s_cbranch_scc1 L_poll_o%=\n"
"L_polled_o%=:\n"
"L_nopollo%=:\n"
"s_barrier\n"
"s_add_u32 s60, s42, 0\n"
"s_add_u32 m0, s45, 0\n"
"s_nop 0\n"
"buffer_load_dwordx4 v165, s[20:23], s60 offen offset:0 sc1 lds\n"
"buffer_load_dwordx4 v166, s[20:23], s60 offen offset:1024 sc1 lds\n"
"buffer_load_dwordx4 v167, s[20:23], s60 offen offset:2048 sc1 lds\n"
"buffer_load_dwordx4 v168, s[20:23], s60 offen offset:3072 sc1 lds\n"
"s_add_u32 m0, s45, 4096\n"
"s_nop 0\n"
"buffer_load_dwordx4 v169, s[20:23], s60 offen offset:0 sc1 lds\n"
"s_add_u32 s60, s42, 2048\n"
"s_add_u32 m0, s45, 36864\n"
"s_nop 0\n"
"buffer_load_dwordx4 v165, s[20:23], s60 offen offset:0 sc1 lds\n"
"buffer_load_dwordx4 v166, s[20:23], s60 offen offset:1024 sc1 lds\n"
"buffer_load_dwordx4 v167, s[20:23], s60 offen offset:2048 sc1 lds\n"
"buffer_load_dwordx4 v168, s[20:23], s60 offen offset:3072 sc1 lds\n"
"s_add_u32 m0, s45, 40960\n"
"s_nop 0\n"
"buffer_load_dwordx4 v169, s[20:23], s60 offen offset:0 sc1 lds\n"
"s_add_u32 s60, s42, 4096\n"
"s_add_u32 m0, s45, 73728\n"
"s_nop 0\n"
"buffer_load_dwordx4 v165, s[20:23], s60 offen offset:0 sc1 lds\n"
"buffer_load_dwordx4 v166, s[20:23], s60 offen offset:1024 sc1 lds\n"
"buffer_load_dwordx4 v167, s[20:23], s60 offen offset:2048 sc1 lds\n"
"buffer_load_dwordx4 v168, s[20:23], s60 offen offset:3072 sc1 lds\n"
"s_add_u32 m0, s45, 77824\n"
"s_nop 0\n"
"buffer_load_dwordx4 v169, s[20:23], s60 offen offset:0 sc1 lds\n"
"s_add_u32 s60, s42, 6144\n"
"s_add_u32 m0, s45, 110592\n"
"s_nop 0\n"
"buffer_load_dwordx4 v165, s[20:23], s60 offen offset:0 sc1 lds\n"
"buffer_load_dwordx4 v166, s[20:23], s60 offen offset:1024 sc1 lds\n"
"buffer_load_dwordx4 v167, s[20:23], s60 offen offset:2048 sc1 lds\n"
"buffer_load_dwordx4 v168, s[20:23], s60 offen offset:3072 sc1 lds\n"
"s_add_u32 m0, s45, 114688\n"
"s_nop 0\n"
"buffer_load_dwordx4 v169, s[20:23], s60 offen offset:0 sc1 lds\n"
"s_add_u32 s42, s42, 8192\n"
"s_waitcnt vmcnt(15)\n"
"s_barrier\n"
"s_mov_b32 s48, 0\n"
"s_mov_b32 s47, 36864\n"
"ds_read_b128 v[16:19], v164 offset:0\n"
"ds_read_b128 v[20:23], v164 offset:1024\n"
"ds_read_b128 v[24:27], v164 offset:2048\n"
"ds_read_b128 v[28:31], v164 offset:3072\n"
"ds_read_b128 v[32:35], v164 offset:4096\n"
"ds_read_b64_tr_b16 v[56:57], v160 offset:20480\n"
"ds_read_b64_tr_b16 v[58:59], v160 offset:24576\n"
"ds_read_b64_tr_b16 v[60:61], v161 offset:20480\n"
"ds_read_b64_tr_b16 v[62:63], v161 offset:24576\n"
"ds_read_b64_tr_b16 v[64:65], v162 offset:20480\n"
"ds_read_b64_tr_b16 v[66:67], v162 offset:24576\n"
"ds_read_b64_tr_b16 v[68:69], v163 offset:20480\n"
"ds_read_b64_tr_b16 v[70:71], v163 offset:24576\n"
"ds_read_b128 v[36:39], v164 offset:10240\n"
"ds_read_b128 v[40:43], v164 offset:11264\n"
"ds_read_b128 v[44:47], v164 offset:12288\n"
"ds_read_b128 v[48:51], v164 offset:13312\n"
"ds_read_b128 v[52:55], v164 offset:14336\n"
"ds_read_b64_tr_b16 v[72:73], v160 offset:28672\n"
"ds_read_b64_tr_b16 v[74:75], v160 offset:32768\n"
"ds_read_b64_tr_b16 v[76:77], v161 offset:28672\n"
"ds_read_b64_tr_b16 v[78:79], v161 offset:32768\n"
"ds_read_b64_tr_b16 v[80:81], v162 offset:28672\n"
"ds_read_b64_tr_b16 v[82:83], v162 offset:32768\n"
"ds_read_b64_tr_b16 v[84:85], v163 offset:28672\n"
"ds_read_b64_tr_b16 v[86:87], v163 offset:32768\n"
"s_mov_b32 s68, 4294856704\n"
"s_mov_b32 s69, 36864\n"
"v_add_u32 v160, s47, v160\n"
"v_add_u32 v161, s47, v161\n"
"v_add_u32 v162, s47, v162\n"
"v_add_u32 v163, s47, v163\n"
"v_add_u32 v164, s47, v164\n"
"s_mov_b32 s48, 1\n"
"s_waitcnt lgkmcnt(0)\n"
"v_mfma_f32_16x16x32_bf16 a[0:3], v[56:59], v[16:19], a[0:3]\n"
"s_waitcnt vmcnt(10) lgkmcnt(0)\n"
"s_barrier\n"
"v_mfma_f32_16x16x32_bf16 a[0:3], v[72:75], v[36:39], a[0:3]\n"
"s_mov_b32 m0, s45\n"
"ds_read_b128 v[88:91], v164 offset:0\n"
"v_mfma_f32_16x16x32_bf16 a[4:7], v[60:63], v[16:19], a[4:7]\n"
"ds_read_b128 v[92:95], v164 offset:1024\n"
"buffer_load_dwordx4 v165, s[20:23], s42 offen offset:0 sc1 lds\n"
"v_mfma_f32_16x16x32_bf16 a[4:7], v[76:79], v[36:39], a[4:7]\n"
"ds_read_b128 v[96:99], v164 offset:2048\n"
"v_mfma_f32_16x16x32_bf16 a[8:11], v[64:67], v[16:19], a[8:11]\n"
"ds_read_b128 v[100:103], v164 offset:3072\n"
"v_mfma_f32_16x16x32_bf16 a[8:11], v[80:83], v[36:39], a[8:11]\n"
"ds_read_b128 v[104:107], v164 offset:4096\n"
"v_mfma_f32_16x16x32_bf16 a[12:15], v[68:71], v[16:19], a[12:15]\n"
"ds_read_b64_tr_b16 v[128:129], v160 offset:20480\n"
"buffer_load_dwordx4 v166, s[20:23], s42 offen offset:1024 sc1 lds\n"
"v_mfma_f32_16x16x32_bf16 a[12:15], v[84:87], v[36:39], a[12:15]\n"
"ds_read_b64_tr_b16 v[130:131], v160 offset:24576\n"
"v_mfma_f32_16x16x32_bf16 a[16:19], v[56:59], v[20:23], a[16:19]\n"
"v_mfma_f32_16x16x32_bf16 a[16:19], v[72:75], v[40:43], a[16:19]\n"
"ds_read_b64_tr_b16 v[132:133], v161 offset:20480\n"
"v_mfma_f32_16x16x32_bf16 a[20:23], v[60:63], v[20:23], a[20:23]\n"
"ds_read_b64_tr_b16 v[134:135], v161 offset:24576\n"
"buffer_load_dwordx4 v167, s[20:23], s42 offen offset:2048 sc1 lds\n"
"v_mfma_f32_16x16x32_bf16 a[20:23], v[76:79], v[40:43], a[20:23]\n"
"ds_read_b64_tr_b16 v[136:137], v162 offset:20480\n"
"v_mfma_f32_16x16x32_bf16 a[24:27], v[64:67], v[20:23], a[24:27]\n"
"ds_read_b64_tr_b16 v[138:139], v162 offset:24576\n"
"v_mfma_f32_16x16x32_bf16 a[24:27], v[80:83], v[40:43], a[24:27]\n"
"ds_read_b64_tr_b16 v[140:141], v163 offset:20480\n"
"v_mfma_f32_16x16x32_bf16 a[28:31], v[68:71], v[20:23], a[28:31]\n"
"ds_read_b64_tr_b16 v[142:143], v163 offset:24576\n"
"buffer_load_dwordx4 v168, s[20:23], s42 offen offset:3072 sc1 lds\n"
"v_mfma_f32_16x16x32_bf16 a[28:31], v[84:87], v[40:43], a[28:31]\n"
"v_mfma_f32_16x16x32_bf16 a[32:35], v[56:59], v[24:27], a[32:35]\n"
"ds_read_b128 v[108:111], v164 offset:10240\n"
"v_mfma_f32_16x16x32_bf16 a[32:35], v[72:75], v[44:47], a[32:35]\n"
"s_add_u32 m0, s45, 4096\n"
"ds_read_b128 v[112:115], v164 offset:11264\n"
"v_mfma_f32_16x16x32_bf16 a[36:39], v[60:63], v[24:27], a[36:39]\n"
"ds_read_b128 v[116:119], v164 offset:12288\n"
"buffer_load_dwordx4 v169, s[20:23], s42 offen offset:0 sc1 lds\n"
"v_mfma_f32_16x16x32_bf16 a[36:39], v[76:79], v[44:47], a[36:39]\n"
"ds_read_b128 v[120:123], v164 offset:13312\n"
"v_mfma_f32_16x16x32_bf16 a[40:43], v[64:67], v[24:27], a[40:43]\n"
"ds_read_b128 v[124:127], v164 offset:14336\n"
"v_mfma_f32_16x16x32_bf16 a[40:43], v[80:83], v[44:47], a[40:43]\n"
"s_mov_b32 m0, s46\n"
"ds_read_b64_tr_b16 v[144:145], v160 offset:28672\n"
"v_mfma_f32_16x16x32_bf16 a[44:47], v[68:71], v[24:27], a[44:47]\n"
"ds_read_b64_tr_b16 v[146:147], v160 offset:32768\n"
"buffer_load_dwordx4 v170, s[32:35], s43 offen offset:0 lds\n"
"v_mfma_f32_16x16x32_bf16 a[44:47], v[84:87], v[44:47], a[44:47]\n"
"v_mfma_f32_16x16x32_bf16 a[48:51], v[56:59], v[28:31], a[48:51]\n"
"ds_read_b64_tr_b16 v[148:149], v161 offset:28672\n"
"v_mfma_f32_16x16x32_bf16 a[48:51], v[72:75], v[48:51], a[48:51]\n"
"ds_read_b64_tr_b16 v[150:151], v161 offset:32768\n"
"v_mfma_f32_16x16x32_bf16 a[52:55], v[60:63], v[28:31], a[52:55]\n"
"ds_read_b64_tr_b16 v[152:153], v162 offset:28672\n"
"buffer_load_dwordx4 v171, s[32:35], s43 offen offset:1024 lds\n"
"v_mfma_f32_16x16x32_bf16 a[52:55], v[76:79], v[48:51], a[52:55]\n"
"ds_read_b64_tr_b16 v[154:155], v162 offset:32768\n"
"v_mfma_f32_16x16x32_bf16 a[56:59], v[64:67], v[28:31], a[56:59]\n"
"ds_read_b64_tr_b16 v[156:157], v163 offset:28672\n"
"v_mfma_f32_16x16x32_bf16 a[56:59], v[80:83], v[48:51], a[56:59]\n"
"ds_read_b64_tr_b16 v[158:159], v163 offset:32768\n"
"v_mfma_f32_16x16x32_bf16 a[60:63], v[68:71], v[28:31], a[60:63]\n"
"buffer_load_dwordx4 v172, s[32:35], s43 offen offset:2048 lds\n"
"v_mfma_f32_16x16x32_bf16 a[60:63], v[84:87], v[48:51], a[60:63]\n"
"s_cmp_eq_u32 s48, 3\n"
"s_cselect_b32 s47, s68, s69\n"
"v_mfma_f32_16x16x32_bf16 a[64:67], v[56:59], v[32:35], a[64:67]\n"
"s_add_u32 s48, s48, 1\n"
"v_mfma_f32_16x16x32_bf16 a[64:67], v[72:75], v[52:55], a[64:67]\n"
"s_cmp_eq_u32 s48, 4\n"
"s_cselect_b32 s48, 0, s48\n"
"v_add_u32 v160, s47, v160\n"
"v_mfma_f32_16x16x32_bf16 a[68:71], v[60:63], v[32:35], a[68:71]\n"
"buffer_load_dwordx4 v173, s[32:35], s43 offen offset:3072 lds\n"
"v_add_u32 v161, s47, v161\n"
"v_mfma_f32_16x16x32_bf16 a[68:71], v[76:79], v[52:55], a[68:71]\n"
"s_add_u32 s42, s42, 2048\n"
"s_add_u32 s43, s43, 81920\n"
"v_add_u32 v162, s47, v162\n"
"v_mfma_f32_16x16x32_bf16 a[72:75], v[64:67], v[32:35], a[72:75]\n"
"s_add_u32 s45, s45, 36864\n"
"s_add_u32 s46, s46, 36864\n"
"v_add_u32 v163, s47, v163\n"
"v_mfma_f32_16x16x32_bf16 a[72:75], v[80:83], v[52:55], a[72:75]\n"
"s_add_u32 s55, s55, 1\n"
"v_add_u32 v164, s47, v164\n"
"v_mfma_f32_16x16x32_bf16 a[76:79], v[68:71], v[32:35], a[76:79]\n"
"s_cmp_eq_u32 s55, 4\n"
"s_cselect_b32 s60, 147456, 0\n"
"s_cselect_b32 s55, 0, s55\n"
"v_mfma_f32_16x16x32_bf16 a[76:79], v[84:87], v[52:55], a[76:79]\n"
"s_sub_u32 s45, s45, s60\n"
"s_sub_u32 s46, s46, s60\n"
"s_waitcnt lgkmcnt(0)\n"
"v_mfma_f32_16x16x32_bf16 a[0:3], v[128:131], v[88:91], a[0:3]\n"
"s_waitcnt vmcnt(14) lgkmcnt(0)\n"
"s_barrier\n"
"v_mfma_f32_16x16x32_bf16 a[0:3], v[144:147], v[108:111], a[0:3]\n"
"s_mov_b32 m0, s45\n"
"ds_read_b128 v[16:19], v164 offset:0\n"
"v_mfma_f32_16x16x32_bf16 a[4:7], v[132:135], v[88:91], a[4:7]\n"
"ds_read_b128 v[20:23], v164 offset:1024\n"
"buffer_load_dwordx4 v165, s[20:23], s42 offen offset:0 sc1 lds\n"
"v_mfma_f32_16x16x32_bf16 a[4:7], v[148:151], v[108:111], a[4:7]\n"
"ds_read_b128 v[24:27], v164 offset:2048\n"
"v_mfma_f32_16x16x32_bf16 a[8:11], v[136:139], v[88:91], a[8:11]\n"
"ds_read_b128 v[28:31], v164 offset:3072\n"
"v_mfma_f32_16x16x32_bf16 a[8:11], v[152:155], v[108:111], a[8:11]\n"
"ds_read_b128 v[32:35], v164 offset:4096\n"
"v_mfma_f32_16x16x32_bf16 a[12:15], v[140:143], v[88:91], a[12:15]\n"
"ds_read_b64_tr_b16 v[56:57], v160 offset:20480\n"
"buffer_load_dwordx4 v166, s[20:23], s42 offen offset:1024 sc1 lds\n"
"v_mfma_f32_16x16x32_bf16 a[12:15], v[156:159], v[108:111], a[12:15]\n"
"ds_read_b64_tr_b16 v[58:59], v160 offset:24576\n"
"v_mfma_f32_16x16x32_bf16 a[16:19], v[128:131], v[92:95], a[16:19]\n"
"v_mfma_f32_16x16x32_bf16 a[16:19], v[144:147], v[112:115], a[16:19]\n"
"ds_read_b64_tr_b16 v[60:61], v161 offset:20480\n"
"v_mfma_f32_16x16x32_bf16 a[20:23], v[132:135], v[92:95], a[20:23]\n"
"ds_read_b64_tr_b16 v[62:63], v161 offset:24576\n"
"buffer_load_dwordx4 v167, s[20:23], s42 offen offset:2048 sc1 lds\n"
"v_mfma_f32_16x16x32_bf16 a[20:23], v[148:151], v[112:115], a[20:23]\n"
"ds_read_b64_tr_b16 v[64:65], v162 offset:20480\n"
"v_mfma_f32_16x16x32_bf16 a[24:27], v[136:139], v[92:95], a[24:27]\n"
"ds_read_b64_tr_b16 v[66:67], v162 offset:24576\n"
"v_mfma_f32_16x16x32_bf16 a[24:27], v[152:155], v[112:115], a[24:27]\n"
"ds_read_b64_tr_b16 v[68:69], v163 offset:20480\n"
"v_mfma_f32_16x16x32_bf16 a[28:31], v[140:143], v[92:95], a[28:31]\n"
"ds_read_b64_tr_b16 v[70:71], v163 offset:24576\n"
"buffer_load_dwordx4 v168, s[20:23], s42 offen offset:3072 sc1 lds\n"
"v_mfma_f32_16x16x32_bf16 a[28:31], v[156:159], v[112:115], a[28:31]\n"
"v_mfma_f32_16x16x32_bf16 a[32:35], v[128:131], v[96:99], a[32:35]\n"
"ds_read_b128 v[36:39], v164 offset:10240\n"
"v_mfma_f32_16x16x32_bf16 a[32:35], v[144:147], v[116:119], a[32:35]\n"
"s_add_u32 m0, s45, 4096\n"
"ds_read_b128 v[40:43], v164 offset:11264\n"
"v_mfma_f32_16x16x32_bf16 a[36:39], v[132:135], v[96:99], a[36:39]\n"
"ds_read_b128 v[44:47], v164 offset:12288\n"
"buffer_load_dwordx4 v169, s[20:23], s42 offen offset:0 sc1 lds\n"
"v_mfma_f32_16x16x32_bf16 a[36:39], v[148:151], v[116:119], a[36:39]\n"
"ds_read_b128 v[48:51], v164 offset:13312\n"
"v_mfma_f32_16x16x32_bf16 a[40:43], v[136:139], v[96:99], a[40:43]\n"
"ds_read_b128 v[52:55], v164 offset:14336\n"
"v_mfma_f32_16x16x32_bf16 a[40:43], v[152:155], v[116:119], a[40:43]\n"
"s_mov_b32 m0, s46\n"
"ds_read_b64_tr_b16 v[72:73], v160 offset:28672\n"
"v_mfma_f32_16x16x32_bf16 a[44:47], v[140:143], v[96:99], a[44:47]\n"
"ds_read_b64_tr_b16 v[74:75], v160 offset:32768\n"
"buffer_load_dwordx4 v170, s[32:35], s43 offen offset:0 lds\n"
"v_mfma_f32_16x16x32_bf16 a[44:47], v[156:159], v[116:119], a[44:47]\n"
"v_mfma_f32_16x16x32_bf16 a[48:51], v[128:131], v[100:103], a[48:51]\n"
"ds_read_b64_tr_b16 v[76:77], v161 offset:28672\n"
"v_mfma_f32_16x16x32_bf16 a[48:51], v[144:147], v[120:123], a[48:51]\n"
"ds_read_b64_tr_b16 v[78:79], v161 offset:32768\n"
"v_mfma_f32_16x16x32_bf16 a[52:55], v[132:135], v[100:103], a[52:55]\n"
"ds_read_b64_tr_b16 v[80:81], v162 offset:28672\n"
"buffer_load_dwordx4 v171, s[32:35], s43 offen offset:1024 lds\n"
"v_mfma_f32_16x16x32_bf16 a[52:55], v[148:151], v[120:123], a[52:55]\n"
"ds_read_b64_tr_b16 v[82:83], v162 offset:32768\n"
"v_mfma_f32_16x16x32_bf16 a[56:59], v[136:139], v[100:103], a[56:59]\n"
"ds_read_b64_tr_b16 v[84:85], v163 offset:28672\n"
"v_mfma_f32_16x16x32_bf16 a[56:59], v[152:155], v[120:123], a[56:59]\n"
"ds_read_b64_tr_b16 v[86:87], v163 offset:32768\n"
"v_mfma_f32_16x16x32_bf16 a[60:63], v[140:143], v[100:103], a[60:63]\n"
"buffer_load_dwordx4 v172, s[32:35], s43 offen offset:2048 lds\n"
"v_mfma_f32_16x16x32_bf16 a[60:63], v[156:159], v[120:123], a[60:63]\n"
"s_cmp_eq_u32 s48, 3\n"
"s_cselect_b32 s47, s68, s69\n"
"v_mfma_f32_16x16x32_bf16 a[64:67], v[128:131], v[104:107], a[64:67]\n"
"s_add_u32 s48, s48, 1\n"
"v_mfma_f32_16x16x32_bf16 a[64:67], v[144:147], v[124:127], a[64:67]\n"
"s_cmp_eq_u32 s48, 4\n"
"s_cselect_b32 s48, 0, s48\n"
"v_add_u32 v160, s47, v160\n"
"v_mfma_f32_16x16x32_bf16 a[68:71], v[132:135], v[104:107], a[68:71]\n"
"buffer_load_dwordx4 v173, s[32:35], s43 offen offset:3072 lds\n"
"v_add_u32 v161, s47, v161\n"
"v_mfma_f32_16x16x32_bf16 a[68:71], v[148:151], v[124:127], a[68:71]\n"
"s_add_u32 s42, s42, 2048\n"
"s_add_u32 s43, s43, 81920\n"
"v_add_u32 v162, s47, v162\n"
"v_mfma_f32_16x16x32_bf16 a[72:75], v[136:139], v[104:107], a[72:75]\n"
"s_add_u32 s45, s45, 36864\n"
"s_add_u32 s46, s46, 36864\n"
"v_add_u32 v163, s47, v163\n"
"v_mfma_f32_16x16x32_bf16 a[72:75], v[152:155], v[124:127], a[72:75]\n"
"s_add_u32 s55, s55, 1\n"
"v_add_u32 v164, s47, v164\n"
"v_mfma_f32_16x16x32_bf16 a[76:79], v[140:143], v[104:107], a[76:79]\n"
"s_cmp_eq_u32 s55, 4\n"
"s_cselect_b32 s60, 147456, 0\n"
"s_cselect_b32 s55, 0, s55\n"
"v_mfma_f32_16x16x32_bf16 a[76:79], v[156:159], v[124:127], a[76:79]\n"
"s_sub_u32 s45, s45, s60\n"
"s_sub_u32 s46, s46, s60\n"
"s_waitcnt lgkmcnt(0)\n"
"v_mfma_f32_16x16x32_bf16 a[0:3], v[56:59], v[16:19], a[0:3]\n"
"s_waitcnt vmcnt(18) lgkmcnt(0)\n"
"s_barrier\n"
"v_mfma_f32_16x16x32_bf16 a[0:3], v[72:75], v[36:39], a[0:3]\n"
"s_mov_b32 m0, s45\n"
"ds_read_b128 v[88:91], v164 offset:0\n"
"v_mfma_f32_16x16x32_bf16 a[4:7], v[60:63], v[16:19], a[4:7]\n"
"ds_read_b128 v[92:95], v164 offset:1024\n"
"buffer_load_dwordx4 v165, s[20:23], s42 offen offset:0 sc1 lds\n"
"v_mfma_f32_16x16x32_bf16 a[4:7], v[76:79], v[36:39], a[4:7]\n"
"ds_read_b128 v[96:99], v164 offset:2048\n"
"v_mfma_f32_16x16x32_bf16 a[8:11], v[64:67], v[16:19], a[8:11]\n"
"ds_read_b128 v[100:103], v164 offset:3072\n"
"v_mfma_f32_16x16x32_bf16 a[8:11], v[80:83], v[36:39], a[8:11]\n"
"ds_read_b128 v[104:107], v164 offset:4096\n"
"v_mfma_f32_16x16x32_bf16 a[12:15], v[68:71], v[16:19], a[12:15]\n"
"ds_read_b64_tr_b16 v[128:129], v160 offset:20480\n"
"buffer_load_dwordx4 v166, s[20:23], s42 offen offset:1024 sc1 lds\n"
"v_mfma_f32_16x16x32_bf16 a[12:15], v[84:87], v[36:39], a[12:15]\n"
"ds_read_b64_tr_b16 v[130:131], v160 offset:24576\n"
"v_mfma_f32_16x16x32_bf16 a[16:19], v[56:59], v[20:23], a[16:19]\n"
"v_mfma_f32_16x16x32_bf16 a[16:19], v[72:75], v[40:43], a[16:19]\n"
"ds_read_b64_tr_b16 v[132:133], v161 offset:20480\n"
"v_mfma_f32_16x16x32_bf16 a[20:23], v[60:63], v[20:23], a[20:23]\n"
"ds_read_b64_tr_b16 v[134:135], v161 offset:24576\n"
"buffer_load_dwordx4 v167, s[20:23], s42 offen offset:2048 sc1 lds\n"
"v_mfma_f32_16x16x32_bf16 a[20:23], v[76:79], v[40:43], a[20:23]\n"
"ds_read_b64_tr_b16 v[136:137], v162 offset:20480\n"
"v_mfma_f32_16x16x32_bf16 a[24:27], v[64:67], v[20:23], a[24:27]\n"
"ds_read_b64_tr_b16 v[138:139], v162 offset:24576\n"
"v_mfma_f32_16x16x32_bf16 a[24:27], v[80:83], v[40:43], a[24:27]\n"
"ds_read_b64_tr_b16 v[140:141], v163 offset:20480\n"
"v_mfma_f32_16x16x32_bf16 a[28:31], v[68:71], v[20:23], a[28:31]\n"
"ds_read_b64_tr_b16 v[142:143], v163 offset:24576\n"
"buffer_load_dwordx4 v168, s[20:23], s42 offen offset:3072 sc1 lds\n"
"v_mfma_f32_16x16x32_bf16 a[28:31], v[84:87], v[40:43], a[28:31]\n"
"v_mfma_f32_16x16x32_bf16 a[32:35], v[56:59], v[24:27], a[32:35]\n"
"ds_read_b128 v[108:111], v164 offset:10240\n"
"v_mfma_f32_16x16x32_bf16 a[32:35], v[72:75], v[44:47], a[32:35]\n"
"s_add_u32 m0, s45, 4096\n"
"ds_read_b128 v[112:115], v164 offset:11264\n"
"v_mfma_f32_16x16x32_bf16 a[36:39], v[60:63], v[24:27], a[36:39]\n"
"ds_read_b128 v[116:119], v164 offset:12288\n"
"buffer_load_dwordx4 v169, s[20:23], s42 offen offset:0 sc1 lds\n"
"v_mfma_f32_16x16x32_bf16 a[36:39], v[76:79], v[44:47], a[36:39]\n"
"ds_read_b128 v[120:123], v164 offset:13312\n"
"v_mfma_f32_16x16x32_bf16 a[40:43], v[64:67], v[24:27], a[40:43]\n"
"ds_read_b128 v[124:127], v164 offset:14336\n"
"v_mfma_f32_16x16x32_bf16 a[40:43], v[80:83], v[44:47], a[40:43]\n"
"s_mov_b32 m0, s46\n"
"ds_read_b64_tr_b16 v[144:145], v160 offset:28672\n"
"v_mfma_f32_16x16x32_bf16 a[44:47], v[68:71], v[24:27], a[44:47]\n"
"ds_read_b64_tr_b16 v[146:147], v160 offset:32768\n"
"buffer_load_dwordx4 v170, s[32:35], s43 offen offset:0 lds\n"
"v_mfma_f32_16x16x32_bf16 a[44:47], v[84:87], v[44:47], a[44:47]\n"
"v_mfma_f32_16x16x32_bf16 a[48:51], v[56:59], v[28:31], a[48:51]\n"
"ds_read_b64_tr_b16 v[148:149], v161 offset:28672\n"
"v_mfma_f32_16x16x32_bf16 a[48:51], v[72:75], v[48:51], a[48:51]\n"
"ds_read_b64_tr_b16 v[150:151], v161 offset:32768\n"
"v_mfma_f32_16x16x32_bf16 a[52:55], v[60:63], v[28:31], a[52:55]\n"
"ds_read_b64_tr_b16 v[152:153], v162 offset:28672\n"
"buffer_load_dwordx4 v171, s[32:35], s43 offen offset:1024 lds\n"
"v_mfma_f32_16x16x32_bf16 a[52:55], v[76:79], v[48:51], a[52:55]\n"
"ds_read_b64_tr_b16 v[154:155], v162 offset:32768\n"
"v_mfma_f32_16x16x32_bf16 a[56:59], v[64:67], v[28:31], a[56:59]\n"
"ds_read_b64_tr_b16 v[156:157], v163 offset:28672\n"
"v_mfma_f32_16x16x32_bf16 a[56:59], v[80:83], v[48:51], a[56:59]\n"
"ds_read_b64_tr_b16 v[158:159], v163 offset:32768\n"
"v_mfma_f32_16x16x32_bf16 a[60:63], v[68:71], v[28:31], a[60:63]\n"
"buffer_load_dwordx4 v172, s[32:35], s43 offen offset:2048 lds\n"
"v_mfma_f32_16x16x32_bf16 a[60:63], v[84:87], v[48:51], a[60:63]\n"
"s_cmp_eq_u32 s48, 3\n"
"s_cselect_b32 s47, s68, s69\n"
"v_mfma_f32_16x16x32_bf16 a[64:67], v[56:59], v[32:35], a[64:67]\n"
"s_add_u32 s48, s48, 1\n"
"v_mfma_f32_16x16x32_bf16 a[64:67], v[72:75], v[52:55], a[64:67]\n"
"s_cmp_eq_u32 s48, 4\n"
"s_cselect_b32 s48, 0, s48\n"
"v_add_u32 v160, s47, v160\n"
"v_mfma_f32_16x16x32_bf16 a[68:71], v[60:63], v[32:35], a[68:71]\n"
"buffer_load_dwordx4 v173, s[32:35], s43 offen offset:3072 lds\n"
"v_add_u32 v161, s47, v161\n"
"v_mfma_f32_16x16x32_bf16 a[68:71], v[76:79], v[52:55], a[68:71]\n"
"s_add_u32 s42, s42, 2048\n"
"s_add_u32 s43, s43, 81920\n"
"v_add_u32 v162, s47, v162\n"
"v_mfma_f32_16x16x32_bf16 a[72:75], v[64:67], v[32:35], a[72:75]\n"
"s_add_u32 s45, s45, 36864\n"
"s_add_u32 s46, s46, 36864\n"
"v_add_u32 v163, s47, v163\n"
"v_mfma_f32_16x16x32_bf16 a[72:75], v[80:83], v[52:55], a[72:75]\n"
"s_add_u32 s55, s55, 1\n"
"v_add_u32 v164, s47, v164\n"
"v_mfma_f32_16x16x32_bf16 a[76:79], v[68:71], v[32:35], a[76:79]\n"
"s_cmp_eq_u32 s55, 4\n"
"s_cselect_b32 s60, 147456, 0\n"
"s_cselect_b32 s55, 0, s55\n"
"v_mfma_f32_16x16x32_bf16 a[76:79], v[84:87], v[52:55], a[76:79]\n"
"s_sub_u32 s45, s45, s60\n"
"s_sub_u32 s46, s46, s60\n"
"s_waitcnt lgkmcnt(0)\n"
"v_mfma_f32_16x16x32_bf16 a[0:3], v[128:131], v[88:91], a[0:3]\n"
"s_waitcnt vmcnt(18) lgkmcnt(0)\n"
"s_barrier\n"
"v_mfma_f32_16x16x32_bf16 a[0:3], v[144:147], v[108:111], a[0:3]\n"
"s_mov_b32 m0, s45\n"
"ds_read_b128 v[16:19], v164 offset:0\n"
"v_mfma_f32_16x16x32_bf16 a[4:7], v[132:135], v[88:91], a[4:7]\n"
"ds_read_b128 v[20:23], v164 offset:1024\n"
"buffer_load_dwordx4 v165, s[20:23], s42 offen offset:0 sc1 lds\n"
"v_mfma_f32_16x16x32_bf16 a[4:7], v[148:151], v[108:111], a[4:7]\n"
"ds_read_b128 v[24:27], v164 offset:2048\n"
"v_mfma_f32_16x16x32_bf16 a[8:11], v[136:139], v[88:91], a[8:11]\n"
"ds_read_b128 v[28:31], v164 offset:3072\n"
"v_mfma_f32_16x16x32_bf16 a[8:11], v[152:155], v[108:111], a[8:11]\n"
"ds_read_b128 v[32:35], v164 offset:4096\n"
"v_mfma_f32_16x16x32_bf16 a[12:15], v[140:143], v[88:91], a[12:15]\n"
"ds_read_b64_tr_b16 v[56:57], v160 offset:20480\n"
"buffer_load_dwordx4 v166, s[20:23], s42 offen offset:1024 sc1 lds\n"
"v_mfma_f32_16x16x32_bf16 a[12:15], v[156:159], v[108:111], a[12:15]\n"
"ds_read_b64_tr_b16 v[58:59], v160 offset:24576\n"
"v_mfma_f32_16x16x32_bf16 a[16:19], v[128:131], v[92:95], a[16:19]\n"
"v_mfma_f32_16x16x32_bf16 a[16:19], v[144:147], v[112:115], a[16:19]\n"
"ds_read_b64_tr_b16 v[60:61], v161 offset:20480\n"
"v_mfma_f32_16x16x32_bf16 a[20:23], v[132:135], v[92:95], a[20:23]\n"
"ds_read_b64_tr_b16 v[62:63], v161 offset:24576\n"
"buffer_load_dwordx4 v167, s[20:23], s42 offen offset:2048 sc1 lds\n"
"v_mfma_f32_16x16x32_bf16 a[20:23], v[148:151], v[112:115], a[20:23]\n"
"ds_read_b64_tr_b16 v[64:65], v162 offset:20480\n"
"v_mfma_f32_16x16x32_bf16 a[24:27], v[136:139], v[92:95], a[24:27]\n"
"ds_read_b64_tr_b16 v[66:67], v162 offset:24576\n"
"v_mfma_f32_16x16x32_bf16 a[24:27], v[152:155], v[112:115], a[24:27]\n"
"ds_read_b64_tr_b16 v[68:69], v163 offset:20480\n"
"v_mfma_f32_16x16x32_bf16 a[28:31], v[140:143], v[92:95], a[28:31]\n"
"ds_read_b64_tr_b16 v[70:71], v163 offset:24576\n"
"buffer_load_dwordx4 v168, s[20:23], s42 offen offset:3072 sc1 lds\n"
"v_mfma_f32_16x16x32_bf16 a[28:31], v[156:159], v[112:115], a[28:31]\n"
"v_mfma_f32_16x16x32_bf16 a[32:35], v[128:131], v[96:99], a[32:35]\n"
"ds_read_b128 v[36:39], v164 offset:10240\n"
"v_mfma_f32_16x16x32_bf16 a[32:35], v[144:147], v[116:119], a[32:35]\n"
"s_add_u32 m0, s45, 4096\n"
"ds_read_b128 v[40:43], v164 offset:11264\n"
"v_mfma_f32_16x16x32_bf16 a[36:39], v[132:135], v[96:99], a[36:39]\n"
"ds_read_b128 v[44:47], v164 offset:12288\n"
"buffer_load_dwordx4 v169, s[20:23], s42 offen offset:0 sc1 lds\n"
"v_mfma_f32_16x16x32_bf16 a[36:39], v[148:151], v[116:119], a[36:39]\n"
"ds_read_b128 v[48:51], v164 offset:13312\n"
"v_mfma_f32_16x16x32_bf16 a[40:43], v[136:139], v[96:99], a[40:43]\n"
"ds_read_b128 v[52:55], v164 offset:14336\n"
"v_mfma_f32_16x16x32_bf16 a[40:43], v[152:155], v[116:119], a[40:43]\n"
"s_mov_b32 m0, s46\n"
"ds_read_b64_tr_b16 v[72:73], v160 offset:28672\n"
"v_mfma_f32_16x16x32_bf16 a[44:47], v[140:143], v[96:99], a[44:47]\n"
"ds_read_b64_tr_b16 v[74:75], v160 offset:32768\n"
"buffer_load_dwordx4 v170, s[32:35], s43 offen offset:0 lds\n"
"v_mfma_f32_16x16x32_bf16 a[44:47], v[156:159], v[116:119], a[44:47]\n"
"v_mfma_f32_16x16x32_bf16 a[48:51], v[128:131], v[100:103], a[48:51]\n"
"ds_read_b64_tr_b16 v[76:77], v161 offset:28672\n"
"v_mfma_f32_16x16x32_bf16 a[48:51], v[144:147], v[120:123], a[48:51]\n"
"ds_read_b64_tr_b16 v[78:79], v161 offset:32768\n"
"v_mfma_f32_16x16x32_bf16 a[52:55], v[132:135], v[100:103], a[52:55]\n"
"ds_read_b64_tr_b16 v[80:81], v162 offset:28672\n"
"buffer_load_dwordx4 v171, s[32:35], s43 offen offset:1024 lds\n"
"v_mfma_f32_16x16x32_bf16 a[52:55], v[148:151], v[120:123], a[52:55]\n"
"ds_read_b64_tr_b16 v[82:83], v162 offset:32768\n"
"v_mfma_f32_16x16x32_bf16 a[56:59], v[136:139], v[100:103], a[56:59]\n"
"ds_read_b64_tr_b16 v[84:85], v163 offset:28672\n"
"v_mfma_f32_16x16x32_bf16 a[56:59], v[152:155], v[120:123], a[56:59]\n"
"ds_read_b64_tr_b16 v[86:87], v163 offset:32768\n"
"v_mfma_f32_16x16x32_bf16 a[60:63], v[140:143], v[100:103], a[60:63]\n"
"buffer_load_dwordx4 v172, s[32:35], s43 offen offset:2048 lds\n"
"v_mfma_f32_16x16x32_bf16 a[60:63], v[156:159], v[120:123], a[60:63]\n"
"s_cmp_eq_u32 s48, 3\n"
"s_cselect_b32 s47, s68, s69\n"
"v_mfma_f32_16x16x32_bf16 a[64:67], v[128:131], v[104:107], a[64:67]\n"
"s_add_u32 s48, s48, 1\n"
"v_mfma_f32_16x16x32_bf16 a[64:67], v[144:147], v[124:127], a[64:67]\n"
"s_cmp_eq_u32 s48, 4\n"
"s_cselect_b32 s48, 0, s48\n"
"v_add_u32 v160, s47, v160\n"
"v_mfma_f32_16x16x32_bf16 a[68:71], v[132:135], v[104:107], a[68:71]\n"
"buffer_load_dwordx4 v173, s[32:35], s43 offen offset:3072 lds\n"
"v_add_u32 v161, s47, v161\n"
"v_mfma_f32_16x16x32_bf16 a[68:71], v[148:151], v[124:127], a[68:71]\n"
"s_add_u32 s42, s42, 2048\n"
"s_add_u32 s43, s43, 81920\n"
"v_add_u32 v162, s47, v162\n"
"v_mfma_f32_16x16x32_bf16 a[72:75], v[136:139], v[104:107], a[72:75]\n"
"s_add_u32 s45, s45, 36864\n"
"s_add_u32 s46, s46, 36864\n"
"v_add_u32 v163, s47, v163\n"
"v_mfma_f32_16x16x32_bf16 a[72:75], v[152:155], v[124:127], a[72:75]\n"
"s_add_u32 s55, s55, 1\n"
"v_add_u32 v164, s47, v164\n"
"v_mfma_f32_16x16x32_bf16 a[76:79], v[140:143], v[104:107], a[76:79]\n"
"s_cmp_eq_u32 s55, 4\n"
"s_cselect_b32 s60, 147456, 0\n"
"s_cselect_b32 s55, 0, s55\n"
"v_mfma_f32_16x16x32_bf16 a[76:79], v[156:159], v[124:127], a[76:79]\n"
"s_sub_u32 s45, s45, s60\n"
"s_sub_u32 s46, s46, s60\n"
"s_waitcnt lgkmcnt(0)\n"
"v_mfma_f32_16x16x32_bf16 a[0:3], v[56:59], v[16:19], a[0:3]\n"
"s_waitcnt vmcnt(18) lgkmcnt(0)\n"
"s_barrier\n"
"v_mfma_f32_16x16x32_bf16 a[0:3], v[72:75], v[36:39], a[0:3]\n"
"s_mov_b32 m0, s45\n"
"ds_read_b128 v[88:91], v164 offset:0\n"
"v_mfma_f32_16x16x32_bf16 a[4:7], v[60:63], v[16:19], a[4:7]\n"
"ds_read_b128 v[92:95], v164 offset:1024\n"
"buffer_load_dwordx4 v165, s[20:23], s42 offen offset:0 sc1 lds\n"
"v_mfma_f32_16x16x32_bf16 a[4:7], v[76:79], v[36:39], a[4:7]\n"
"ds_read_b128 v[96:99], v164 offset:2048\n"
"v_mfma_f32_16x16x32_bf16 a[8:11], v[64:67], v[16:19], a[8:11]\n"
"ds_read_b128 v[100:103], v164 offset:3072\n"
"v_mfma_f32_16x16x32_bf16 a[8:11], v[80:83], v[36:39], a[8:11]\n"
"ds_read_b128 v[104:107], v164 offset:4096\n"
"v_mfma_f32_16x16x32_bf16 a[12:15], v[68:71], v[16:19], a[12:15]\n"
"ds_read_b64_tr_b16 v[128:129], v160 offset:20480\n"
"buffer_load_dwordx4 v166, s[20:23], s42 offen offset:1024 sc1 lds\n"
"v_mfma_f32_16x16x32_bf16 a[12:15], v[84:87], v[36:39], a[12:15]\n"
"ds_read_b64_tr_b16 v[130:131], v160 offset:24576\n"
"v_mfma_f32_16x16x32_bf16 a[16:19], v[56:59], v[20:23], a[16:19]\n"
"v_mfma_f32_16x16x32_bf16 a[16:19], v[72:75], v[40:43], a[16:19]\n"
"ds_read_b64_tr_b16 v[132:133], v161 offset:20480\n"
"v_mfma_f32_16x16x32_bf16 a[20:23], v[60:63], v[20:23], a[20:23]\n"
"ds_read_b64_tr_b16 v[134:135], v161 offset:24576\n"
"buffer_load_dwordx4 v167, s[20:23], s42 offen offset:2048 sc1 lds\n"
"v_mfma_f32_16x16x32_bf16 a[20:23], v[76:79], v[40:43], a[20:23]\n"
"ds_read_b64_tr_b16 v[136:137], v162 offset:20480\n"
"v_mfma_f32_16x16x32_bf16 a[24:27], v[64:67], v[20:23], a[24:27]\n"
"ds_read_b64_tr_b16 v[138:139], v162 offset:24576\n"
"v_mfma_f32_16x16x32_bf16 a[24:27], v[80:83], v[40:43], a[24:27]\n"
"ds_read_b64_tr_b16 v[140:141], v163 offset:20480\n"
"v_mfma_f32_16x16x32_bf16 a[28:31], v[68:71], v[20:23], a[28:31]\n"
"ds_read_b64_tr_b16 v[142:143], v163 offset:24576\n"
"buffer_load_dwordx4 v168, s[20:23], s42 offen offset:3072 sc1 lds\n"
"v_mfma_f32_16x16x32_bf16 a[28:31], v[84:87], v[40:43], a[28:31]\n"
"v_mfma_f32_16x16x32_bf16 a[32:35], v[56:59], v[24:27], a[32:35]\n"
"ds_read_b128 v[108:111], v164 offset:10240\n"
"v_mfma_f32_16x16x32_bf16 a[32:35], v[72:75], v[44:47], a[32:35]\n"
"s_add_u32 m0, s45, 4096\n"
"ds_read_b128 v[112:115], v164 offset:11264\n"
"v_mfma_f32_16x16x32_bf16 a[36:39], v[60:63], v[24:27], a[36:39]\n"
"ds_read_b128 v[116:119], v164 offset:12288\n"
"buffer_load_dwordx4 v169, s[20:23], s42 offen offset:0 sc1 lds\n"
"v_mfma_f32_16x16x32_bf16 a[36:39], v[76:79], v[44:47], a[36:39]\n"
"ds_read_b128 v[120:123], v164 offset:13312\n"
"v_mfma_f32_16x16x32_bf16 a[40:43], v[64:67], v[24:27], a[40:43]\n"
"ds_read_b128 v[124:127], v164 offset:14336\n"
"v_mfma_f32_16x16x32_bf16 a[40:43], v[80:83], v[44:47], a[40:43]\n"
"s_mov_b32 m0, s46\n"
"ds_read_b64_tr_b16 v[144:145], v160 offset:28672\n"
"v_mfma_f32_16x16x32_bf16 a[44:47], v[68:71], v[24:27], a[44:47]\n"
"ds_read_b64_tr_b16 v[146:147], v160 offset:32768\n"
"buffer_load_dwordx4 v170, s[32:35], s43 offen offset:0 lds\n"
"v_mfma_f32_16x16x32_bf16 a[44:47], v[84:87], v[44:47], a[44:47]\n"
"v_mfma_f32_16x16x32_bf16 a[48:51], v[56:59], v[28:31], a[48:51]\n"
"ds_read_b64_tr_b16 v[148:149], v161 offset:28672\n"
"v_mfma_f32_16x16x32_bf16 a[48:51], v[72:75], v[48:51], a[48:51]\n"
"ds_read_b64_tr_b16 v[150:151], v161 offset:32768\n"
"v_mfma_f32_16x16x32_bf16 a[52:55], v[60:63], v[28:31], a[52:55]\n"
"ds_read_b64_tr_b16 v[152:153], v162 offset:28672\n"
"buffer_load_dwordx4 v171, s[32:35], s43 offen offset:1024 lds\n"
"v_mfma_f32_16x16x32_bf16 a[52:55], v[76:79], v[48:51], a[52:55]\n"
"ds_read_b64_tr_b16 v[154:155], v162 offset:32768\n"
"v_mfma_f32_16x16x32_bf16 a[56:59], v[64:67], v[28:31], a[56:59]\n"
"ds_read_b64_tr_b16 v[156:157], v163 offset:28672\n"
"v_mfma_f32_16x16x32_bf16 a[56:59], v[80:83], v[48:51], a[56:59]\n"
"ds_read_b64_tr_b16 v[158:159], v163 offset:32768\n"
"v_mfma_f32_16x16x32_bf16 a[60:63], v[68:71], v[28:31], a[60:63]\n"
"buffer_load_dwordx4 v172, s[32:35], s43 offen offset:2048 lds\n"
"v_mfma_f32_16x16x32_bf16 a[60:63], v[84:87], v[48:51], a[60:63]\n"
"s_cmp_eq_u32 s48, 3\n"
"s_cselect_b32 s47, s68, s69\n"
"v_mfma_f32_16x16x32_bf16 a[64:67], v[56:59], v[32:35], a[64:67]\n"
"s_add_u32 s48, s48, 1\n"
"v_mfma_f32_16x16x32_bf16 a[64:67], v[72:75], v[52:55], a[64:67]\n"
"s_cmp_eq_u32 s48, 4\n"
"s_cselect_b32 s48, 0, s48\n"
"v_add_u32 v160, s47, v160\n"
"v_mfma_f32_16x16x32_bf16 a[68:71], v[60:63], v[32:35], a[68:71]\n"
"buffer_load_dwordx4 v173, s[32:35], s43 offen offset:3072 lds\n"
"v_add_u32 v161, s47, v161\n"
"v_mfma_f32_16x16x32_bf16 a[68:71], v[76:79], v[52:55], a[68:71]\n"
"s_add_u32 s42, s42, 2048\n"
"s_add_u32 s43, s43, 81920\n"
"v_add_u32 v162, s47, v162\n"
"v_mfma_f32_16x16x32_bf16 a[72:75], v[64:67], v[32:35], a[72:75]\n"
"s_add_u32 s45, s45, 36864\n"
"s_add_u32 s46, s46, 36864\n"
"v_add_u32 v163, s47, v163\n"
"v_mfma_f32_16x16x32_bf16 a[72:75], v[80:83], v[52:55], a[72:75]\n"
"s_add_u32 s55, s55, 1\n"
"v_add_u32 v164, s47, v164\n"
"v_mfma_f32_16x16x32_bf16 a[76:79], v[68:71], v[32:35], a[76:79]\n"
"s_cmp_eq_u32 s55, 4\n"
"s_cselect_b32 s60, 147456, 0\n"
"s_cselect_b32 s55, 0, s55\n"
"v_mfma_f32_16x16x32_bf16 a[76:79], v[84:87], v[52:55], a[76:79]\n"
"s_sub_u32 s45, s45, s60\n"
"s_sub_u32 s46, s46, s60\n"
"s_waitcnt lgkmcnt(0)\n"
"v_mfma_f32_16x16x32_bf16 a[0:3], v[128:131], v[88:91], a[0:3]\n"
"s_waitcnt vmcnt(18) lgkmcnt(0)\n"
"s_barrier\n"
"v_mfma_f32_16x16x32_bf16 a[0:3], v[144:147], v[108:111], a[0:3]\n"
"s_mov_b32 m0, s45\n"
"ds_read_b128 v[16:19], v164 offset:0\n"
"v_mfma_f32_16x16x32_bf16 a[4:7], v[132:135], v[88:91], a[4:7]\n"
"ds_read_b128 v[20:23], v164 offset:1024\n"
"buffer_load_dwordx4 v165, s[20:23], s42 offen offset:0 sc1 lds\n"
"v_mfma_f32_16x16x32_bf16 a[4:7], v[148:151], v[108:111], a[4:7]\n"
"ds_read_b128 v[24:27], v164 offset:2048\n"
"v_mfma_f32_16x16x32_bf16 a[8:11], v[136:139], v[88:91], a[8:11]\n"
"ds_read_b128 v[28:31], v164 offset:3072\n"
"v_mfma_f32_16x16x32_bf16 a[8:11], v[152:155], v[108:111], a[8:11]\n"
"ds_read_b128 v[32:35], v164 offset:4096\n"
"v_mfma_f32_16x16x32_bf16 a[12:15], v[140:143], v[88:91], a[12:15]\n"
"ds_read_b64_tr_b16 v[56:57], v160 offset:20480\n"
"buffer_load_dwordx4 v166, s[20:23], s42 offen offset:1024 sc1 lds\n"
"v_mfma_f32_16x16x32_bf16 a[12:15], v[156:159], v[108:111], a[12:15]\n"
"ds_read_b64_tr_b16 v[58:59], v160 offset:24576\n"
"v_mfma_f32_16x16x32_bf16 a[16:19], v[128:131], v[92:95], a[16:19]\n"
"v_mfma_f32_16x16x32_bf16 a[16:19], v[144:147], v[112:115], a[16:19]\n"
"ds_read_b64_tr_b16 v[60:61], v161 offset:20480\n"
"v_mfma_f32_16x16x32_bf16 a[20:23], v[132:135], v[92:95], a[20:23]\n"
"ds_read_b64_tr_b16 v[62:63], v161 offset:24576\n"
"buffer_load_dwordx4 v167, s[20:23], s42 offen offset:2048 sc1 lds\n"
"v_mfma_f32_16x16x32_bf16 a[20:23], v[148:151], v[112:115], a[20:23]\n"
"ds_read_b64_tr_b16 v[64:65], v162 offset:20480\n"
"v_mfma_f32_16x16x32_bf16 a[24:27], v[136:139], v[92:95], a[24:27]\n"
"ds_read_b64_tr_b16 v[66:67], v162 offset:24576\n"
"v_mfma_f32_16x16x32_bf16 a[24:27], v[152:155], v[112:115], a[24:27]\n"
"ds_read_b64_tr_b16 v[68:69], v163 offset:20480\n"
"v_mfma_f32_16x16x32_bf16 a[28:31], v[140:143], v[92:95], a[28:31]\n"
"ds_read_b64_tr_b16 v[70:71], v163 offset:24576\n"
"buffer_load_dwordx4 v168, s[20:23], s42 offen offset:3072 sc1 lds\n"
"v_mfma_f32_16x16x32_bf16 a[28:31], v[156:159], v[112:115], a[28:31]\n"
"v_mfma_f32_16x16x32_bf16 a[32:35], v[128:131], v[96:99], a[32:35]\n"
"ds_read_b128 v[36:39], v164 offset:10240\n"
"v_mfma_f32_16x16x32_bf16 a[32:35], v[144:147], v[116:119], a[32:35]\n"
"s_add_u32 m0, s45, 4096\n"
"ds_read_b128 v[40:43], v164 offset:11264\n"
"v_mfma_f32_16x16x32_bf16 a[36:39], v[132:135], v[96:99], a[36:39]\n"
"ds_read_b128 v[44:47], v164 offset:12288\n"
"buffer_load_dwordx4 v169, s[20:23], s42 offen offset:0 sc1 lds\n"
"v_mfma_f32_16x16x32_bf16 a[36:39], v[148:151], v[116:119], a[36:39]\n"
"ds_read_b128 v[48:51], v164 offset:13312\n"
"v_mfma_f32_16x16x32_bf16 a[40:43], v[136:139], v[96:99], a[40:43]\n"
"ds_read_b128 v[52:55], v164 offset:14336\n"
"v_mfma_f32_16x16x32_bf16 a[40:43], v[152:155], v[116:119], a[40:43]\n"
"s_mov_b32 m0, s46\n"
"ds_read_b64_tr_b16 v[72:73], v160 offset:28672\n"
"v_mfma_f32_16x16x32_bf16 a[44:47], v[140:143], v[96:99], a[44:47]\n"
"ds_read_b64_tr_b16 v[74:75], v160 offset:32768\n"
"buffer_load_dwordx4 v170, s[32:35], s43 offen offset:0 lds\n"
"v_mfma_f32_16x16x32_bf16 a[44:47], v[156:159], v[116:119], a[44:47]\n"
"v_mfma_f32_16x16x32_bf16 a[48:51], v[128:131], v[100:103], a[48:51]\n"
"ds_read_b64_tr_b16 v[76:77], v161 offset:28672\n"
"v_mfma_f32_16x16x32_bf16 a[48:51], v[144:147], v[120:123], a[48:51]\n"
"ds_read_b64_tr_b16 v[78:79], v161 offset:32768\n"
"v_mfma_f32_16x16x32_bf16 a[52:55], v[132:135], v[100:103], a[52:55]\n"
"ds_read_b64_tr_b16 v[80:81], v162 offset:28672\n"
"buffer_load_dwordx4 v171, s[32:35], s43 offen offset:1024 lds\n"
"v_mfma_f32_16x16x32_bf16 a[52:55], v[148:151], v[120:123], a[52:55]\n"
"ds_read_b64_tr_b16 v[82:83], v162 offset:32768\n"
"v_mfma_f32_16x16x32_bf16 a[56:59], v[136:139], v[100:103], a[56:59]\n"
"ds_read_b64_tr_b16 v[84:85], v163 offset:28672\n"
"v_mfma_f32_16x16x32_bf16 a[56:59], v[152:155], v[120:123], a[56:59]\n"
"ds_read_b64_tr_b16 v[86:87], v163 offset:32768\n"
"v_mfma_f32_16x16x32_bf16 a[60:63], v[140:143], v[100:103], a[60:63]\n"
"buffer_load_dwordx4 v172, s[32:35], s43 offen offset:2048 lds\n"
"v_mfma_f32_16x16x32_bf16 a[60:63], v[156:159], v[120:123], a[60:63]\n"
"s_cmp_eq_u32 s48, 3\n"
"s_cselect_b32 s47, s68, s69\n"
"v_mfma_f32_16x16x32_bf16 a[64:67], v[128:131], v[104:107], a[64:67]\n"
"s_add_u32 s48, s48, 1\n"
"v_mfma_f32_16x16x32_bf16 a[64:67], v[144:147], v[124:127], a[64:67]\n"
"s_cmp_eq_u32 s48, 4\n"
"s_cselect_b32 s48, 0, s48\n"
"v_add_u32 v160, s47, v160\n"
"v_mfma_f32_16x16x32_bf16 a[68:71], v[132:135], v[104:107], a[68:71]\n"
"buffer_load_dwordx4 v173, s[32:35], s43 offen offset:3072 lds\n"
"v_add_u32 v161, s47, v161\n"
"v_mfma_f32_16x16x32_bf16 a[68:71], v[148:151], v[124:127], a[68:71]\n"
"s_add_u32 s42, s42, 2048\n"
"s_add_u32 s43, s43, 81920\n"
"v_add_u32 v162, s47, v162\n"
"v_mfma_f32_16x16x32_bf16 a[72:75], v[136:139], v[104:107], a[72:75]\n"
"s_add_u32 s45, s45, 36864\n"
"s_add_u32 s46, s46, 36864\n"
"v_add_u32 v163, s47, v163\n"
"v_mfma_f32_16x16x32_bf16 a[72:75], v[152:155], v[124:127], a[72:75]\n"
"s_add_u32 s55, s55, 1\n"
"v_add_u32 v164, s47, v164\n"
"v_mfma_f32_16x16x32_bf16 a[76:79], v[140:143], v[104:107], a[76:79]\n"
"s_cmp_eq_u32 s55, 4\n"
"s_cselect_b32 s60, 147456, 0\n"
"s_cselect_b32 s55, 0, s55\n"
"v_mfma_f32_16x16x32_bf16 a[76:79], v[156:159], v[124:127], a[76:79]\n"
"s_sub_u32 s45, s45, s60\n"
"s_sub_u32 s46, s46, s60\n"
"s_waitcnt lgkmcnt(0)\n"
"v_mfma_f32_16x16x32_bf16 a[0:3], v[56:59], v[16:19], a[0:3]\n"
"s_waitcnt vmcnt(18) lgkmcnt(0)\n"
"s_barrier\n"
"v_mfma_f32_16x16x32_bf16 a[0:3], v[72:75], v[36:39], a[0:3]\n"
"s_mov_b32 m0, s45\n"
"ds_read_b128 v[88:91], v164 offset:0\n"
"v_mfma_f32_16x16x32_bf16 a[4:7], v[60:63], v[16:19], a[4:7]\n"
"ds_read_b128 v[92:95], v164 offset:1024\n"
"buffer_load_dwordx4 v165, s[20:23], s42 offen offset:0 sc1 lds\n"
"v_mfma_f32_16x16x32_bf16 a[4:7], v[76:79], v[36:39], a[4:7]\n"
"ds_read_b128 v[96:99], v164 offset:2048\n"
"v_mfma_f32_16x16x32_bf16 a[8:11], v[64:67], v[16:19], a[8:11]\n"
"ds_read_b128 v[100:103], v164 offset:3072\n"
"v_mfma_f32_16x16x32_bf16 a[8:11], v[80:83], v[36:39], a[8:11]\n"
"ds_read_b128 v[104:107], v164 offset:4096\n"
"v_mfma_f32_16x16x32_bf16 a[12:15], v[68:71], v[16:19], a[12:15]\n"
"ds_read_b64_tr_b16 v[128:129], v160 offset:20480\n"
"buffer_load_dwordx4 v166, s[20:23], s42 offen offset:1024 sc1 lds\n"
"v_mfma_f32_16x16x32_bf16 a[12:15], v[84:87], v[36:39], a[12:15]\n"
"ds_read_b64_tr_b16 v[130:131], v160 offset:24576\n"
"v_mfma_f32_16x16x32_bf16 a[16:19], v[56:59], v[20:23], a[16:19]\n"
"v_mfma_f32_16x16x32_bf16 a[16:19], v[72:75], v[40:43], a[16:19]\n"
"ds_read_b64_tr_b16 v[132:133], v161 offset:20480\n"
"v_mfma_f32_16x16x32_bf16 a[20:23], v[60:63], v[20:23], a[20:23]\n"
"ds_read_b64_tr_b16 v[134:135], v161 offset:24576\n"
"buffer_load_dwordx4 v167, s[20:23], s42 offen offset:2048 sc1 lds\n"
"v_mfma_f32_16x16x32_bf16 a[20:23], v[76:79], v[40:43], a[20:23]\n"
"ds_read_b64_tr_b16 v[136:137], v162 offset:20480\n"
"v_mfma_f32_16x16x32_bf16 a[24:27], v[64:67], v[20:23], a[24:27]\n"
"ds_read_b64_tr_b16 v[138:139], v162 offset:24576\n"
"v_mfma_f32_16x16x32_bf16 a[24:27], v[80:83], v[40:43], a[24:27]\n"
"ds_read_b64_tr_b16 v[140:141], v163 offset:20480\n"
"v_mfma_f32_16x16x32_bf16 a[28:31], v[68:71], v[20:23], a[28:31]\n"
"ds_read_b64_tr_b16 v[142:143], v163 offset:24576\n"
"buffer_load_dwordx4 v168, s[20:23], s42 offen offset:3072 sc1 lds\n"
"v_mfma_f32_16x16x32_bf16 a[28:31], v[84:87], v[40:43], a[28:31]\n"
"v_mfma_f32_16x16x32_bf16 a[32:35], v[56:59], v[24:27], a[32:35]\n"
"ds_read_b128 v[108:111], v164 offset:10240\n"
"v_mfma_f32_16x16x32_bf16 a[32:35], v[72:75], v[44:47], a[32:35]\n"
"s_add_u32 m0, s45, 4096\n"
"ds_read_b128 v[112:115], v164 offset:11264\n"
"v_mfma_f32_16x16x32_bf16 a[36:39], v[60:63], v[24:27], a[36:39]\n"
"ds_read_b128 v[116:119], v164 offset:12288\n"
"buffer_load_dwordx4 v169, s[20:23], s42 offen offset:0 sc1 lds\n"
"v_mfma_f32_16x16x32_bf16 a[36:39], v[76:79], v[44:47], a[36:39]\n"
"ds_read_b128 v[120:123], v164 offset:13312\n"
"v_mfma_f32_16x16x32_bf16 a[40:43], v[64:67], v[24:27], a[40:43]\n"
"ds_read_b128 v[124:127], v164 offset:14336\n"
"v_mfma_f32_16x16x32_bf16 a[40:43], v[80:83], v[44:47], a[40:43]\n"
"s_mov_b32 m0, s46\n"
"ds_read_b64_tr_b16 v[144:145], v160 offset:28672\n"
"v_mfma_f32_16x16x32_bf16 a[44:47], v[68:71], v[24:27], a[44:47]\n"
"ds_read_b64_tr_b16 v[146:147], v160 offset:32768\n"
"buffer_load_dwordx4 v170, s[32:35], s43 offen offset:0 lds\n"
"v_mfma_f32_16x16x32_bf16 a[44:47], v[84:87], v[44:47], a[44:47]\n"
"v_mfma_f32_16x16x32_bf16 a[48:51], v[56:59], v[28:31], a[48:51]\n"
"ds_read_b64_tr_b16 v[148:149], v161 offset:28672\n"
"v_mfma_f32_16x16x32_bf16 a[48:51], v[72:75], v[48:51], a[48:51]\n"
"ds_read_b64_tr_b16 v[150:151], v161 offset:32768\n"
"v_mfma_f32_16x16x32_bf16 a[52:55], v[60:63], v[28:31], a[52:55]\n"
"ds_read_b64_tr_b16 v[152:153], v162 offset:28672\n"
"buffer_load_dwordx4 v171, s[32:35], s43 offen offset:1024 lds\n"
"v_mfma_f32_16x16x32_bf16 a[52:55], v[76:79], v[48:51], a[52:55]\n"
"ds_read_b64_tr_b16 v[154:155], v162 offset:32768\n"
"v_mfma_f32_16x16x32_bf16 a[56:59], v[64:67], v[28:31], a[56:59]\n"
"ds_read_b64_tr_b16 v[156:157], v163 offset:28672\n"
"v_mfma_f32_16x16x32_bf16 a[56:59], v[80:83], v[48:51], a[56:59]\n"
"ds_read_b64_tr_b16 v[158:159], v163 offset:32768\n"
"v_mfma_f32_16x16x32_bf16 a[60:63], v[68:71], v[28:31], a[60:63]\n"
"buffer_load_dwordx4 v172, s[32:35], s43 offen offset:2048 lds\n"
"v_mfma_f32_16x16x32_bf16 a[60:63], v[84:87], v[48:51], a[60:63]\n"
"s_cmp_eq_u32 s48, 3\n"
"s_cselect_b32 s47, s68, s69\n"
"v_mfma_f32_16x16x32_bf16 a[64:67], v[56:59], v[32:35], a[64:67]\n"
"s_add_u32 s48, s48, 1\n"
"v_mfma_f32_16x16x32_bf16 a[64:67], v[72:75], v[52:55], a[64:67]\n"
"s_cmp_eq_u32 s48, 4\n"
"s_cselect_b32 s48, 0, s48\n"
"v_add_u32 v160, s47, v160\n"
"v_mfma_f32_16x16x32_bf16 a[68:71], v[60:63], v[32:35], a[68:71]\n"
"buffer_load_dwordx4 v173, s[32:35], s43 offen offset:3072 lds\n"
"v_add_u32 v161, s47, v161\n"
"v_mfma_f32_16x16x32_bf16 a[68:71], v[76:79], v[52:55], a[68:71]\n"
"s_add_u32 s42, s42, 2048\n"
"s_add_u32 s43, s43, 81920\n"
"v_add_u32 v162, s47, v162\n"
"v_mfma_f32_16x16x32_bf16 a[72:75], v[64:67], v[32:35], a[72:75]\n"
"s_add_u32 s45, s45, 36864\n"
"s_add_u32 s46, s46, 36864\n"
"v_add_u32 v163, s47, v163\n"
"v_mfma_f32_16x16x32_bf16 a[72:75], v[80:83], v[52:55], a[72:75]\n"
"s_add_u32 s55, s55, 1\n"
"v_add_u32 v164, s47, v164\n"
"v_mfma_f32_16x16x32_bf16 a[76:79], v[68:71], v[32:35], a[76:79]\n"
"s_cmp_eq_u32 s55, 4\n"
"s_cselect_b32 s60, 147456, 0\n"
"s_cselect_b32 s55, 0, s55\n"
"v_mfma_f32_16x16x32_bf16 a[76:79], v[84:87], v[52:55], a[76:79]\n"
"s_sub_u32 s45, s45, s60\n"
"s_sub_u32 s46, s46, s60\n"
"s_waitcnt lgkmcnt(0)\n"
"v_mfma_f32_16x16x32_bf16 a[0:3], v[128:131], v[88:91], a[0:3]\n"
"s_waitcnt vmcnt(18) lgkmcnt(0)\n"
"s_barrier\n"
"v_mfma_f32_16x16x32_bf16 a[0:3], v[144:147], v[108:111], a[0:3]\n"
"s_mov_b32 m0, s45\n"
"ds_read_b128 v[16:19], v164 offset:0\n"
"v_mfma_f32_16x16x32_bf16 a[4:7], v[132:135], v[88:91], a[4:7]\n"
"ds_read_b128 v[20:23], v164 offset:1024\n"
"buffer_load_dwordx4 v165, s[20:23], s42 offen offset:0 sc1 lds\n"
"v_mfma_f32_16x16x32_bf16 a[4:7], v[148:151], v[108:111], a[4:7]\n"
"ds_read_b128 v[24:27], v164 offset:2048\n"
"v_mfma_f32_16x16x32_bf16 a[8:11], v[136:139], v[88:91], a[8:11]\n"
"ds_read_b128 v[28:31], v164 offset:3072\n"
"v_mfma_f32_16x16x32_bf16 a[8:11], v[152:155], v[108:111], a[8:11]\n"
"ds_read_b128 v[32:35], v164 offset:4096\n"
"v_mfma_f32_16x16x32_bf16 a[12:15], v[140:143], v[88:91], a[12:15]\n"
"ds_read_b64_tr_b16 v[56:57], v160 offset:20480\n"
"buffer_load_dwordx4 v166, s[20:23], s42 offen offset:1024 sc1 lds\n"
"v_mfma_f32_16x16x32_bf16 a[12:15], v[156:159], v[108:111], a[12:15]\n"
"ds_read_b64_tr_b16 v[58:59], v160 offset:24576\n"
"v_mfma_f32_16x16x32_bf16 a[16:19], v[128:131], v[92:95], a[16:19]\n"
"v_mfma_f32_16x16x32_bf16 a[16:19], v[144:147], v[112:115], a[16:19]\n"
"ds_read_b64_tr_b16 v[60:61], v161 offset:20480\n"
"v_mfma_f32_16x16x32_bf16 a[20:23], v[132:135], v[92:95], a[20:23]\n"
"ds_read_b64_tr_b16 v[62:63], v161 offset:24576\n"
"buffer_load_dwordx4 v167, s[20:23], s42 offen offset:2048 sc1 lds\n"
"v_mfma_f32_16x16x32_bf16 a[20:23], v[148:151], v[112:115], a[20:23]\n"
"ds_read_b64_tr_b16 v[64:65], v162 offset:20480\n"
"v_mfma_f32_16x16x32_bf16 a[24:27], v[136:139], v[92:95], a[24:27]\n"
"ds_read_b64_tr_b16 v[66:67], v162 offset:24576\n"
"v_mfma_f32_16x16x32_bf16 a[24:27], v[152:155], v[112:115], a[24:27]\n"
"ds_read_b64_tr_b16 v[68:69], v163 offset:20480\n"
"v_mfma_f32_16x16x32_bf16 a[28:31], v[140:143], v[92:95], a[28:31]\n"
"ds_read_b64_tr_b16 v[70:71], v163 offset:24576\n"
"buffer_load_dwordx4 v168, s[20:23], s42 offen offset:3072 sc1 lds\n"
"v_mfma_f32_16x16x32_bf16 a[28:31], v[156:159], v[112:115], a[28:31]\n"
"v_mfma_f32_16x16x32_bf16 a[32:35], v[128:131], v[96:99], a[32:35]\n"
"ds_read_b128 v[36:39], v164 offset:10240\n"
"v_mfma_f32_16x16x32_bf16 a[32:35], v[144:147], v[116:119], a[32:35]\n"
"s_add_u32 m0, s45, 4096\n"
"ds_read_b128 v[40:43], v164 offset:11264\n"
"v_mfma_f32_16x16x32_bf16 a[36:39], v[132:135], v[96:99], a[36:39]\n"
"ds_read_b128 v[44:47], v164 offset:12288\n"
"buffer_load_dwordx4 v169, s[20:23], s42 offen offset:0 sc1 lds\n"
"v_mfma_f32_16x16x32_bf16 a[36:39], v[148:151], v[116:119], a[36:39]\n"
"ds_read_b128 v[48:51], v164 offset:13312\n"
"v_mfma_f32_16x16x32_bf16 a[40:43], v[136:139], v[96:99], a[40:43]\n"
"ds_read_b128 v[52:55], v164 offset:14336\n"
"v_mfma_f32_16x16x32_bf16 a[40:43], v[152:155], v[116:119], a[40:43]\n"
"s_mov_b32 m0, s46\n"
"ds_read_b64_tr_b16 v[72:73], v160 offset:28672\n"
"v_mfma_f32_16x16x32_bf16 a[44:47], v[140:143], v[96:99], a[44:47]\n"
"ds_read_b64_tr_b16 v[74:75], v160 offset:32768\n"
"buffer_load_dwordx4 v170, s[32:35], s43 offen offset:0 lds\n"
"v_mfma_f32_16x16x32_bf16 a[44:47], v[156:159], v[116:119], a[44:47]\n"
"v_mfma_f32_16x16x32_bf16 a[48:51], v[128:131], v[100:103], a[48:51]\n"
"ds_read_b64_tr_b16 v[76:77], v161 offset:28672\n"
"v_mfma_f32_16x16x32_bf16 a[48:51], v[144:147], v[120:123], a[48:51]\n"
"ds_read_b64_tr_b16 v[78:79], v161 offset:32768\n"
"v_mfma_f32_16x16x32_bf16 a[52:55], v[132:135], v[100:103], a[52:55]\n"
"ds_read_b64_tr_b16 v[80:81], v162 offset:28672\n"
"buffer_load_dwordx4 v171, s[32:35], s43 offen offset:1024 lds\n"
"v_mfma_f32_16x16x32_bf16 a[52:55], v[148:151], v[120:123], a[52:55]\n"
"ds_read_b64_tr_b16 v[82:83], v162 offset:32768\n"
"v_mfma_f32_16x16x32_bf16 a[56:59], v[136:139], v[100:103], a[56:59]\n"
"ds_read_b64_tr_b16 v[84:85], v163 offset:28672\n"
"v_mfma_f32_16x16x32_bf16 a[56:59], v[152:155], v[120:123], a[56:59]\n"
"ds_read_b64_tr_b16 v[86:87], v163 offset:32768\n"
"v_mfma_f32_16x16x32_bf16 a[60:63], v[140:143], v[100:103], a[60:63]\n"
"buffer_load_dwordx4 v172, s[32:35], s43 offen offset:2048 lds\n"
"v_mfma_f32_16x16x32_bf16 a[60:63], v[156:159], v[120:123], a[60:63]\n"
"s_cmp_eq_u32 s48, 3\n"
"s_cselect_b32 s47, s68, s69\n"
"v_mfma_f32_16x16x32_bf16 a[64:67], v[128:131], v[104:107], a[64:67]\n"
"s_add_u32 s48, s48, 1\n"
"v_mfma_f32_16x16x32_bf16 a[64:67], v[144:147], v[124:127], a[64:67]\n"
"s_cmp_eq_u32 s48, 4\n"
"s_cselect_b32 s48, 0, s48\n"
"v_add_u32 v160, s47, v160\n"
"v_mfma_f32_16x16x32_bf16 a[68:71], v[132:135], v[104:107], a[68:71]\n"
"buffer_load_dwordx4 v173, s[32:35], s43 offen offset:3072 lds\n"
"v_add_u32 v161, s47, v161\n"
"v_mfma_f32_16x16x32_bf16 a[68:71], v[148:151], v[124:127], a[68:71]\n"
"s_add_u32 s42, s42, 2048\n"
"s_add_u32 s43, s43, 81920\n"
"v_add_u32 v162, s47, v162\n"
"v_mfma_f32_16x16x32_bf16 a[72:75], v[136:139], v[104:107], a[72:75]\n"
"s_add_u32 s45, s45, 36864\n"
"s_add_u32 s46, s46, 36864\n"
"v_add_u32 v163, s47, v163\n"
"v_mfma_f32_16x16x32_bf16 a[72:75], v[152:155], v[124:127], a[72:75]\n"
"s_add_u32 s55, s55, 1\n"
"v_add_u32 v164, s47, v164\n"
"v_mfma_f32_16x16x32_bf16 a[76:79], v[140:143], v[104:107], a[76:79]\n"
"s_cmp_eq_u32 s55, 4\n"
"s_cselect_b32 s60, 147456, 0\n"
"s_cselect_b32 s55, 0, s55\n"
"v_mfma_f32_16x16x32_bf16 a[76:79], v[156:159], v[124:127], a[76:79]\n"
"s_sub_u32 s45, s45, s60\n"
"s_sub_u32 s46, s46, s60\n"
"s_waitcnt lgkmcnt(0)\n"
"v_mfma_f32_16x16x32_bf16 a[0:3], v[56:59], v[16:19], a[0:3]\n"
"s_waitcnt vmcnt(18) lgkmcnt(0)\n"
"s_barrier\n"
"v_mfma_f32_16x16x32_bf16 a[0:3], v[72:75], v[36:39], a[0:3]\n"
"s_mov_b32 m0, s45\n"
"ds_read_b128 v[88:91], v164 offset:0\n"
"v_mfma_f32_16x16x32_bf16 a[4:7], v[60:63], v[16:19], a[4:7]\n"
"ds_read_b128 v[92:95], v164 offset:1024\n"
"buffer_load_dwordx4 v165, s[20:23], s42 offen offset:0 sc1 lds\n"
"v_mfma_f32_16x16x32_bf16 a[4:7], v[76:79], v[36:39], a[4:7]\n"
"ds_read_b128 v[96:99], v164 offset:2048\n"
"v_mfma_f32_16x16x32_bf16 a[8:11], v[64:67], v[16:19], a[8:11]\n"
"ds_read_b128 v[100:103], v164 offset:3072\n"
"v_mfma_f32_16x16x32_bf16 a[8:11], v[80:83], v[36:39], a[8:11]\n"
"ds_read_b128 v[104:107], v164 offset:4096\n"
"v_mfma_f32_16x16x32_bf16 a[12:15], v[68:71], v[16:19], a[12:15]\n"
"ds_read_b64_tr_b16 v[128:129], v160 offset:20480\n"
"buffer_load_dwordx4 v166, s[20:23], s42 offen offset:1024 sc1 lds\n"
"v_mfma_f32_16x16x32_bf16 a[12:15], v[84:87], v[36:39], a[12:15]\n"
"ds_read_b64_tr_b16 v[130:131], v160 offset:24576\n"
"v_mfma_f32_16x16x32_bf16 a[16:19], v[56:59], v[20:23], a[16:19]\n"
"v_mfma_f32_16x16x32_bf16 a[16:19], v[72:75], v[40:43], a[16:19]\n"
"ds_read_b64_tr_b16 v[132:133], v161 offset:20480\n"
"v_mfma_f32_16x16x32_bf16 a[20:23], v[60:63], v[20:23], a[20:23]\n"
"ds_read_b64_tr_b16 v[134:135], v161 offset:24576\n"
"buffer_load_dwordx4 v167, s[20:23], s42 offen offset:2048 sc1 lds\n"
"v_mfma_f32_16x16x32_bf16 a[20:23], v[76:79], v[40:43], a[20:23]\n"
"ds_read_b64_tr_b16 v[136:137], v162 offset:20480\n"
"v_mfma_f32_16x16x32_bf16 a[24:27], v[64:67], v[20:23], a[24:27]\n"
"ds_read_b64_tr_b16 v[138:139], v162 offset:24576\n"
"v_mfma_f32_16x16x32_bf16 a[24:27], v[80:83], v[40:43], a[24:27]\n"
"ds_read_b64_tr_b16 v[140:141], v163 offset:20480\n"
"v_mfma_f32_16x16x32_bf16 a[28:31], v[68:71], v[20:23], a[28:31]\n"
"ds_read_b64_tr_b16 v[142:143], v163 offset:24576\n"
"buffer_load_dwordx4 v168, s[20:23], s42 offen offset:3072 sc1 lds\n"
"v_mfma_f32_16x16x32_bf16 a[28:31], v[84:87], v[40:43], a[28:31]\n"
"v_mfma_f32_16x16x32_bf16 a[32:35], v[56:59], v[24:27], a[32:35]\n"
"ds_read_b128 v[108:111], v164 offset:10240\n"
"v_mfma_f32_16x16x32_bf16 a[32:35], v[72:75], v[44:47], a[32:35]\n"
"s_add_u32 m0, s45, 4096\n"
"ds_read_b128 v[112:115], v164 offset:11264\n"
"v_mfma_f32_16x16x32_bf16 a[36:39], v[60:63], v[24:27], a[36:39]\n"
"ds_read_b128 v[116:119], v164 offset:12288\n"
"buffer_load_dwordx4 v169, s[20:23], s42 offen offset:0 sc1 lds\n"
"v_mfma_f32_16x16x32_bf16 a[36:39], v[76:79], v[44:47], a[36:39]\n"
"ds_read_b128 v[120:123], v164 offset:13312\n"
"v_mfma_f32_16x16x32_bf16 a[40:43], v[64:67], v[24:27], a[40:43]\n"
"ds_read_b128 v[124:127], v164 offset:14336\n"
"v_mfma_f32_16x16x32_bf16 a[40:43], v[80:83], v[44:47], a[40:43]\n"
"s_mov_b32 m0, s46\n"
"ds_read_b64_tr_b16 v[144:145], v160 offset:28672\n"
"v_mfma_f32_16x16x32_bf16 a[44:47], v[68:71], v[24:27], a[44:47]\n"
"ds_read_b64_tr_b16 v[146:147], v160 offset:32768\n"
"buffer_load_dwordx4 v170, s[32:35], s43 offen offset:0 lds\n"
"v_mfma_f32_16x16x32_bf16 a[44:47], v[84:87], v[44:47], a[44:47]\n"
"v_mfma_f32_16x16x32_bf16 a[48:51], v[56:59], v[28:31], a[48:51]\n"
"ds_read_b64_tr_b16 v[148:149], v161 offset:28672\n"
"v_mfma_f32_16x16x32_bf16 a[48:51], v[72:75], v[48:51], a[48:51]\n"
"ds_read_b64_tr_b16 v[150:151], v161 offset:32768\n"
"v_mfma_f32_16x16x32_bf16 a[52:55], v[60:63], v[28:31], a[52:55]\n"
"ds_read_b64_tr_b16 v[152:153], v162 offset:28672\n"
"buffer_load_dwordx4 v171, s[32:35], s43 offen offset:1024 lds\n"
"v_mfma_f32_16x16x32_bf16 a[52:55], v[76:79], v[48:51], a[52:55]\n"
"ds_read_b64_tr_b16 v[154:155], v162 offset:32768\n"
"v_mfma_f32_16x16x32_bf16 a[56:59], v[64:67], v[28:31], a[56:59]\n"
"ds_read_b64_tr_b16 v[156:157], v163 offset:28672\n"
"v_mfma_f32_16x16x32_bf16 a[56:59], v[80:83], v[48:51], a[56:59]\n"
"ds_read_b64_tr_b16 v[158:159], v163 offset:32768\n"
"v_mfma_f32_16x16x32_bf16 a[60:63], v[68:71], v[28:31], a[60:63]\n"
"buffer_load_dwordx4 v172, s[32:35], s43 offen offset:2048 lds\n"
"v_mfma_f32_16x16x32_bf16 a[60:63], v[84:87], v[48:51], a[60:63]\n"
"s_cmp_eq_u32 s48, 3\n"
"s_cselect_b32 s47, s68, s69\n"
"v_mfma_f32_16x16x32_bf16 a[64:67], v[56:59], v[32:35], a[64:67]\n"
"s_add_u32 s48, s48, 1\n"
"v_mfma_f32_16x16x32_bf16 a[64:67], v[72:75], v[52:55], a[64:67]\n"
"s_cmp_eq_u32 s48, 4\n"
"s_cselect_b32 s48, 0, s48\n"
"v_add_u32 v160, s47, v160\n"
"v_mfma_f32_16x16x32_bf16 a[68:71], v[60:63], v[32:35], a[68:71]\n"
"buffer_load_dwordx4 v173, s[32:35], s43 offen offset:3072 lds\n"
"v_add_u32 v161, s47, v161\n"
"v_mfma_f32_16x16x32_bf16 a[68:71], v[76:79], v[52:55], a[68:71]\n"
"s_add_u32 s42, s42, 2048\n"
"s_add_u32 s43, s43, 81920\n"
"v_add_u32 v162, s47, v162\n"
"v_mfma_f32_16x16x32_bf16 a[72:75], v[64:67], v[32:35], a[72:75]\n"
"s_add_u32 s45, s45, 36864\n"
"s_add_u32 s46, s46, 36864\n"
"v_add_u32 v163, s47, v163\n"
"v_mfma_f32_16x16x32_bf16 a[72:75], v[80:83], v[52:55], a[72:75]\n"
"s_add_u32 s55, s55, 1\n"
"v_add_u32 v164, s47, v164\n"
"v_mfma_f32_16x16x32_bf16 a[76:79], v[68:71], v[32:35], a[76:79]\n"
"s_cmp_eq_u32 s55, 4\n"
"s_cselect_b32 s60, 147456, 0\n"
"s_cselect_b32 s55, 0, s55\n"
"v_mfma_f32_16x16x32_bf16 a[76:79], v[84:87], v[52:55], a[76:79]\n"
"s_sub_u32 s45, s45, s60\n"
"s_sub_u32 s46, s46, s60\n"
"s_waitcnt lgkmcnt(0)\n"
"v_mfma_f32_16x16x32_bf16 a[0:3], v[128:131], v[88:91], a[0:3]\n"
"s_waitcnt vmcnt(18) lgkmcnt(0)\n"
"s_barrier\n"
"v_mfma_f32_16x16x32_bf16 a[0:3], v[144:147], v[108:111], a[0:3]\n"
"s_mov_b32 m0, s45\n"
"ds_read_b128 v[16:19], v164 offset:0\n"
"v_mfma_f32_16x16x32_bf16 a[4:7], v[132:135], v[88:91], a[4:7]\n"
"ds_read_b128 v[20:23], v164 offset:1024\n"
"buffer_load_dwordx4 v165, s[20:23], s42 offen offset:0 sc1 lds\n"
"v_mfma_f32_16x16x32_bf16 a[4:7], v[148:151], v[108:111], a[4:7]\n"
"ds_read_b128 v[24:27], v164 offset:2048\n"
"v_mfma_f32_16x16x32_bf16 a[8:11], v[136:139], v[88:91], a[8:11]\n"
"ds_read_b128 v[28:31], v164 offset:3072\n"
"v_mfma_f32_16x16x32_bf16 a[8:11], v[152:155], v[108:111], a[8:11]\n"
"ds_read_b128 v[32:35], v164 offset:4096\n"
"v_mfma_f32_16x16x32_bf16 a[12:15], v[140:143], v[88:91], a[12:15]\n"
"ds_read_b64_tr_b16 v[56:57], v160 offset:20480\n"
"buffer_load_dwordx4 v166, s[20:23], s42 offen offset:1024 sc1 lds\n"
"v_mfma_f32_16x16x32_bf16 a[12:15], v[156:159], v[108:111], a[12:15]\n"
"ds_read_b64_tr_b16 v[58:59], v160 offset:24576\n"
"v_mfma_f32_16x16x32_bf16 a[16:19], v[128:131], v[92:95], a[16:19]\n"
"v_mfma_f32_16x16x32_bf16 a[16:19], v[144:147], v[112:115], a[16:19]\n"
"ds_read_b64_tr_b16 v[60:61], v161 offset:20480\n"
"v_mfma_f32_16x16x32_bf16 a[20:23], v[132:135], v[92:95], a[20:23]\n"
"ds_read_b64_tr_b16 v[62:63], v161 offset:24576\n"
"buffer_load_dwordx4 v167, s[20:23], s42 offen offset:2048 sc1 lds\n"
"v_mfma_f32_16x16x32_bf16 a[20:23], v[148:151], v[112:115], a[20:23]\n"
"ds_read_b64_tr_b16 v[64:65], v162 offset:20480\n"
"v_mfma_f32_16x16x32_bf16 a[24:27], v[136:139], v[92:95], a[24:27]\n"
"ds_read_b64_tr_b16 v[66:67], v162 offset:24576\n"
"v_mfma_f32_16x16x32_bf16 a[24:27], v[152:155], v[112:115], a[24:27]\n"
"ds_read_b64_tr_b16 v[68:69], v163 offset:20480\n"
"v_mfma_f32_16x16x32_bf16 a[28:31], v[140:143], v[92:95], a[28:31]\n"
"ds_read_b64_tr_b16 v[70:71], v163 offset:24576\n"
"buffer_load_dwordx4 v168, s[20:23], s42 offen offset:3072 sc1 lds\n"
"v_mfma_f32_16x16x32_bf16 a[28:31], v[156:159], v[112:115], a[28:31]\n"
"v_mfma_f32_16x16x32_bf16 a[32:35], v[128:131], v[96:99], a[32:35]\n"
"ds_read_b128 v[36:39], v164 offset:10240\n"
"v_mfma_f32_16x16x32_bf16 a[32:35], v[144:147], v[116:119], a[32:35]\n"
"s_add_u32 m0, s45, 4096\n"
"ds_read_b128 v[40:43], v164 offset:11264\n"
"v_mfma_f32_16x16x32_bf16 a[36:39], v[132:135], v[96:99], a[36:39]\n"
"ds_read_b128 v[44:47], v164 offset:12288\n"
"buffer_load_dwordx4 v169, s[20:23], s42 offen offset:0 sc1 lds\n"
"v_mfma_f32_16x16x32_bf16 a[36:39], v[148:151], v[116:119], a[36:39]\n"
"ds_read_b128 v[48:51], v164 offset:13312\n"
"v_mfma_f32_16x16x32_bf16 a[40:43], v[136:139], v[96:99], a[40:43]\n"
"ds_read_b128 v[52:55], v164 offset:14336\n"
"v_mfma_f32_16x16x32_bf16 a[40:43], v[152:155], v[116:119], a[40:43]\n"
"s_mov_b32 m0, s46\n"
"ds_read_b64_tr_b16 v[72:73], v160 offset:28672\n"
"v_mfma_f32_16x16x32_bf16 a[44:47], v[140:143], v[96:99], a[44:47]\n"
"ds_read_b64_tr_b16 v[74:75], v160 offset:32768\n"
"buffer_load_dwordx4 v170, s[32:35], s43 offen offset:0 lds\n"
"v_mfma_f32_16x16x32_bf16 a[44:47], v[156:159], v[116:119], a[44:47]\n"
"v_mfma_f32_16x16x32_bf16 a[48:51], v[128:131], v[100:103], a[48:51]\n"
"ds_read_b64_tr_b16 v[76:77], v161 offset:28672\n"
"v_mfma_f32_16x16x32_bf16 a[48:51], v[144:147], v[120:123], a[48:51]\n"
"ds_read_b64_tr_b16 v[78:79], v161 offset:32768\n"
"v_mfma_f32_16x16x32_bf16 a[52:55], v[132:135], v[100:103], a[52:55]\n"
"ds_read_b64_tr_b16 v[80:81], v162 offset:28672\n"
"buffer_load_dwordx4 v171, s[32:35], s43 offen offset:1024 lds\n"
"v_mfma_f32_16x16x32_bf16 a[52:55], v[148:151], v[120:123], a[52:55]\n"
"ds_read_b64_tr_b16 v[82:83], v162 offset:32768\n"
"v_mfma_f32_16x16x32_bf16 a[56:59], v[136:139], v[100:103], a[56:59]\n"
"ds_read_b64_tr_b16 v[84:85], v163 offset:28672\n"
"v_mfma_f32_16x16x32_bf16 a[56:59], v[152:155], v[120:123], a[56:59]\n"
"ds_read_b64_tr_b16 v[86:87], v163 offset:32768\n"
"v_mfma_f32_16x16x32_bf16 a[60:63], v[140:143], v[100:103], a[60:63]\n"
"buffer_load_dwordx4 v172, s[32:35], s43 offen offset:2048 lds\n"
"v_mfma_f32_16x16x32_bf16 a[60:63], v[156:159], v[120:123], a[60:63]\n"
"s_cmp_eq_u32 s48, 3\n"
"s_cselect_b32 s47, s68, s69\n"
"v_mfma_f32_16x16x32_bf16 a[64:67], v[128:131], v[104:107], a[64:67]\n"
"s_add_u32 s48, s48, 1\n"
"v_mfma_f32_16x16x32_bf16 a[64:67], v[144:147], v[124:127], a[64:67]\n"
"s_cmp_eq_u32 s48, 4\n"
"s_cselect_b32 s48, 0, s48\n"
"v_add_u32 v160, s47, v160\n"
"v_mfma_f32_16x16x32_bf16 a[68:71], v[132:135], v[104:107], a[68:71]\n"
"buffer_load_dwordx4 v173, s[32:35], s43 offen offset:3072 lds\n"
"v_add_u32 v161, s47, v161\n"
"v_mfma_f32_16x16x32_bf16 a[68:71], v[148:151], v[124:127], a[68:71]\n"
"s_add_u32 s42, s42, 2048\n"
"s_add_u32 s43, s43, 81920\n"
"v_add_u32 v162, s47, v162\n"
"v_mfma_f32_16x16x32_bf16 a[72:75], v[136:139], v[104:107], a[72:75]\n"
"s_add_u32 s45, s45, 36864\n"
"s_add_u32 s46, s46, 36864\n"
"v_add_u32 v163, s47, v163\n"
"v_mfma_f32_16x16x32_bf16 a[72:75], v[152:155], v[124:127], a[72:75]\n"
"s_add_u32 s55, s55, 1\n"
"v_add_u32 v164, s47, v164\n"
"v_mfma_f32_16x16x32_bf16 a[76:79], v[140:143], v[104:107], a[76:79]\n"
"s_cmp_eq_u32 s55, 4\n"
"s_cselect_b32 s60, 147456, 0\n"
"s_cselect_b32 s55, 0, s55\n"
"v_mfma_f32_16x16x32_bf16 a[76:79], v[156:159], v[124:127], a[76:79]\n"
"s_sub_u32 s45, s45, s60\n"
"s_sub_u32 s46, s46, s60\n"
"s_waitcnt lgkmcnt(0)\n"
"v_mfma_f32_16x16x32_bf16 a[0:3], v[56:59], v[16:19], a[0:3]\n"
"s_waitcnt vmcnt(18) lgkmcnt(0)\n"
"s_barrier\n"
"v_mfma_f32_16x16x32_bf16 a[0:3], v[72:75], v[36:39], a[0:3]\n"
"s_mov_b32 m0, s45\n"
"ds_read_b128 v[88:91], v164 offset:0\n"
"v_mfma_f32_16x16x32_bf16 a[4:7], v[60:63], v[16:19], a[4:7]\n"
"ds_read_b128 v[92:95], v164 offset:1024\n"
"buffer_load_dwordx4 v165, s[20:23], s42 offen offset:0 sc1 lds\n"
"v_mfma_f32_16x16x32_bf16 a[4:7], v[76:79], v[36:39], a[4:7]\n"
"ds_read_b128 v[96:99], v164 offset:2048\n"
"v_mfma_f32_16x16x32_bf16 a[8:11], v[64:67], v[16:19], a[8:11]\n"
"ds_read_b128 v[100:103], v164 offset:3072\n"
"v_mfma_f32_16x16x32_bf16 a[8:11], v[80:83], v[36:39], a[8:11]\n"
"ds_read_b128 v[104:107], v164 offset:4096\n"
"v_mfma_f32_16x16x32_bf16 a[12:15], v[68:71], v[16:19], a[12:15]\n"
"ds_read_b64_tr_b16 v[128:129], v160 offset:20480\n"
"buffer_load_dwordx4 v166, s[20:23], s42 offen offset:1024 sc1 lds\n"
"v_mfma_f32_16x16x32_bf16 a[12:15], v[84:87], v[36:39], a[12:15]\n"
"ds_read_b64_tr_b16 v[130:131], v160 offset:24576\n"
"v_mfma_f32_16x16x32_bf16 a[16:19], v[56:59], v[20:23], a[16:19]\n"
"v_mfma_f32_16x16x32_bf16 a[16:19], v[72:75], v[40:43], a[16:19]\n"
"ds_read_b64_tr_b16 v[132:133], v161 offset:20480\n"
"v_mfma_f32_16x16x32_bf16 a[20:23], v[60:63], v[20:23], a[20:23]\n"
"ds_read_b64_tr_b16 v[134:135], v161 offset:24576\n"
"buffer_load_dwordx4 v167, s[20:23], s42 offen offset:2048 sc1 lds\n"
"v_mfma_f32_16x16x32_bf16 a[20:23], v[76:79], v[40:43], a[20:23]\n"
"ds_read_b64_tr_b16 v[136:137], v162 offset:20480\n"
"v_mfma_f32_16x16x32_bf16 a[24:27], v[64:67], v[20:23], a[24:27]\n"
"ds_read_b64_tr_b16 v[138:139], v162 offset:24576\n"
"v_mfma_f32_16x16x32_bf16 a[24:27], v[80:83], v[40:43], a[24:27]\n"
"ds_read_b64_tr_b16 v[140:141], v163 offset:20480\n"
"v_mfma_f32_16x16x32_bf16 a[28:31], v[68:71], v[20:23], a[28:31]\n"
"ds_read_b64_tr_b16 v[142:143], v163 offset:24576\n"
"buffer_load_dwordx4 v168, s[20:23], s42 offen offset:3072 sc1 lds\n"
"v_mfma_f32_16x16x32_bf16 a[28:31], v[84:87], v[40:43], a[28:31]\n"
"v_mfma_f32_16x16x32_bf16 a[32:35], v[56:59], v[24:27], a[32:35]\n"
"ds_read_b128 v[108:111], v164 offset:10240\n"
"v_mfma_f32_16x16x32_bf16 a[32:35], v[72:75], v[44:47], a[32:35]\n"
"s_add_u32 m0, s45, 4096\n"
"ds_read_b128 v[112:115], v164 offset:11264\n"
"v_mfma_f32_16x16x32_bf16 a[36:39], v[60:63], v[24:27], a[36:39]\n"
"ds_read_b128 v[116:119], v164 offset:12288\n"
"buffer_load_dwordx4 v169, s[20:23], s42 offen offset:0 sc1 lds\n"
"v_mfma_f32_16x16x32_bf16 a[36:39], v[76:79], v[44:47], a[36:39]\n"
"ds_read_b128 v[120:123], v164 offset:13312\n"
"v_mfma_f32_16x16x32_bf16 a[40:43], v[64:67], v[24:27], a[40:43]\n"
"ds_read_b128 v[124:127], v164 offset:14336\n"
"v_mfma_f32_16x16x32_bf16 a[40:43], v[80:83], v[44:47], a[40:43]\n"
"s_mov_b32 m0, s46\n"
"ds_read_b64_tr_b16 v[144:145], v160 offset:28672\n"
"v_mfma_f32_16x16x32_bf16 a[44:47], v[68:71], v[24:27], a[44:47]\n"
"ds_read_b64_tr_b16 v[146:147], v160 offset:32768\n"
"buffer_load_dwordx4 v170, s[32:35], s43 offen offset:0 lds\n"
"v_mfma_f32_16x16x32_bf16 a[44:47], v[84:87], v[44:47], a[44:47]\n"
"v_mfma_f32_16x16x32_bf16 a[48:51], v[56:59], v[28:31], a[48:51]\n"
"ds_read_b64_tr_b16 v[148:149], v161 offset:28672\n"
"v_mfma_f32_16x16x32_bf16 a[48:51], v[72:75], v[48:51], a[48:51]\n"
"ds_read_b64_tr_b16 v[150:151], v161 offset:32768\n"
"v_mfma_f32_16x16x32_bf16 a[52:55], v[60:63], v[28:31], a[52:55]\n"
"ds_read_b64_tr_b16 v[152:153], v162 offset:28672\n"
"buffer_load_dwordx4 v171, s[32:35], s43 offen offset:1024 lds\n"
"v_mfma_f32_16x16x32_bf16 a[52:55], v[76:79], v[48:51], a[52:55]\n"
"ds_read_b64_tr_b16 v[154:155], v162 offset:32768\n"
"v_mfma_f32_16x16x32_bf16 a[56:59], v[64:67], v[28:31], a[56:59]\n"
"ds_read_b64_tr_b16 v[156:157], v163 offset:28672\n"
"v_mfma_f32_16x16x32_bf16 a[56:59], v[80:83], v[48:51], a[56:59]\n"
"ds_read_b64_tr_b16 v[158:159], v163 offset:32768\n"
"v_mfma_f32_16x16x32_bf16 a[60:63], v[68:71], v[28:31], a[60:63]\n"
"buffer_load_dwordx4 v172, s[32:35], s43 offen offset:2048 lds\n"
"v_mfma_f32_16x16x32_bf16 a[60:63], v[84:87], v[48:51], a[60:63]\n"
"s_cmp_eq_u32 s48, 3\n"
"s_cselect_b32 s47, s68, s69\n"
"v_mfma_f32_16x16x32_bf16 a[64:67], v[56:59], v[32:35], a[64:67]\n"
"s_add_u32 s48, s48, 1\n"
"v_mfma_f32_16x16x32_bf16 a[64:67], v[72:75], v[52:55], a[64:67]\n"
"s_cmp_eq_u32 s48, 4\n"
"s_cselect_b32 s48, 0, s48\n"
"v_add_u32 v160, s47, v160\n"
"v_mfma_f32_16x16x32_bf16 a[68:71], v[60:63], v[32:35], a[68:71]\n"
"buffer_load_dwordx4 v173, s[32:35], s43 offen offset:3072 lds\n"
"v_add_u32 v161, s47, v161\n"
"v_mfma_f32_16x16x32_bf16 a[68:71], v[76:79], v[52:55], a[68:71]\n"
"s_add_u32 s42, s42, 2048\n"
"s_add_u32 s43, s43, 81920\n"
"v_add_u32 v162, s47, v162\n"
"v_mfma_f32_16x16x32_bf16 a[72:75], v[64:67], v[32:35], a[72:75]\n"
"s_add_u32 s45, s45, 36864\n"
"s_add_u32 s46, s46, 36864\n"
"v_add_u32 v163, s47, v163\n"
"v_mfma_f32_16x16x32_bf16 a[72:75], v[80:83], v[52:55], a[72:75]\n"
"s_add_u32 s55, s55, 1\n"
"v_add_u32 v164, s47, v164\n"
"v_mfma_f32_16x16x32_bf16 a[76:79], v[68:71], v[32:35], a[76:79]\n"
"s_cmp_eq_u32 s55, 4\n"
"s_cselect_b32 s60, 147456, 0\n"
"s_cselect_b32 s55, 0, s55\n"
"v_mfma_f32_16x16x32_bf16 a[76:79], v[84:87], v[52:55], a[76:79]\n"
"s_sub_u32 s45, s45, s60\n"
"s_sub_u32 s46, s46, s60\n"
"s_waitcnt lgkmcnt(0)\n"
"v_mfma_f32_16x16x32_bf16 a[0:3], v[128:131], v[88:91], a[0:3]\n"
"s_waitcnt vmcnt(18) lgkmcnt(0)\n"
"s_barrier\n"
"v_mfma_f32_16x16x32_bf16 a[0:3], v[144:147], v[108:111], a[0:3]\n"
"s_mov_b32 m0, s45\n"
"ds_read_b128 v[16:19], v164 offset:0\n"
"v_mfma_f32_16x16x32_bf16 a[4:7], v[132:135], v[88:91], a[4:7]\n"
"ds_read_b128 v[20:23], v164 offset:1024\n"
"buffer_load_dwordx4 v165, s[20:23], s42 offen offset:0 sc1 lds\n"
"v_mfma_f32_16x16x32_bf16 a[4:7], v[148:151], v[108:111], a[4:7]\n"
"ds_read_b128 v[24:27], v164 offset:2048\n"
"v_mfma_f32_16x16x32_bf16 a[8:11], v[136:139], v[88:91], a[8:11]\n"
"ds_read_b128 v[28:31], v164 offset:3072\n"
"v_mfma_f32_16x16x32_bf16 a[8:11], v[152:155], v[108:111], a[8:11]\n"
"ds_read_b128 v[32:35], v164 offset:4096\n"
"v_mfma_f32_16x16x32_bf16 a[12:15], v[140:143], v[88:91], a[12:15]\n"
"ds_read_b64_tr_b16 v[56:57], v160 offset:20480\n"
"buffer_load_dwordx4 v166, s[20:23], s42 offen offset:1024 sc1 lds\n"
"v_mfma_f32_16x16x32_bf16 a[12:15], v[156:159], v[108:111], a[12:15]\n"
"ds_read_b64_tr_b16 v[58:59], v160 offset:24576\n"
"v_mfma_f32_16x16x32_bf16 a[16:19], v[128:131], v[92:95], a[16:19]\n"
"v_mfma_f32_16x16x32_bf16 a[16:19], v[144:147], v[112:115], a[16:19]\n"
"ds_read_b64_tr_b16 v[60:61], v161 offset:20480\n"
"v_mfma_f32_16x16x32_bf16 a[20:23], v[132:135], v[92:95], a[20:23]\n"
"ds_read_b64_tr_b16 v[62:63], v161 offset:24576\n"
"buffer_load_dwordx4 v167, s[20:23], s42 offen offset:2048 sc1 lds\n"
"v_mfma_f32_16x16x32_bf16 a[20:23], v[148:151], v[112:115], a[20:23]\n"
"ds_read_b64_tr_b16 v[64:65], v162 offset:20480\n"
"v_mfma_f32_16x16x32_bf16 a[24:27], v[136:139], v[92:95], a[24:27]\n"
"ds_read_b64_tr_b16 v[66:67], v162 offset:24576\n"
"v_mfma_f32_16x16x32_bf16 a[24:27], v[152:155], v[112:115], a[24:27]\n"
"ds_read_b64_tr_b16 v[68:69], v163 offset:20480\n"
"v_mfma_f32_16x16x32_bf16 a[28:31], v[140:143], v[92:95], a[28:31]\n"
"ds_read_b64_tr_b16 v[70:71], v163 offset:24576\n"
"buffer_load_dwordx4 v168, s[20:23], s42 offen offset:3072 sc1 lds\n"
"v_mfma_f32_16x16x32_bf16 a[28:31], v[156:159], v[112:115], a[28:31]\n"
"v_mfma_f32_16x16x32_bf16 a[32:35], v[128:131], v[96:99], a[32:35]\n"
"ds_read_b128 v[36:39], v164 offset:10240\n"
"v_mfma_f32_16x16x32_bf16 a[32:35], v[144:147], v[116:119], a[32:35]\n"
"s_add_u32 m0, s45, 4096\n"
"ds_read_b128 v[40:43], v164 offset:11264\n"
"v_mfma_f32_16x16x32_bf16 a[36:39], v[132:135], v[96:99], a[36:39]\n"
"ds_read_b128 v[44:47], v164 offset:12288\n"
"buffer_load_dwordx4 v169, s[20:23], s42 offen offset:0 sc1 lds\n"
"v_mfma_f32_16x16x32_bf16 a[36:39], v[148:151], v[116:119], a[36:39]\n"
"ds_read_b128 v[48:51], v164 offset:13312\n"
"v_mfma_f32_16x16x32_bf16 a[40:43], v[136:139], v[96:99], a[40:43]\n"
"ds_read_b128 v[52:55], v164 offset:14336\n"
"v_mfma_f32_16x16x32_bf16 a[40:43], v[152:155], v[116:119], a[40:43]\n"
"s_mov_b32 m0, s46\n"
"ds_read_b64_tr_b16 v[72:73], v160 offset:28672\n"
"v_mfma_f32_16x16x32_bf16 a[44:47], v[140:143], v[96:99], a[44:47]\n"
"ds_read_b64_tr_b16 v[74:75], v160 offset:32768\n"
"buffer_load_dwordx4 v170, s[32:35], s43 offen offset:0 lds\n"
"v_mfma_f32_16x16x32_bf16 a[44:47], v[156:159], v[116:119], a[44:47]\n"
"v_mfma_f32_16x16x32_bf16 a[48:51], v[128:131], v[100:103], a[48:51]\n"
"ds_read_b64_tr_b16 v[76:77], v161 offset:28672\n"
"v_mfma_f32_16x16x32_bf16 a[48:51], v[144:147], v[120:123], a[48:51]\n"
"ds_read_b64_tr_b16 v[78:79], v161 offset:32768\n"
"v_mfma_f32_16x16x32_bf16 a[52:55], v[132:135], v[100:103], a[52:55]\n"
"ds_read_b64_tr_b16 v[80:81], v162 offset:28672\n"
"buffer_load_dwordx4 v171, s[32:35], s43 offen offset:1024 lds\n"
"v_mfma_f32_16x16x32_bf16 a[52:55], v[148:151], v[120:123], a[52:55]\n"
"ds_read_b64_tr_b16 v[82:83], v162 offset:32768\n"
"v_mfma_f32_16x16x32_bf16 a[56:59], v[136:139], v[100:103], a[56:59]\n"
"ds_read_b64_tr_b16 v[84:85], v163 offset:28672\n"
"v_mfma_f32_16x16x32_bf16 a[56:59], v[152:155], v[120:123], a[56:59]\n"
"ds_read_b64_tr_b16 v[86:87], v163 offset:32768\n"
"v_mfma_f32_16x16x32_bf16 a[60:63], v[140:143], v[100:103], a[60:63]\n"
"buffer_load_dwordx4 v172, s[32:35], s43 offen offset:2048 lds\n"
"v_mfma_f32_16x16x32_bf16 a[60:63], v[156:159], v[120:123], a[60:63]\n"
"s_cmp_eq_u32 s48, 3\n"
"s_cselect_b32 s47, s68, s69\n"
"v_mfma_f32_16x16x32_bf16 a[64:67], v[128:131], v[104:107], a[64:67]\n"
"s_add_u32 s48, s48, 1\n"
"v_mfma_f32_16x16x32_bf16 a[64:67], v[144:147], v[124:127], a[64:67]\n"
"s_cmp_eq_u32 s48, 4\n"
"s_cselect_b32 s48, 0, s48\n"
"v_add_u32 v160, s47, v160\n"
"v_mfma_f32_16x16x32_bf16 a[68:71], v[132:135], v[104:107], a[68:71]\n"
"buffer_load_dwordx4 v173, s[32:35], s43 offen offset:3072 lds\n"
"v_add_u32 v161, s47, v161\n"
"v_mfma_f32_16x16x32_bf16 a[68:71], v[148:151], v[124:127], a[68:71]\n"
"s_add_u32 s42, s42, 2048\n"
"s_add_u32 s43, s43, 81920\n"
"v_add_u32 v162, s47, v162\n"
"v_mfma_f32_16x16x32_bf16 a[72:75], v[136:139], v[104:107], a[72:75]\n"
"s_add_u32 s45, s45, 36864\n"
"s_add_u32 s46, s46, 36864\n"
"v_add_u32 v163, s47, v163\n"
"v_mfma_f32_16x16x32_bf16 a[72:75], v[152:155], v[124:127], a[72:75]\n"
"s_add_u32 s55, s55, 1\n"
"v_add_u32 v164, s47, v164\n"
"v_mfma_f32_16x16x32_bf16 a[76:79], v[140:143], v[104:107], a[76:79]\n"
"s_cmp_eq_u32 s55, 4\n"
"s_cselect_b32 s60, 147456, 0\n"
"s_cselect_b32 s55, 0, s55\n"
"v_mfma_f32_16x16x32_bf16 a[76:79], v[156:159], v[124:127], a[76:79]\n"
"s_sub_u32 s45, s45, s60\n"
"s_sub_u32 s46, s46, s60\n"
"s_waitcnt lgkmcnt(0)\n"
"v_mfma_f32_16x16x32_bf16 a[0:3], v[56:59], v[16:19], a[0:3]\n"
"s_waitcnt vmcnt(18) lgkmcnt(0)\n"
"s_barrier\n"
"v_mfma_f32_16x16x32_bf16 a[0:3], v[72:75], v[36:39], a[0:3]\n"
"s_mov_b32 m0, s45\n"
"ds_read_b128 v[88:91], v164 offset:0\n"
"v_mfma_f32_16x16x32_bf16 a[4:7], v[60:63], v[16:19], a[4:7]\n"
"ds_read_b128 v[92:95], v164 offset:1024\n"
"buffer_load_dwordx4 v165, s[20:23], s42 offen offset:0 sc1 lds\n"
"v_mfma_f32_16x16x32_bf16 a[4:7], v[76:79], v[36:39], a[4:7]\n"
"ds_read_b128 v[96:99], v164 offset:2048\n"
"v_mfma_f32_16x16x32_bf16 a[8:11], v[64:67], v[16:19], a[8:11]\n"
"ds_read_b128 v[100:103], v164 offset:3072\n"
"v_mfma_f32_16x16x32_bf16 a[8:11], v[80:83], v[36:39], a[8:11]\n"
"ds_read_b128 v[104:107], v164 offset:4096\n"
"v_mfma_f32_16x16x32_bf16 a[12:15], v[68:71], v[16:19], a[12:15]\n"
"ds_read_b64_tr_b16 v[128:129], v160 offset:20480\n"
"buffer_load_dwordx4 v166, s[20:23], s42 offen offset:1024 sc1 lds\n"
"v_mfma_f32_16x16x32_bf16 a[12:15], v[84:87], v[36:39], a[12:15]\n"
"ds_read_b64_tr_b16 v[130:131], v160 offset:24576\n"
"v_mfma_f32_16x16x32_bf16 a[16:19], v[56:59], v[20:23], a[16:19]\n"
"v_mfma_f32_16x16x32_bf16 a[16:19], v[72:75], v[40:43], a[16:19]\n"
"ds_read_b64_tr_b16 v[132:133], v161 offset:20480\n"
"v_mfma_f32_16x16x32_bf16 a[20:23], v[60:63], v[20:23], a[20:23]\n"
"ds_read_b64_tr_b16 v[134:135], v161 offset:24576\n"
"buffer_load_dwordx4 v167, s[20:23], s42 offen offset:2048 sc1 lds\n"
"v_mfma_f32_16x16x32_bf16 a[20:23], v[76:79], v[40:43], a[20:23]\n"
"ds_read_b64_tr_b16 v[136:137], v162 offset:20480\n"
"v_mfma_f32_16x16x32_bf16 a[24:27], v[64:67], v[20:23], a[24:27]\n"
"ds_read_b64_tr_b16 v[138:139], v162 offset:24576\n"
"v_mfma_f32_16x16x32_bf16 a[24:27], v[80:83], v[40:43], a[24:27]\n"
"ds_read_b64_tr_b16 v[140:141], v163 offset:20480\n"
"v_mfma_f32_16x16x32_bf16 a[28:31], v[68:71], v[20:23], a[28:31]\n"
"ds_read_b64_tr_b16 v[142:143], v163 offset:24576\n"
"buffer_load_dwordx4 v168, s[20:23], s42 offen offset:3072 sc1 lds\n"
"v_mfma_f32_16x16x32_bf16 a[28:31], v[84:87], v[40:43], a[28:31]\n"
"v_mfma_f32_16x16x32_bf16 a[32:35], v[56:59], v[24:27], a[32:35]\n"
"ds_read_b128 v[108:111], v164 offset:10240\n"
"v_mfma_f32_16x16x32_bf16 a[32:35], v[72:75], v[44:47], a[32:35]\n"
"s_add_u32 m0, s45, 4096\n"
"ds_read_b128 v[112:115], v164 offset:11264\n"
"v_mfma_f32_16x16x32_bf16 a[36:39], v[60:63], v[24:27], a[36:39]\n"
"ds_read_b128 v[116:119], v164 offset:12288\n"
"buffer_load_dwordx4 v169, s[20:23], s42 offen offset:0 sc1 lds\n"
"v_mfma_f32_16x16x32_bf16 a[36:39], v[76:79], v[44:47], a[36:39]\n"
"ds_read_b128 v[120:123], v164 offset:13312\n"
"v_mfma_f32_16x16x32_bf16 a[40:43], v[64:67], v[24:27], a[40:43]\n"
"ds_read_b128 v[124:127], v164 offset:14336\n"
"v_mfma_f32_16x16x32_bf16 a[40:43], v[80:83], v[44:47], a[40:43]\n"
"s_mov_b32 m0, s46\n"
"ds_read_b64_tr_b16 v[144:145], v160 offset:28672\n"
"v_mfma_f32_16x16x32_bf16 a[44:47], v[68:71], v[24:27], a[44:47]\n"
"ds_read_b64_tr_b16 v[146:147], v160 offset:32768\n"
"buffer_load_dwordx4 v170, s[32:35], s43 offen offset:0 lds\n"
"v_mfma_f32_16x16x32_bf16 a[44:47], v[84:87], v[44:47], a[44:47]\n"
"v_mfma_f32_16x16x32_bf16 a[48:51], v[56:59], v[28:31], a[48:51]\n"
"ds_read_b64_tr_b16 v[148:149], v161 offset:28672\n"
"v_mfma_f32_16x16x32_bf16 a[48:51], v[72:75], v[48:51], a[48:51]\n"
"ds_read_b64_tr_b16 v[150:151], v161 offset:32768\n"
"v_mfma_f32_16x16x32_bf16 a[52:55], v[60:63], v[28:31], a[52:55]\n"
"ds_read_b64_tr_b16 v[152:153], v162 offset:28672\n"
"buffer_load_dwordx4 v171, s[32:35], s43 offen offset:1024 lds\n"
"v_mfma_f32_16x16x32_bf16 a[52:55], v[76:79], v[48:51], a[52:55]\n"
"ds_read_b64_tr_b16 v[154:155], v162 offset:32768\n"
"v_mfma_f32_16x16x32_bf16 a[56:59], v[64:67], v[28:31], a[56:59]\n"
"ds_read_b64_tr_b16 v[156:157], v163 offset:28672\n"
"v_mfma_f32_16x16x32_bf16 a[56:59], v[80:83], v[48:51], a[56:59]\n"
"ds_read_b64_tr_b16 v[158:159], v163 offset:32768\n"
"v_mfma_f32_16x16x32_bf16 a[60:63], v[68:71], v[28:31], a[60:63]\n"
"buffer_load_dwordx4 v172, s[32:35], s43 offen offset:2048 lds\n"
"v_mfma_f32_16x16x32_bf16 a[60:63], v[84:87], v[48:51], a[60:63]\n"
"s_cmp_eq_u32 s48, 3\n"
"s_cselect_b32 s47, s68, s69\n"
"v_mfma_f32_16x16x32_bf16 a[64:67], v[56:59], v[32:35], a[64:67]\n"
"s_add_u32 s48, s48, 1\n"
"v_mfma_f32_16x16x32_bf16 a[64:67], v[72:75], v[52:55], a[64:67]\n"
"s_cmp_eq_u32 s48, 4\n"
"s_cselect_b32 s48, 0, s48\n"
"v_add_u32 v160, s47, v160\n"
"v_mfma_f32_16x16x32_bf16 a[68:71], v[60:63], v[32:35], a[68:71]\n"
"buffer_load_dwordx4 v173, s[32:35], s43 offen offset:3072 lds\n"
"v_add_u32 v161, s47, v161\n"
"v_mfma_f32_16x16x32_bf16 a[68:71], v[76:79], v[52:55], a[68:71]\n"
"s_add_u32 s42, s42, 2048\n"
"s_add_u32 s43, s43, 81920\n"
"v_add_u32 v162, s47, v162\n"
"v_mfma_f32_16x16x32_bf16 a[72:75], v[64:67], v[32:35], a[72:75]\n"
"s_add_u32 s45, s45, 36864\n"
"s_add_u32 s46, s46, 36864\n"
"v_add_u32 v163, s47, v163\n"
"v_mfma_f32_16x16x32_bf16 a[72:75], v[80:83], v[52:55], a[72:75]\n"
"s_add_u32 s55, s55, 1\n"
"v_add_u32 v164, s47, v164\n"
"v_mfma_f32_16x16x32_bf16 a[76:79], v[68:71], v[32:35], a[76:79]\n"
"s_cmp_eq_u32 s55, 4\n"
"s_cselect_b32 s60, 147456, 0\n"
"s_cselect_b32 s55, 0, s55\n"
"v_mfma_f32_16x16x32_bf16 a[76:79], v[84:87], v[52:55], a[76:79]\n"
"s_sub_u32 s45, s45, s60\n"
"s_sub_u32 s46, s46, s60\n"
"s_waitcnt lgkmcnt(0)\n"
"v_mfma_f32_16x16x32_bf16 a[0:3], v[128:131], v[88:91], a[0:3]\n"
"s_waitcnt vmcnt(18) lgkmcnt(0)\n"
"s_barrier\n"
"v_mfma_f32_16x16x32_bf16 a[0:3], v[144:147], v[108:111], a[0:3]\n"
"s_mov_b32 m0, s45\n"
"ds_read_b128 v[16:19], v164 offset:0\n"
"v_mfma_f32_16x16x32_bf16 a[4:7], v[132:135], v[88:91], a[4:7]\n"
"ds_read_b128 v[20:23], v164 offset:1024\n"
"buffer_load_dwordx4 v165, s[20:23], s42 offen offset:0 sc1 lds\n"
"v_mfma_f32_16x16x32_bf16 a[4:7], v[148:151], v[108:111], a[4:7]\n"
"ds_read_b128 v[24:27], v164 offset:2048\n"
"v_mfma_f32_16x16x32_bf16 a[8:11], v[136:139], v[88:91], a[8:11]\n"
"ds_read_b128 v[28:31], v164 offset:3072\n"
"v_mfma_f32_16x16x32_bf16 a[8:11], v[152:155], v[108:111], a[8:11]\n"
"ds_read_b128 v[32:35], v164 offset:4096\n"
"v_mfma_f32_16x16x32_bf16 a[12:15], v[140:143], v[88:91], a[12:15]\n"
"ds_read_b64_tr_b16 v[56:57], v160 offset:20480\n"
"buffer_load_dwordx4 v166, s[20:23], s42 offen offset:1024 sc1 lds\n"
"v_mfma_f32_16x16x32_bf16 a[12:15], v[156:159], v[108:111], a[12:15]\n"
"ds_read_b64_tr_b16 v[58:59], v160 offset:24576\n"
"v_mfma_f32_16x16x32_bf16 a[16:19], v[128:131], v[92:95], a[16:19]\n"
"v_mfma_f32_16x16x32_bf16 a[16:19], v[144:147], v[112:115], a[16:19]\n"
"ds_read_b64_tr_b16 v[60:61], v161 offset:20480\n"
"v_mfma_f32_16x16x32_bf16 a[20:23], v[132:135], v[92:95], a[20:23]\n"
"ds_read_b64_tr_b16 v[62:63], v161 offset:24576\n"
"buffer_load_dwordx4 v167, s[20:23], s42 offen offset:2048 sc1 lds\n"
"v_mfma_f32_16x16x32_bf16 a[20:23], v[148:151], v[112:115], a[20:23]\n"
"ds_read_b64_tr_b16 v[64:65], v162 offset:20480\n"
"v_mfma_f32_16x16x32_bf16 a[24:27], v[136:139], v[92:95], a[24:27]\n"
"ds_read_b64_tr_b16 v[66:67], v162 offset:24576\n"
"v_mfma_f32_16x16x32_bf16 a[24:27], v[152:155], v[112:115], a[24:27]\n"
"ds_read_b64_tr_b16 v[68:69], v163 offset:20480\n"
"v_mfma_f32_16x16x32_bf16 a[28:31], v[140:143], v[92:95], a[28:31]\n"
"ds_read_b64_tr_b16 v[70:71], v163 offset:24576\n"
"buffer_load_dwordx4 v168, s[20:23], s42 offen offset:3072 sc1 lds\n"
"v_mfma_f32_16x16x32_bf16 a[28:31], v[156:159], v[112:115], a[28:31]\n"
"v_mfma_f32_16x16x32_bf16 a[32:35], v[128:131], v[96:99], a[32:35]\n"
"ds_read_b128 v[36:39], v164 offset:10240\n"
"v_mfma_f32_16x16x32_bf16 a[32:35], v[144:147], v[116:119], a[32:35]\n"
"s_add_u32 m0, s45, 4096\n"
"ds_read_b128 v[40:43], v164 offset:11264\n"
"v_mfma_f32_16x16x32_bf16 a[36:39], v[132:135], v[96:99], a[36:39]\n"
"ds_read_b128 v[44:47], v164 offset:12288\n"
"buffer_load_dwordx4 v169, s[20:23], s42 offen offset:0 sc1 lds\n"
"v_mfma_f32_16x16x32_bf16 a[36:39], v[148:151], v[116:119], a[36:39]\n"
"ds_read_b128 v[48:51], v164 offset:13312\n"
"v_mfma_f32_16x16x32_bf16 a[40:43], v[136:139], v[96:99], a[40:43]\n"
"ds_read_b128 v[52:55], v164 offset:14336\n"
"v_mfma_f32_16x16x32_bf16 a[40:43], v[152:155], v[116:119], a[40:43]\n"
"s_mov_b32 m0, s46\n"
"ds_read_b64_tr_b16 v[72:73], v160 offset:28672\n"
"v_mfma_f32_16x16x32_bf16 a[44:47], v[140:143], v[96:99], a[44:47]\n"
"ds_read_b64_tr_b16 v[74:75], v160 offset:32768\n"
"buffer_load_dwordx4 v170, s[32:35], s43 offen offset:0 lds\n"
"v_mfma_f32_16x16x32_bf16 a[44:47], v[156:159], v[116:119], a[44:47]\n"
"v_mfma_f32_16x16x32_bf16 a[48:51], v[128:131], v[100:103], a[48:51]\n"
"ds_read_b64_tr_b16 v[76:77], v161 offset:28672\n"
"v_mfma_f32_16x16x32_bf16 a[48:51], v[144:147], v[120:123], a[48:51]\n"
"ds_read_b64_tr_b16 v[78:79], v161 offset:32768\n"
"v_mfma_f32_16x16x32_bf16 a[52:55], v[132:135], v[100:103], a[52:55]\n"
"ds_read_b64_tr_b16 v[80:81], v162 offset:28672\n"
"buffer_load_dwordx4 v171, s[32:35], s43 offen offset:1024 lds\n"
"v_mfma_f32_16x16x32_bf16 a[52:55], v[148:151], v[120:123], a[52:55]\n"
"ds_read_b64_tr_b16 v[82:83], v162 offset:32768\n"
"v_mfma_f32_16x16x32_bf16 a[56:59], v[136:139], v[100:103], a[56:59]\n"
"ds_read_b64_tr_b16 v[84:85], v163 offset:28672\n"
"v_mfma_f32_16x16x32_bf16 a[56:59], v[152:155], v[120:123], a[56:59]\n"
"ds_read_b64_tr_b16 v[86:87], v163 offset:32768\n"
"v_mfma_f32_16x16x32_bf16 a[60:63], v[140:143], v[100:103], a[60:63]\n"
"buffer_load_dwordx4 v172, s[32:35], s43 offen offset:2048 lds\n"
"v_mfma_f32_16x16x32_bf16 a[60:63], v[156:159], v[120:123], a[60:63]\n"
"s_cmp_eq_u32 s48, 3\n"
"s_cselect_b32 s47, s68, s69\n"
"v_mfma_f32_16x16x32_bf16 a[64:67], v[128:131], v[104:107], a[64:67]\n"
"s_add_u32 s48, s48, 1\n"
"v_mfma_f32_16x16x32_bf16 a[64:67], v[144:147], v[124:127], a[64:67]\n"
"s_cmp_eq_u32 s48, 4\n"
"s_cselect_b32 s48, 0, s48\n"
"v_add_u32 v160, s47, v160\n"
"v_mfma_f32_16x16x32_bf16 a[68:71], v[132:135], v[104:107], a[68:71]\n"
"buffer_load_dwordx4 v173, s[32:35], s43 offen offset:3072 lds\n"
"v_add_u32 v161, s47, v161\n"
"v_mfma_f32_16x16x32_bf16 a[68:71], v[148:151], v[124:127], a[68:71]\n"
"s_add_u32 s42, s42, 2048\n"
"s_add_u32 s43, s43, 81920\n"
"v_add_u32 v162, s47, v162\n"
"v_mfma_f32_16x16x32_bf16 a[72:75], v[136:139], v[104:107], a[72:75]\n"
"s_add_u32 s45, s45, 36864\n"
"s_add_u32 s46, s46, 36864\n"
"v_add_u32 v163, s47, v163\n"
"v_mfma_f32_16x16x32_bf16 a[72:75], v[152:155], v[124:127], a[72:75]\n"
"s_add_u32 s55, s55, 1\n"
"v_add_u32 v164, s47, v164\n"
"v_mfma_f32_16x16x32_bf16 a[76:79], v[140:143], v[104:107], a[76:79]\n"
"s_cmp_eq_u32 s55, 4\n"
"s_cselect_b32 s60, 147456, 0\n"
"s_cselect_b32 s55, 0, s55\n"
"v_mfma_f32_16x16x32_bf16 a[76:79], v[156:159], v[124:127], a[76:79]\n"
"s_sub_u32 s45, s45, s60\n"
"s_sub_u32 s46, s46, s60\n"
"s_waitcnt lgkmcnt(0)\n"
"v_mfma_f32_16x16x32_bf16 a[0:3], v[56:59], v[16:19], a[0:3]\n"
"s_waitcnt vmcnt(18) lgkmcnt(0)\n"
"s_barrier\n"
"v_mfma_f32_16x16x32_bf16 a[0:3], v[72:75], v[36:39], a[0:3]\n"
"s_mov_b32 m0, s45\n"
"ds_read_b128 v[88:91], v164 offset:0\n"
"v_mfma_f32_16x16x32_bf16 a[4:7], v[60:63], v[16:19], a[4:7]\n"
"ds_read_b128 v[92:95], v164 offset:1024\n"
"buffer_load_dwordx4 v165, s[20:23], s42 offen offset:0 sc1 lds\n"
"v_mfma_f32_16x16x32_bf16 a[4:7], v[76:79], v[36:39], a[4:7]\n"
"ds_read_b128 v[96:99], v164 offset:2048\n"
"v_mfma_f32_16x16x32_bf16 a[8:11], v[64:67], v[16:19], a[8:11]\n"
"ds_read_b128 v[100:103], v164 offset:3072\n"
"v_mfma_f32_16x16x32_bf16 a[8:11], v[80:83], v[36:39], a[8:11]\n"
"ds_read_b128 v[104:107], v164 offset:4096\n"
"v_mfma_f32_16x16x32_bf16 a[12:15], v[68:71], v[16:19], a[12:15]\n"
"ds_read_b64_tr_b16 v[128:129], v160 offset:20480\n"
"buffer_load_dwordx4 v166, s[20:23], s42 offen offset:1024 sc1 lds\n"
"v_mfma_f32_16x16x32_bf16 a[12:15], v[84:87], v[36:39], a[12:15]\n"
"ds_read_b64_tr_b16 v[130:131], v160 offset:24576\n"
"v_mfma_f32_16x16x32_bf16 a[16:19], v[56:59], v[20:23], a[16:19]\n"
"v_mfma_f32_16x16x32_bf16 a[16:19], v[72:75], v[40:43], a[16:19]\n"
"ds_read_b64_tr_b16 v[132:133], v161 offset:20480\n"
"v_mfma_f32_16x16x32_bf16 a[20:23], v[60:63], v[20:23], a[20:23]\n"
"ds_read_b64_tr_b16 v[134:135], v161 offset:24576\n"
"buffer_load_dwordx4 v167, s[20:23], s42 offen offset:2048 sc1 lds\n"
"v_mfma_f32_16x16x32_bf16 a[20:23], v[76:79], v[40:43], a[20:23]\n"
"ds_read_b64_tr_b16 v[136:137], v162 offset:20480\n"
"v_mfma_f32_16x16x32_bf16 a[24:27], v[64:67], v[20:23], a[24:27]\n"
"ds_read_b64_tr_b16 v[138:139], v162 offset:24576\n"
"v_mfma_f32_16x16x32_bf16 a[24:27], v[80:83], v[40:43], a[24:27]\n"
"ds_read_b64_tr_b16 v[140:141], v163 offset:20480\n"
"v_mfma_f32_16x16x32_bf16 a[28:31], v[68:71], v[20:23], a[28:31]\n"
"ds_read_b64_tr_b16 v[142:143], v163 offset:24576\n"
"buffer_load_dwordx4 v168, s[20:23], s42 offen offset:3072 sc1 lds\n"
"v_mfma_f32_16x16x32_bf16 a[28:31], v[84:87], v[40:43], a[28:31]\n"
"v_mfma_f32_16x16x32_bf16 a[32:35], v[56:59], v[24:27], a[32:35]\n"
"ds_read_b128 v[108:111], v164 offset:10240\n"
"v_mfma_f32_16x16x32_bf16 a[32:35], v[72:75], v[44:47], a[32:35]\n"
"s_add_u32 m0, s45, 4096\n"
"ds_read_b128 v[112:115], v164 offset:11264\n"
"v_mfma_f32_16x16x32_bf16 a[36:39], v[60:63], v[24:27], a[36:39]\n"
"ds_read_b128 v[116:119], v164 offset:12288\n"
"buffer_load_dwordx4 v169, s[20:23], s42 offen offset:0 sc1 lds\n"
"v_mfma_f32_16x16x32_bf16 a[36:39], v[76:79], v[44:47], a[36:39]\n"
"ds_read_b128 v[120:123], v164 offset:13312\n"
"v_mfma_f32_16x16x32_bf16 a[40:43], v[64:67], v[24:27], a[40:43]\n"
"ds_read_b128 v[124:127], v164 offset:14336\n"
"v_mfma_f32_16x16x32_bf16 a[40:43], v[80:83], v[44:47], a[40:43]\n"
"s_mov_b32 m0, s46\n"
"ds_read_b64_tr_b16 v[144:145], v160 offset:28672\n"
"v_mfma_f32_16x16x32_bf16 a[44:47], v[68:71], v[24:27], a[44:47]\n"
"ds_read_b64_tr_b16 v[146:147], v160 offset:32768\n"
"buffer_load_dwordx4 v170, s[32:35], s43 offen offset:0 lds\n"
"v_mfma_f32_16x16x32_bf16 a[44:47], v[84:87], v[44:47], a[44:47]\n"
"v_mfma_f32_16x16x32_bf16 a[48:51], v[56:59], v[28:31], a[48:51]\n"
"ds_read_b64_tr_b16 v[148:149], v161 offset:28672\n"
"v_mfma_f32_16x16x32_bf16 a[48:51], v[72:75], v[48:51], a[48:51]\n"
"ds_read_b64_tr_b16 v[150:151], v161 offset:32768\n"
"v_mfma_f32_16x16x32_bf16 a[52:55], v[60:63], v[28:31], a[52:55]\n"
"ds_read_b64_tr_b16 v[152:153], v162 offset:28672\n"
"buffer_load_dwordx4 v171, s[32:35], s43 offen offset:1024 lds\n"
"v_mfma_f32_16x16x32_bf16 a[52:55], v[76:79], v[48:51], a[52:55]\n"
"ds_read_b64_tr_b16 v[154:155], v162 offset:32768\n"
"v_mfma_f32_16x16x32_bf16 a[56:59], v[64:67], v[28:31], a[56:59]\n"
"ds_read_b64_tr_b16 v[156:157], v163 offset:28672\n"
"v_mfma_f32_16x16x32_bf16 a[56:59], v[80:83], v[48:51], a[56:59]\n"
"ds_read_b64_tr_b16 v[158:159], v163 offset:32768\n"
"v_mfma_f32_16x16x32_bf16 a[60:63], v[68:71], v[28:31], a[60:63]\n"
"buffer_load_dwordx4 v172, s[32:35], s43 offen offset:2048 lds\n"
"v_mfma_f32_16x16x32_bf16 a[60:63], v[84:87], v[48:51], a[60:63]\n"
"s_cmp_eq_u32 s48, 3\n"
"s_cselect_b32 s47, s68, s69\n"
"v_mfma_f32_16x16x32_bf16 a[64:67], v[56:59], v[32:35], a[64:67]\n"
"s_add_u32 s48, s48, 1\n"
"v_mfma_f32_16x16x32_bf16 a[64:67], v[72:75], v[52:55], a[64:67]\n"
"s_cmp_eq_u32 s48, 4\n"
"s_cselect_b32 s48, 0, s48\n"
"v_add_u32 v160, s47, v160\n"
"v_mfma_f32_16x16x32_bf16 a[68:71], v[60:63], v[32:35], a[68:71]\n"
"buffer_load_dwordx4 v173, s[32:35], s43 offen offset:3072 lds\n"
"v_add_u32 v161, s47, v161\n"
"v_mfma_f32_16x16x32_bf16 a[68:71], v[76:79], v[52:55], a[68:71]\n"
"s_add_u32 s42, s42, 2048\n"
"s_add_u32 s43, s43, 81920\n"
"v_add_u32 v162, s47, v162\n"
"v_mfma_f32_16x16x32_bf16 a[72:75], v[64:67], v[32:35], a[72:75]\n"
"s_add_u32 s45, s45, 36864\n"
"s_add_u32 s46, s46, 36864\n"
"v_add_u32 v163, s47, v163\n"
"v_mfma_f32_16x16x32_bf16 a[72:75], v[80:83], v[52:55], a[72:75]\n"
"s_add_u32 s55, s55, 1\n"
"v_add_u32 v164, s47, v164\n"
"v_mfma_f32_16x16x32_bf16 a[76:79], v[68:71], v[32:35], a[76:79]\n"
"s_cmp_eq_u32 s55, 4\n"
"s_cselect_b32 s60, 147456, 0\n"
"s_cselect_b32 s55, 0, s55\n"
"v_mfma_f32_16x16x32_bf16 a[76:79], v[84:87], v[52:55], a[76:79]\n"
"s_sub_u32 s45, s45, s60\n"
"s_sub_u32 s46, s46, s60\n"
"s_waitcnt lgkmcnt(0)\n"
"v_mfma_f32_16x16x32_bf16 a[0:3], v[128:131], v[88:91], a[0:3]\n"
"s_waitcnt vmcnt(18) lgkmcnt(0)\n"
"s_barrier\n"
"v_mfma_f32_16x16x32_bf16 a[0:3], v[144:147], v[108:111], a[0:3]\n"
"s_mov_b32 m0, s45\n"
"ds_read_b128 v[16:19], v164 offset:0\n"
"v_mfma_f32_16x16x32_bf16 a[4:7], v[132:135], v[88:91], a[4:7]\n"
"ds_read_b128 v[20:23], v164 offset:1024\n"
"buffer_load_dwordx4 v165, s[20:23], s42 offen offset:0 sc1 lds\n"
"v_mfma_f32_16x16x32_bf16 a[4:7], v[148:151], v[108:111], a[4:7]\n"
"ds_read_b128 v[24:27], v164 offset:2048\n"
"v_mfma_f32_16x16x32_bf16 a[8:11], v[136:139], v[88:91], a[8:11]\n"
"ds_read_b128 v[28:31], v164 offset:3072\n"
"v_mfma_f32_16x16x32_bf16 a[8:11], v[152:155], v[108:111], a[8:11]\n"
"ds_read_b128 v[32:35], v164 offset:4096\n"
"v_mfma_f32_16x16x32_bf16 a[12:15], v[140:143], v[88:91], a[12:15]\n"
"ds_read_b64_tr_b16 v[56:57], v160 offset:20480\n"
"buffer_load_dwordx4 v166, s[20:23], s42 offen offset:1024 sc1 lds\n"
"v_mfma_f32_16x16x32_bf16 a[12:15], v[156:159], v[108:111], a[12:15]\n"
"ds_read_b64_tr_b16 v[58:59], v160 offset:24576\n"
"v_mfma_f32_16x16x32_bf16 a[16:19], v[128:131], v[92:95], a[16:19]\n"
"v_mfma_f32_16x16x32_bf16 a[16:19], v[144:147], v[112:115], a[16:19]\n"
"ds_read_b64_tr_b16 v[60:61], v161 offset:20480\n"
"v_mfma_f32_16x16x32_bf16 a[20:23], v[132:135], v[92:95], a[20:23]\n"
"ds_read_b64_tr_b16 v[62:63], v161 offset:24576\n"
"buffer_load_dwordx4 v167, s[20:23], s42 offen offset:2048 sc1 lds\n"
"v_mfma_f32_16x16x32_bf16 a[20:23], v[148:151], v[112:115], a[20:23]\n"
"ds_read_b64_tr_b16 v[64:65], v162 offset:20480\n"
"v_mfma_f32_16x16x32_bf16 a[24:27], v[136:139], v[92:95], a[24:27]\n"
"ds_read_b64_tr_b16 v[66:67], v162 offset:24576\n"
"v_mfma_f32_16x16x32_bf16 a[24:27], v[152:155], v[112:115], a[24:27]\n"
"ds_read_b64_tr_b16 v[68:69], v163 offset:20480\n"
"v_mfma_f32_16x16x32_bf16 a[28:31], v[140:143], v[92:95], a[28:31]\n"
"ds_read_b64_tr_b16 v[70:71], v163 offset:24576\n"
"buffer_load_dwordx4 v168, s[20:23], s42 offen offset:3072 sc1 lds\n"
"v_mfma_f32_16x16x32_bf16 a[28:31], v[156:159], v[112:115], a[28:31]\n"
"v_mfma_f32_16x16x32_bf16 a[32:35], v[128:131], v[96:99], a[32:35]\n"
"ds_read_b128 v[36:39], v164 offset:10240\n"
"v_mfma_f32_16x16x32_bf16 a[32:35], v[144:147], v[116:119], a[32:35]\n"
"s_add_u32 m0, s45, 4096\n"
"ds_read_b128 v[40:43], v164 offset:11264\n"
"v_mfma_f32_16x16x32_bf16 a[36:39], v[132:135], v[96:99], a[36:39]\n"
"ds_read_b128 v[44:47], v164 offset:12288\n"
"buffer_load_dwordx4 v169, s[20:23], s42 offen offset:0 sc1 lds\n"
"v_mfma_f32_16x16x32_bf16 a[36:39], v[148:151], v[116:119], a[36:39]\n"
"ds_read_b128 v[48:51], v164 offset:13312\n"
"v_mfma_f32_16x16x32_bf16 a[40:43], v[136:139], v[96:99], a[40:43]\n"
"ds_read_b128 v[52:55], v164 offset:14336\n"
"v_mfma_f32_16x16x32_bf16 a[40:43], v[152:155], v[116:119], a[40:43]\n"
"s_mov_b32 m0, s46\n"
"ds_read_b64_tr_b16 v[72:73], v160 offset:28672\n"
"v_mfma_f32_16x16x32_bf16 a[44:47], v[140:143], v[96:99], a[44:47]\n"
"ds_read_b64_tr_b16 v[74:75], v160 offset:32768\n"
"buffer_load_dwordx4 v170, s[32:35], s43 offen offset:0 lds\n"
"v_mfma_f32_16x16x32_bf16 a[44:47], v[156:159], v[116:119], a[44:47]\n"
"v_mfma_f32_16x16x32_bf16 a[48:51], v[128:131], v[100:103], a[48:51]\n"
"ds_read_b64_tr_b16 v[76:77], v161 offset:28672\n"
"v_mfma_f32_16x16x32_bf16 a[48:51], v[144:147], v[120:123], a[48:51]\n"
"ds_read_b64_tr_b16 v[78:79], v161 offset:32768\n"
"v_mfma_f32_16x16x32_bf16 a[52:55], v[132:135], v[100:103], a[52:55]\n"
"ds_read_b64_tr_b16 v[80:81], v162 offset:28672\n"
"buffer_load_dwordx4 v171, s[32:35], s43 offen offset:1024 lds\n"
"v_mfma_f32_16x16x32_bf16 a[52:55], v[148:151], v[120:123], a[52:55]\n"
"ds_read_b64_tr_b16 v[82:83], v162 offset:32768\n"
"v_mfma_f32_16x16x32_bf16 a[56:59], v[136:139], v[100:103], a[56:59]\n"
"ds_read_b64_tr_b16 v[84:85], v163 offset:28672\n"
"v_mfma_f32_16x16x32_bf16 a[56:59], v[152:155], v[120:123], a[56:59]\n"
"ds_read_b64_tr_b16 v[86:87], v163 offset:32768\n"
"v_mfma_f32_16x16x32_bf16 a[60:63], v[140:143], v[100:103], a[60:63]\n"
"buffer_load_dwordx4 v172, s[32:35], s43 offen offset:2048 lds\n"
"v_mfma_f32_16x16x32_bf16 a[60:63], v[156:159], v[120:123], a[60:63]\n"
"s_cmp_eq_u32 s48, 3\n"
"s_cselect_b32 s47, s68, s69\n"
"v_mfma_f32_16x16x32_bf16 a[64:67], v[128:131], v[104:107], a[64:67]\n"
"s_add_u32 s48, s48, 1\n"
"v_mfma_f32_16x16x32_bf16 a[64:67], v[144:147], v[124:127], a[64:67]\n"
"s_cmp_eq_u32 s48, 4\n"
"s_cselect_b32 s48, 0, s48\n"
"v_add_u32 v160, s47, v160\n"
"v_mfma_f32_16x16x32_bf16 a[68:71], v[132:135], v[104:107], a[68:71]\n"
"buffer_load_dwordx4 v173, s[32:35], s43 offen offset:3072 lds\n"
"v_add_u32 v161, s47, v161\n"
"v_mfma_f32_16x16x32_bf16 a[68:71], v[148:151], v[124:127], a[68:71]\n"
"s_add_u32 s42, s42, 2048\n"
"s_add_u32 s43, s43, 81920\n"
"v_add_u32 v162, s47, v162\n"
"v_mfma_f32_16x16x32_bf16 a[72:75], v[136:139], v[104:107], a[72:75]\n"
"s_add_u32 s45, s45, 36864\n"
"s_add_u32 s46, s46, 36864\n"
"v_add_u32 v163, s47, v163\n"
"v_mfma_f32_16x16x32_bf16 a[72:75], v[152:155], v[124:127], a[72:75]\n"
"s_add_u32 s55, s55, 1\n"
"v_add_u32 v164, s47, v164\n"
"v_mfma_f32_16x16x32_bf16 a[76:79], v[140:143], v[104:107], a[76:79]\n"
"s_cmp_eq_u32 s55, 4\n"
"s_cselect_b32 s60, 147456, 0\n"
"s_cselect_b32 s55, 0, s55\n"
"v_mfma_f32_16x16x32_bf16 a[76:79], v[156:159], v[124:127], a[76:79]\n"
"s_sub_u32 s45, s45, s60\n"
"s_sub_u32 s46, s46, s60\n"
"s_waitcnt lgkmcnt(0)\n"
"v_mfma_f32_16x16x32_bf16 a[0:3], v[56:59], v[16:19], a[0:3]\n"
"s_waitcnt vmcnt(18) lgkmcnt(0)\n"
"s_barrier\n"
"v_mfma_f32_16x16x32_bf16 a[0:3], v[72:75], v[36:39], a[0:3]\n"
"s_mov_b32 m0, s45\n"
"ds_read_b128 v[88:91], v164 offset:0\n"
"v_mfma_f32_16x16x32_bf16 a[4:7], v[60:63], v[16:19], a[4:7]\n"
"ds_read_b128 v[92:95], v164 offset:1024\n"
"buffer_load_dwordx4 v165, s[20:23], s42 offen offset:0 sc1 lds\n"
"v_mfma_f32_16x16x32_bf16 a[4:7], v[76:79], v[36:39], a[4:7]\n"
"ds_read_b128 v[96:99], v164 offset:2048\n"
"v_mfma_f32_16x16x32_bf16 a[8:11], v[64:67], v[16:19], a[8:11]\n"
"ds_read_b128 v[100:103], v164 offset:3072\n"
"v_mfma_f32_16x16x32_bf16 a[8:11], v[80:83], v[36:39], a[8:11]\n"
"ds_read_b128 v[104:107], v164 offset:4096\n"
"v_mfma_f32_16x16x32_bf16 a[12:15], v[68:71], v[16:19], a[12:15]\n"
"ds_read_b64_tr_b16 v[128:129], v160 offset:20480\n"
"buffer_load_dwordx4 v166, s[20:23], s42 offen offset:1024 sc1 lds\n"
"v_mfma_f32_16x16x32_bf16 a[12:15], v[84:87], v[36:39], a[12:15]\n"
"ds_read_b64_tr_b16 v[130:131], v160 offset:24576\n"
"v_mfma_f32_16x16x32_bf16 a[16:19], v[56:59], v[20:23], a[16:19]\n"
"v_mfma_f32_16x16x32_bf16 a[16:19], v[72:75], v[40:43], a[16:19]\n"
"ds_read_b64_tr_b16 v[132:133], v161 offset:20480\n"
"v_mfma_f32_16x16x32_bf16 a[20:23], v[60:63], v[20:23], a[20:23]\n"
"ds_read_b64_tr_b16 v[134:135], v161 offset:24576\n"
"buffer_load_dwordx4 v167, s[20:23], s42 offen offset:2048 sc1 lds\n"
"v_mfma_f32_16x16x32_bf16 a[20:23], v[76:79], v[40:43], a[20:23]\n"
"ds_read_b64_tr_b16 v[136:137], v162 offset:20480\n"
"v_mfma_f32_16x16x32_bf16 a[24:27], v[64:67], v[20:23], a[24:27]\n"
"ds_read_b64_tr_b16 v[138:139], v162 offset:24576\n"
"v_mfma_f32_16x16x32_bf16 a[24:27], v[80:83], v[40:43], a[24:27]\n"
"ds_read_b64_tr_b16 v[140:141], v163 offset:20480\n"
"v_mfma_f32_16x16x32_bf16 a[28:31], v[68:71], v[20:23], a[28:31]\n"
"ds_read_b64_tr_b16 v[142:143], v163 offset:24576\n"
"buffer_load_dwordx4 v168, s[20:23], s42 offen offset:3072 sc1 lds\n"
"v_mfma_f32_16x16x32_bf16 a[28:31], v[84:87], v[40:43], a[28:31]\n"
"v_mfma_f32_16x16x32_bf16 a[32:35], v[56:59], v[24:27], a[32:35]\n"
"ds_read_b128 v[108:111], v164 offset:10240\n"
"v_mfma_f32_16x16x32_bf16 a[32:35], v[72:75], v[44:47], a[32:35]\n"
"s_add_u32 m0, s45, 4096\n"
"ds_read_b128 v[112:115], v164 offset:11264\n"
"v_mfma_f32_16x16x32_bf16 a[36:39], v[60:63], v[24:27], a[36:39]\n"
"ds_read_b128 v[116:119], v164 offset:12288\n"
"buffer_load_dwordx4 v169, s[20:23], s42 offen offset:0 sc1 lds\n"
"v_mfma_f32_16x16x32_bf16 a[36:39], v[76:79], v[44:47], a[36:39]\n"
"ds_read_b128 v[120:123], v164 offset:13312\n"
"v_mfma_f32_16x16x32_bf16 a[40:43], v[64:67], v[24:27], a[40:43]\n"
"ds_read_b128 v[124:127], v164 offset:14336\n"
"v_mfma_f32_16x16x32_bf16 a[40:43], v[80:83], v[44:47], a[40:43]\n"
"s_mov_b32 m0, s46\n"
"ds_read_b64_tr_b16 v[144:145], v160 offset:28672\n"
"v_mfma_f32_16x16x32_bf16 a[44:47], v[68:71], v[24:27], a[44:47]\n"
"ds_read_b64_tr_b16 v[146:147], v160 offset:32768\n"
"buffer_load_dwordx4 v170, s[32:35], s43 offen offset:0 lds\n"
"v_mfma_f32_16x16x32_bf16 a[44:47], v[84:87], v[44:47], a[44:47]\n"
"v_mfma_f32_16x16x32_bf16 a[48:51], v[56:59], v[28:31], a[48:51]\n"
"ds_read_b64_tr_b16 v[148:149], v161 offset:28672\n"
"v_mfma_f32_16x16x32_bf16 a[48:51], v[72:75], v[48:51], a[48:51]\n"
"ds_read_b64_tr_b16 v[150:151], v161 offset:32768\n"
"v_mfma_f32_16x16x32_bf16 a[52:55], v[60:63], v[28:31], a[52:55]\n"
"ds_read_b64_tr_b16 v[152:153], v162 offset:28672\n"
"buffer_load_dwordx4 v171, s[32:35], s43 offen offset:1024 lds\n"
"v_mfma_f32_16x16x32_bf16 a[52:55], v[76:79], v[48:51], a[52:55]\n"
"ds_read_b64_tr_b16 v[154:155], v162 offset:32768\n"
"v_mfma_f32_16x16x32_bf16 a[56:59], v[64:67], v[28:31], a[56:59]\n"
"ds_read_b64_tr_b16 v[156:157], v163 offset:28672\n"
"v_mfma_f32_16x16x32_bf16 a[56:59], v[80:83], v[48:51], a[56:59]\n"
"ds_read_b64_tr_b16 v[158:159], v163 offset:32768\n"
"v_mfma_f32_16x16x32_bf16 a[60:63], v[68:71], v[28:31], a[60:63]\n"
"buffer_load_dwordx4 v172, s[32:35], s43 offen offset:2048 lds\n"
"v_mfma_f32_16x16x32_bf16 a[60:63], v[84:87], v[48:51], a[60:63]\n"
"s_cmp_eq_u32 s48, 3\n"
"s_cselect_b32 s47, s68, s69\n"
"v_mfma_f32_16x16x32_bf16 a[64:67], v[56:59], v[32:35], a[64:67]\n"
"s_add_u32 s48, s48, 1\n"
"v_mfma_f32_16x16x32_bf16 a[64:67], v[72:75], v[52:55], a[64:67]\n"
"s_cmp_eq_u32 s48, 4\n"
"s_cselect_b32 s48, 0, s48\n"
"v_add_u32 v160, s47, v160\n"
"v_mfma_f32_16x16x32_bf16 a[68:71], v[60:63], v[32:35], a[68:71]\n"
"buffer_load_dwordx4 v173, s[32:35], s43 offen offset:3072 lds\n"
"v_add_u32 v161, s47, v161\n"
"v_mfma_f32_16x16x32_bf16 a[68:71], v[76:79], v[52:55], a[68:71]\n"
"s_add_u32 s42, s42, 2048\n"
"s_add_u32 s43, s43, 81920\n"
"v_add_u32 v162, s47, v162\n"
"v_mfma_f32_16x16x32_bf16 a[72:75], v[64:67], v[32:35], a[72:75]\n"
"s_add_u32 s45, s45, 36864\n"
"s_add_u32 s46, s46, 36864\n"
"v_add_u32 v163, s47, v163\n"
"v_mfma_f32_16x16x32_bf16 a[72:75], v[80:83], v[52:55], a[72:75]\n"
"s_add_u32 s55, s55, 1\n"
"v_add_u32 v164, s47, v164\n"
"v_mfma_f32_16x16x32_bf16 a[76:79], v[68:71], v[32:35], a[76:79]\n"
"s_cmp_eq_u32 s55, 4\n"
"s_cselect_b32 s60, 147456, 0\n"
"s_cselect_b32 s55, 0, s55\n"
"v_mfma_f32_16x16x32_bf16 a[76:79], v[84:87], v[52:55], a[76:79]\n"
"s_sub_u32 s45, s45, s60\n"
"s_sub_u32 s46, s46, s60\n"
"s_waitcnt lgkmcnt(0)\n"
"v_mfma_f32_16x16x32_bf16 a[0:3], v[128:131], v[88:91], a[0:3]\n"
"s_waitcnt vmcnt(18) lgkmcnt(0)\n"
"s_barrier\n"
"v_mfma_f32_16x16x32_bf16 a[0:3], v[144:147], v[108:111], a[0:3]\n"
"s_mov_b32 m0, s45\n"
"ds_read_b128 v[16:19], v164 offset:0\n"
"v_mfma_f32_16x16x32_bf16 a[4:7], v[132:135], v[88:91], a[4:7]\n"
"ds_read_b128 v[20:23], v164 offset:1024\n"
"buffer_load_dwordx4 v165, s[20:23], s42 offen offset:0 sc1 lds\n"
"v_mfma_f32_16x16x32_bf16 a[4:7], v[148:151], v[108:111], a[4:7]\n"
"ds_read_b128 v[24:27], v164 offset:2048\n"
"v_mfma_f32_16x16x32_bf16 a[8:11], v[136:139], v[88:91], a[8:11]\n"
"ds_read_b128 v[28:31], v164 offset:3072\n"
"v_mfma_f32_16x16x32_bf16 a[8:11], v[152:155], v[108:111], a[8:11]\n"
"ds_read_b128 v[32:35], v164 offset:4096\n"
"v_mfma_f32_16x16x32_bf16 a[12:15], v[140:143], v[88:91], a[12:15]\n"
"ds_read_b64_tr_b16 v[56:57], v160 offset:20480\n"
"buffer_load_dwordx4 v166, s[20:23], s42 offen offset:1024 sc1 lds\n"
"v_mfma_f32_16x16x32_bf16 a[12:15], v[156:159], v[108:111], a[12:15]\n"
"ds_read_b64_tr_b16 v[58:59], v160 offset:24576\n"
"v_mfma_f32_16x16x32_bf16 a[16:19], v[128:131], v[92:95], a[16:19]\n"
"v_mfma_f32_16x16x32_bf16 a[16:19], v[144:147], v[112:115], a[16:19]\n"
"ds_read_b64_tr_b16 v[60:61], v161 offset:20480\n"
"v_mfma_f32_16x16x32_bf16 a[20:23], v[132:135], v[92:95], a[20:23]\n"
"ds_read_b64_tr_b16 v[62:63], v161 offset:24576\n"
"buffer_load_dwordx4 v167, s[20:23], s42 offen offset:2048 sc1 lds\n"
"v_mfma_f32_16x16x32_bf16 a[20:23], v[148:151], v[112:115], a[20:23]\n"
"ds_read_b64_tr_b16 v[64:65], v162 offset:20480\n"
"v_mfma_f32_16x16x32_bf16 a[24:27], v[136:139], v[92:95], a[24:27]\n"
"ds_read_b64_tr_b16 v[66:67], v162 offset:24576\n"
"v_mfma_f32_16x16x32_bf16 a[24:27], v[152:155], v[112:115], a[24:27]\n"
"ds_read_b64_tr_b16 v[68:69], v163 offset:20480\n"
"v_mfma_f32_16x16x32_bf16 a[28:31], v[140:143], v[92:95], a[28:31]\n"
"ds_read_b64_tr_b16 v[70:71], v163 offset:24576\n"
"buffer_load_dwordx4 v168, s[20:23], s42 offen offset:3072 sc1 lds\n"
"v_mfma_f32_16x16x32_bf16 a[28:31], v[156:159], v[112:115], a[28:31]\n"
"v_mfma_f32_16x16x32_bf16 a[32:35], v[128:131], v[96:99], a[32:35]\n"
"ds_read_b128 v[36:39], v164 offset:10240\n"
"v_mfma_f32_16x16x32_bf16 a[32:35], v[144:147], v[116:119], a[32:35]\n"
"s_add_u32 m0, s45, 4096\n"
"ds_read_b128 v[40:43], v164 offset:11264\n"
"v_mfma_f32_16x16x32_bf16 a[36:39], v[132:135], v[96:99], a[36:39]\n"
"ds_read_b128 v[44:47], v164 offset:12288\n"
"buffer_load_dwordx4 v169, s[20:23], s42 offen offset:0 sc1 lds\n"
"v_mfma_f32_16x16x32_bf16 a[36:39], v[148:151], v[116:119], a[36:39]\n"
"ds_read_b128 v[48:51], v164 offset:13312\n"
"v_mfma_f32_16x16x32_bf16 a[40:43], v[136:139], v[96:99], a[40:43]\n"
"ds_read_b128 v[52:55], v164 offset:14336\n"
"v_mfma_f32_16x16x32_bf16 a[40:43], v[152:155], v[116:119], a[40:43]\n"
"s_mov_b32 m0, s46\n"
"ds_read_b64_tr_b16 v[72:73], v160 offset:28672\n"
"v_mfma_f32_16x16x32_bf16 a[44:47], v[140:143], v[96:99], a[44:47]\n"
"ds_read_b64_tr_b16 v[74:75], v160 offset:32768\n"
"buffer_load_dwordx4 v170, s[32:35], s43 offen offset:0 lds\n"
"v_mfma_f32_16x16x32_bf16 a[44:47], v[156:159], v[116:119], a[44:47]\n"
"v_mfma_f32_16x16x32_bf16 a[48:51], v[128:131], v[100:103], a[48:51]\n"
"ds_read_b64_tr_b16 v[76:77], v161 offset:28672\n"
"v_mfma_f32_16x16x32_bf16 a[48:51], v[144:147], v[120:123], a[48:51]\n"
"ds_read_b64_tr_b16 v[78:79], v161 offset:32768\n"
"v_mfma_f32_16x16x32_bf16 a[52:55], v[132:135], v[100:103], a[52:55]\n"
"ds_read_b64_tr_b16 v[80:81], v162 offset:28672\n"
"buffer_load_dwordx4 v171, s[32:35], s43 offen offset:1024 lds\n"
"v_mfma_f32_16x16x32_bf16 a[52:55], v[148:151], v[120:123], a[52:55]\n"
"ds_read_b64_tr_b16 v[82:83], v162 offset:32768\n"
"v_mfma_f32_16x16x32_bf16 a[56:59], v[136:139], v[100:103], a[56:59]\n"
"ds_read_b64_tr_b16 v[84:85], v163 offset:28672\n"
"v_mfma_f32_16x16x32_bf16 a[56:59], v[152:155], v[120:123], a[56:59]\n"
"ds_read_b64_tr_b16 v[86:87], v163 offset:32768\n"
"v_mfma_f32_16x16x32_bf16 a[60:63], v[140:143], v[100:103], a[60:63]\n"
"buffer_load_dwordx4 v172, s[32:35], s43 offen offset:2048 lds\n"
"v_mfma_f32_16x16x32_bf16 a[60:63], v[156:159], v[120:123], a[60:63]\n"
"s_cmp_eq_u32 s48, 3\n"
"s_cselect_b32 s47, s68, s69\n"
"v_mfma_f32_16x16x32_bf16 a[64:67], v[128:131], v[104:107], a[64:67]\n"
"s_add_u32 s48, s48, 1\n"
"v_mfma_f32_16x16x32_bf16 a[64:67], v[144:147], v[124:127], a[64:67]\n"
"s_cmp_eq_u32 s48, 4\n"
"s_cselect_b32 s48, 0, s48\n"
"v_add_u32 v160, s47, v160\n"
"v_mfma_f32_16x16x32_bf16 a[68:71], v[132:135], v[104:107], a[68:71]\n"
"buffer_load_dwordx4 v173, s[32:35], s43 offen offset:3072 lds\n"
"v_add_u32 v161, s47, v161\n"
"v_mfma_f32_16x16x32_bf16 a[68:71], v[148:151], v[124:127], a[68:71]\n"
"s_add_u32 s42, s42, 2048\n"
"s_add_u32 s43, s43, 81920\n"
"v_add_u32 v162, s47, v162\n"
"v_mfma_f32_16x16x32_bf16 a[72:75], v[136:139], v[104:107], a[72:75]\n"
"s_add_u32 s45, s45, 36864\n"
"s_add_u32 s46, s46, 36864\n"
"v_add_u32 v163, s47, v163\n"
"v_mfma_f32_16x16x32_bf16 a[72:75], v[152:155], v[124:127], a[72:75]\n"
"s_add_u32 s55, s55, 1\n"
"v_add_u32 v164, s47, v164\n"
"v_mfma_f32_16x16x32_bf16 a[76:79], v[140:143], v[104:107], a[76:79]\n"
"s_cmp_eq_u32 s55, 4\n"
"s_cselect_b32 s60, 147456, 0\n"
"s_cselect_b32 s55, 0, s55\n"
"v_mfma_f32_16x16x32_bf16 a[76:79], v[156:159], v[124:127], a[76:79]\n"
"s_sub_u32 s45, s45, s60\n"
"s_sub_u32 s46, s46, s60\n"
"s_waitcnt lgkmcnt(0)\n"
"v_mfma_f32_16x16x32_bf16 a[0:3], v[56:59], v[16:19], a[0:3]\n"
"s_waitcnt vmcnt(18) lgkmcnt(0)\n"
"s_barrier\n"
"v_mfma_f32_16x16x32_bf16 a[0:3], v[72:75], v[36:39], a[0:3]\n"
"s_mov_b32 m0, s45\n"
"ds_read_b128 v[88:91], v164 offset:0\n"
"v_mfma_f32_16x16x32_bf16 a[4:7], v[60:63], v[16:19], a[4:7]\n"
"ds_read_b128 v[92:95], v164 offset:1024\n"
"buffer_load_dwordx4 v165, s[20:23], s42 offen offset:0 sc1 lds\n"
"v_mfma_f32_16x16x32_bf16 a[4:7], v[76:79], v[36:39], a[4:7]\n"
"ds_read_b128 v[96:99], v164 offset:2048\n"
"v_mfma_f32_16x16x32_bf16 a[8:11], v[64:67], v[16:19], a[8:11]\n"
"ds_read_b128 v[100:103], v164 offset:3072\n"
"v_mfma_f32_16x16x32_bf16 a[8:11], v[80:83], v[36:39], a[8:11]\n"
"ds_read_b128 v[104:107], v164 offset:4096\n"
"v_mfma_f32_16x16x32_bf16 a[12:15], v[68:71], v[16:19], a[12:15]\n"
"ds_read_b64_tr_b16 v[128:129], v160 offset:20480\n"
"buffer_load_dwordx4 v166, s[20:23], s42 offen offset:1024 sc1 lds\n"
"v_mfma_f32_16x16x32_bf16 a[12:15], v[84:87], v[36:39], a[12:15]\n"
"ds_read_b64_tr_b16 v[130:131], v160 offset:24576\n"
"v_mfma_f32_16x16x32_bf16 a[16:19], v[56:59], v[20:23], a[16:19]\n"
"v_mfma_f32_16x16x32_bf16 a[16:19], v[72:75], v[40:43], a[16:19]\n"
"ds_read_b64_tr_b16 v[132:133], v161 offset:20480\n"
"v_mfma_f32_16x16x32_bf16 a[20:23], v[60:63], v[20:23], a[20:23]\n"
"ds_read_b64_tr_b16 v[134:135], v161 offset:24576\n"
"buffer_load_dwordx4 v167, s[20:23], s42 offen offset:2048 sc1 lds\n"
"v_mfma_f32_16x16x32_bf16 a[20:23], v[76:79], v[40:43], a[20:23]\n"
"ds_read_b64_tr_b16 v[136:137], v162 offset:20480\n"
"v_mfma_f32_16x16x32_bf16 a[24:27], v[64:67], v[20:23], a[24:27]\n"
"ds_read_b64_tr_b16 v[138:139], v162 offset:24576\n"
"v_mfma_f32_16x16x32_bf16 a[24:27], v[80:83], v[40:43], a[24:27]\n"
"ds_read_b64_tr_b16 v[140:141], v163 offset:20480\n"
"v_mfma_f32_16x16x32_bf16 a[28:31], v[68:71], v[20:23], a[28:31]\n"
"ds_read_b64_tr_b16 v[142:143], v163 offset:24576\n"
"buffer_load_dwordx4 v168, s[20:23], s42 offen offset:3072 sc1 lds\n"
"v_mfma_f32_16x16x32_bf16 a[28:31], v[84:87], v[40:43], a[28:31]\n"
"v_mfma_f32_16x16x32_bf16 a[32:35], v[56:59], v[24:27], a[32:35]\n"
"ds_read_b128 v[108:111], v164 offset:10240\n"
"v_mfma_f32_16x16x32_bf16 a[32:35], v[72:75], v[44:47], a[32:35]\n"
"s_add_u32 m0, s45, 4096\n"
"ds_read_b128 v[112:115], v164 offset:11264\n"
"v_mfma_f32_16x16x32_bf16 a[36:39], v[60:63], v[24:27], a[36:39]\n"
"ds_read_b128 v[116:119], v164 offset:12288\n"
"buffer_load_dwordx4 v169, s[20:23], s42 offen offset:0 sc1 lds\n"
"v_mfma_f32_16x16x32_bf16 a[36:39], v[76:79], v[44:47], a[36:39]\n"
"ds_read_b128 v[120:123], v164 offset:13312\n"
"v_mfma_f32_16x16x32_bf16 a[40:43], v[64:67], v[24:27], a[40:43]\n"
"ds_read_b128 v[124:127], v164 offset:14336\n"
"v_mfma_f32_16x16x32_bf16 a[40:43], v[80:83], v[44:47], a[40:43]\n"
"s_mov_b32 m0, s46\n"
"ds_read_b64_tr_b16 v[144:145], v160 offset:28672\n"
"v_mfma_f32_16x16x32_bf16 a[44:47], v[68:71], v[24:27], a[44:47]\n"
"ds_read_b64_tr_b16 v[146:147], v160 offset:32768\n"
"buffer_load_dwordx4 v170, s[32:35], s43 offen offset:0 lds\n"
"v_mfma_f32_16x16x32_bf16 a[44:47], v[84:87], v[44:47], a[44:47]\n"
"v_mfma_f32_16x16x32_bf16 a[48:51], v[56:59], v[28:31], a[48:51]\n"
"ds_read_b64_tr_b16 v[148:149], v161 offset:28672\n"
"v_mfma_f32_16x16x32_bf16 a[48:51], v[72:75], v[48:51], a[48:51]\n"
"ds_read_b64_tr_b16 v[150:151], v161 offset:32768\n"
"v_mfma_f32_16x16x32_bf16 a[52:55], v[60:63], v[28:31], a[52:55]\n"
"ds_read_b64_tr_b16 v[152:153], v162 offset:28672\n"
"buffer_load_dwordx4 v171, s[32:35], s43 offen offset:1024 lds\n"
"v_mfma_f32_16x16x32_bf16 a[52:55], v[76:79], v[48:51], a[52:55]\n"
"ds_read_b64_tr_b16 v[154:155], v162 offset:32768\n"
"v_mfma_f32_16x16x32_bf16 a[56:59], v[64:67], v[28:31], a[56:59]\n"
"ds_read_b64_tr_b16 v[156:157], v163 offset:28672\n"
"v_mfma_f32_16x16x32_bf16 a[56:59], v[80:83], v[48:51], a[56:59]\n"
"ds_read_b64_tr_b16 v[158:159], v163 offset:32768\n"
"v_mfma_f32_16x16x32_bf16 a[60:63], v[68:71], v[28:31], a[60:63]\n"
"buffer_load_dwordx4 v172, s[32:35], s43 offen offset:2048 lds\n"
"v_mfma_f32_16x16x32_bf16 a[60:63], v[84:87], v[48:51], a[60:63]\n"
"s_cmp_eq_u32 s48, 3\n"
"s_cselect_b32 s47, s68, s69\n"
"v_mfma_f32_16x16x32_bf16 a[64:67], v[56:59], v[32:35], a[64:67]\n"
"s_add_u32 s48, s48, 1\n"
"v_mfma_f32_16x16x32_bf16 a[64:67], v[72:75], v[52:55], a[64:67]\n"
"s_cmp_eq_u32 s48, 4\n"
"s_cselect_b32 s48, 0, s48\n"
"v_add_u32 v160, s47, v160\n"
"v_mfma_f32_16x16x32_bf16 a[68:71], v[60:63], v[32:35], a[68:71]\n"
"buffer_load_dwordx4 v173, s[32:35], s43 offen offset:3072 lds\n"
"v_add_u32 v161, s47, v161\n"
"v_mfma_f32_16x16x32_bf16 a[68:71], v[76:79], v[52:55], a[68:71]\n"
"s_add_u32 s42, s42, 2048\n"
"s_add_u32 s43, s43, 81920\n"
"v_add_u32 v162, s47, v162\n"
"v_mfma_f32_16x16x32_bf16 a[72:75], v[64:67], v[32:35], a[72:75]\n"
"s_add_u32 s45, s45, 36864\n"
"s_add_u32 s46, s46, 36864\n"
"v_add_u32 v163, s47, v163\n"
"v_mfma_f32_16x16x32_bf16 a[72:75], v[80:83], v[52:55], a[72:75]\n"
"s_add_u32 s55, s55, 1\n"
"v_add_u32 v164, s47, v164\n"
"v_mfma_f32_16x16x32_bf16 a[76:79], v[68:71], v[32:35], a[76:79]\n"
"s_cmp_eq_u32 s55, 4\n"
"s_cselect_b32 s60, 147456, 0\n"
"s_cselect_b32 s55, 0, s55\n"
"v_mfma_f32_16x16x32_bf16 a[76:79], v[84:87], v[52:55], a[76:79]\n"
"s_sub_u32 s45, s45, s60\n"
"s_sub_u32 s46, s46, s60\n"
"s_waitcnt lgkmcnt(0)\n"
"v_mfma_f32_16x16x32_bf16 a[0:3], v[128:131], v[88:91], a[0:3]\n"
"s_waitcnt vmcnt(18) lgkmcnt(0)\n"
"s_barrier\n"
"v_mfma_f32_16x16x32_bf16 a[0:3], v[144:147], v[108:111], a[0:3]\n"
"s_mov_b32 m0, s45\n"
"ds_read_b128 v[16:19], v164 offset:0\n"
"v_mfma_f32_16x16x32_bf16 a[4:7], v[132:135], v[88:91], a[4:7]\n"
"ds_read_b128 v[20:23], v164 offset:1024\n"
"buffer_load_dwordx4 v165, s[20:23], s42 offen offset:0 sc1 lds\n"
"v_mfma_f32_16x16x32_bf16 a[4:7], v[148:151], v[108:111], a[4:7]\n"
"ds_read_b128 v[24:27], v164 offset:2048\n"
"v_mfma_f32_16x16x32_bf16 a[8:11], v[136:139], v[88:91], a[8:11]\n"
"ds_read_b128 v[28:31], v164 offset:3072\n"
"v_mfma_f32_16x16x32_bf16 a[8:11], v[152:155], v[108:111], a[8:11]\n"
"ds_read_b128 v[32:35], v164 offset:4096\n"
"v_mfma_f32_16x16x32_bf16 a[12:15], v[140:143], v[88:91], a[12:15]\n"
"ds_read_b64_tr_b16 v[56:57], v160 offset:20480\n"
"buffer_load_dwordx4 v166, s[20:23], s42 offen offset:1024 sc1 lds\n"
"v_mfma_f32_16x16x32_bf16 a[12:15], v[156:159], v[108:111], a[12:15]\n"
"ds_read_b64_tr_b16 v[58:59], v160 offset:24576\n"
"v_mfma_f32_16x16x32_bf16 a[16:19], v[128:131], v[92:95], a[16:19]\n"
"v_mfma_f32_16x16x32_bf16 a[16:19], v[144:147], v[112:115], a[16:19]\n"
"ds_read_b64_tr_b16 v[60:61], v161 offset:20480\n"
"v_mfma_f32_16x16x32_bf16 a[20:23], v[132:135], v[92:95], a[20:23]\n"
"ds_read_b64_tr_b16 v[62:63], v161 offset:24576\n"
"buffer_load_dwordx4 v167, s[20:23], s42 offen offset:2048 sc1 lds\n"
"v_mfma_f32_16x16x32_bf16 a[20:23], v[148:151], v[112:115], a[20:23]\n"
"ds_read_b64_tr_b16 v[64:65], v162 offset:20480\n"
"v_mfma_f32_16x16x32_bf16 a[24:27], v[136:139], v[92:95], a[24:27]\n"
"ds_read_b64_tr_b16 v[66:67], v162 offset:24576\n"
"v_mfma_f32_16x16x32_bf16 a[24:27], v[152:155], v[112:115], a[24:27]\n"
"ds_read_b64_tr_b16 v[68:69], v163 offset:20480\n"
"v_mfma_f32_16x16x32_bf16 a[28:31], v[140:143], v[92:95], a[28:31]\n"
"ds_read_b64_tr_b16 v[70:71], v163 offset:24576\n"
"buffer_load_dwordx4 v168, s[20:23], s42 offen offset:3072 sc1 lds\n"
"v_mfma_f32_16x16x32_bf16 a[28:31], v[156:159], v[112:115], a[28:31]\n"
"v_mfma_f32_16x16x32_bf16 a[32:35], v[128:131], v[96:99], a[32:35]\n"
"ds_read_b128 v[36:39], v164 offset:10240\n"
"v_mfma_f32_16x16x32_bf16 a[32:35], v[144:147], v[116:119], a[32:35]\n"
"s_add_u32 m0, s45, 4096\n"
"ds_read_b128 v[40:43], v164 offset:11264\n"
"v_mfma_f32_16x16x32_bf16 a[36:39], v[132:135], v[96:99], a[36:39]\n"
"ds_read_b128 v[44:47], v164 offset:12288\n"
"buffer_load_dwordx4 v169, s[20:23], s42 offen offset:0 sc1 lds\n"
"v_mfma_f32_16x16x32_bf16 a[36:39], v[148:151], v[116:119], a[36:39]\n"
"ds_read_b128 v[48:51], v164 offset:13312\n"
"v_mfma_f32_16x16x32_bf16 a[40:43], v[136:139], v[96:99], a[40:43]\n"
"ds_read_b128 v[52:55], v164 offset:14336\n"
"v_mfma_f32_16x16x32_bf16 a[40:43], v[152:155], v[116:119], a[40:43]\n"
"s_mov_b32 m0, s46\n"
"ds_read_b64_tr_b16 v[72:73], v160 offset:28672\n"
"v_mfma_f32_16x16x32_bf16 a[44:47], v[140:143], v[96:99], a[44:47]\n"
"ds_read_b64_tr_b16 v[74:75], v160 offset:32768\n"
"buffer_load_dwordx4 v170, s[32:35], s43 offen offset:0 lds\n"
"v_mfma_f32_16x16x32_bf16 a[44:47], v[156:159], v[116:119], a[44:47]\n"
"v_mfma_f32_16x16x32_bf16 a[48:51], v[128:131], v[100:103], a[48:51]\n"
"ds_read_b64_tr_b16 v[76:77], v161 offset:28672\n"
"v_mfma_f32_16x16x32_bf16 a[48:51], v[144:147], v[120:123], a[48:51]\n"
"ds_read_b64_tr_b16 v[78:79], v161 offset:32768\n"
"v_mfma_f32_16x16x32_bf16 a[52:55], v[132:135], v[100:103], a[52:55]\n"
"ds_read_b64_tr_b16 v[80:81], v162 offset:28672\n"
"buffer_load_dwordx4 v171, s[32:35], s43 offen offset:1024 lds\n"
"v_mfma_f32_16x16x32_bf16 a[52:55], v[148:151], v[120:123], a[52:55]\n"
"ds_read_b64_tr_b16 v[82:83], v162 offset:32768\n"
"v_mfma_f32_16x16x32_bf16 a[56:59], v[136:139], v[100:103], a[56:59]\n"
"ds_read_b64_tr_b16 v[84:85], v163 offset:28672\n"
"v_mfma_f32_16x16x32_bf16 a[56:59], v[152:155], v[120:123], a[56:59]\n"
"ds_read_b64_tr_b16 v[86:87], v163 offset:32768\n"
"v_mfma_f32_16x16x32_bf16 a[60:63], v[140:143], v[100:103], a[60:63]\n"
"buffer_load_dwordx4 v172, s[32:35], s43 offen offset:2048 lds\n"
"v_mfma_f32_16x16x32_bf16 a[60:63], v[156:159], v[120:123], a[60:63]\n"
"s_cmp_eq_u32 s48, 3\n"
"s_cselect_b32 s47, s68, s69\n"
"v_mfma_f32_16x16x32_bf16 a[64:67], v[128:131], v[104:107], a[64:67]\n"
"s_add_u32 s48, s48, 1\n"
"v_mfma_f32_16x16x32_bf16 a[64:67], v[144:147], v[124:127], a[64:67]\n"
"s_cmp_eq_u32 s48, 4\n"
"s_cselect_b32 s48, 0, s48\n"
"v_add_u32 v160, s47, v160\n"
"v_mfma_f32_16x16x32_bf16 a[68:71], v[132:135], v[104:107], a[68:71]\n"
"buffer_load_dwordx4 v173, s[32:35], s43 offen offset:3072 lds\n"
"v_add_u32 v161, s47, v161\n"
"v_mfma_f32_16x16x32_bf16 a[68:71], v[148:151], v[124:127], a[68:71]\n"
"s_add_u32 s42, s42, 2048\n"
"s_add_u32 s43, s43, 81920\n"
"v_add_u32 v162, s47, v162\n"
"v_mfma_f32_16x16x32_bf16 a[72:75], v[136:139], v[104:107], a[72:75]\n"
"s_add_u32 s45, s45, 36864\n"
"s_add_u32 s46, s46, 36864\n"
"v_add_u32 v163, s47, v163\n"
"v_mfma_f32_16x16x32_bf16 a[72:75], v[152:155], v[124:127], a[72:75]\n"
"s_add_u32 s55, s55, 1\n"
"v_add_u32 v164, s47, v164\n"
"v_mfma_f32_16x16x32_bf16 a[76:79], v[140:143], v[104:107], a[76:79]\n"
"s_cmp_eq_u32 s55, 4\n"
"s_cselect_b32 s60, 147456, 0\n"
"s_cselect_b32 s55, 0, s55\n"
"v_mfma_f32_16x16x32_bf16 a[76:79], v[156:159], v[124:127], a[76:79]\n"
"s_sub_u32 s45, s45, s60\n"
"s_sub_u32 s46, s46, s60\n"
"s_waitcnt lgkmcnt(0)\n"
"v_mfma_f32_16x16x32_bf16 a[0:3], v[56:59], v[16:19], a[0:3]\n"
"s_waitcnt vmcnt(18) lgkmcnt(0)\n"
"s_barrier\n"
"v_mfma_f32_16x16x32_bf16 a[0:3], v[72:75], v[36:39], a[0:3]\n"
"ds_read_b128 v[88:91], v164 offset:0\n"
"v_mfma_f32_16x16x32_bf16 a[4:7], v[60:63], v[16:19], a[4:7]\n"
"ds_read_b128 v[92:95], v164 offset:1024\n"
"v_mfma_f32_16x16x32_bf16 a[4:7], v[76:79], v[36:39], a[4:7]\n"
"ds_read_b128 v[96:99], v164 offset:2048\n"
"v_mfma_f32_16x16x32_bf16 a[8:11], v[64:67], v[16:19], a[8:11]\n"
"ds_read_b128 v[100:103], v164 offset:3072\n"
"v_mfma_f32_16x16x32_bf16 a[8:11], v[80:83], v[36:39], a[8:11]\n"
"ds_read_b128 v[104:107], v164 offset:4096\n"
"v_mfma_f32_16x16x32_bf16 a[12:15], v[68:71], v[16:19], a[12:15]\n"
"ds_read_b64_tr_b16 v[128:129], v160 offset:20480\n"
"v_mfma_f32_16x16x32_bf16 a[12:15], v[84:87], v[36:39], a[12:15]\n"
"ds_read_b64_tr_b16 v[130:131], v160 offset:24576\n"
"v_mfma_f32_16x16x32_bf16 a[16:19], v[56:59], v[20:23], a[16:19]\n"
"v_mfma_f32_16x16x32_bf16 a[16:19], v[72:75], v[40:43], a[16:19]\n"
"ds_read_b64_tr_b16 v[132:133], v161 offset:20480\n"
"v_mfma_f32_16x16x32_bf16 a[20:23], v[60:63], v[20:23], a[20:23]\n"
"ds_read_b64_tr_b16 v[134:135], v161 offset:24576\n"
"v_mfma_f32_16x16x32_bf16 a[20:23], v[76:79], v[40:43], a[20:23]\n"
"ds_read_b64_tr_b16 v[136:137], v162 offset:20480\n"
"v_mfma_f32_16x16x32_bf16 a[24:27], v[64:67], v[20:23], a[24:27]\n"
"ds_read_b64_tr_b16 v[138:139], v162 offset:24576\n"
"v_mfma_f32_16x16x32_bf16 a[24:27], v[80:83], v[40:43], a[24:27]\n"
"ds_read_b64_tr_b16 v[140:141], v163 offset:20480\n"
"v_mfma_f32_16x16x32_bf16 a[28:31], v[68:71], v[20:23], a[28:31]\n"
"ds_read_b64_tr_b16 v[142:143], v163 offset:24576\n"
"v_mfma_f32_16x16x32_bf16 a[28:31], v[84:87], v[40:43], a[28:31]\n"
"v_mfma_f32_16x16x32_bf16 a[32:35], v[56:59], v[24:27], a[32:35]\n"
"ds_read_b128 v[108:111], v164 offset:10240\n"
"v_mfma_f32_16x16x32_bf16 a[32:35], v[72:75], v[44:47], a[32:35]\n"
"ds_read_b128 v[112:115], v164 offset:11264\n"
"v_mfma_f32_16x16x32_bf16 a[36:39], v[60:63], v[24:27], a[36:39]\n"
"ds_read_b128 v[116:119], v164 offset:12288\n"
"v_mfma_f32_16x16x32_bf16 a[36:39], v[76:79], v[44:47], a[36:39]\n"
"ds_read_b128 v[120:123], v164 offset:13312\n"
"v_mfma_f32_16x16x32_bf16 a[40:43], v[64:67], v[24:27], a[40:43]\n"
"ds_read_b128 v[124:127], v164 offset:14336\n"
"v_mfma_f32_16x16x32_bf16 a[40:43], v[80:83], v[44:47], a[40:43]\n"
"ds_read_b64_tr_b16 v[144:145], v160 offset:28672\n"
"v_mfma_f32_16x16x32_bf16 a[44:47], v[68:71], v[24:27], a[44:47]\n"
"ds_read_b64_tr_b16 v[146:147], v160 offset:32768\n"
"v_mfma_f32_16x16x32_bf16 a[44:47], v[84:87], v[44:47], a[44:47]\n"
"v_mfma_f32_16x16x32_bf16 a[48:51], v[56:59], v[28:31], a[48:51]\n"
"ds_read_b64_tr_b16 v[148:149], v161 offset:28672\n"
"v_mfma_f32_16x16x32_bf16 a[48:51], v[72:75], v[48:51], a[48:51]\n"
"ds_read_b64_tr_b16 v[150:151], v161 offset:32768\n"
"v_mfma_f32_16x16x32_bf16 a[52:55], v[60:63], v[28:31], a[52:55]\n"
"ds_read_b64_tr_b16 v[152:153], v162 offset:28672\n"
"v_mfma_f32_16x16x32_bf16 a[52:55], v[76:79], v[48:51], a[52:55]\n"
"ds_read_b64_tr_b16 v[154:155], v162 offset:32768\n"
"v_mfma_f32_16x16x32_bf16 a[56:59], v[64:67], v[28:31], a[56:59]\n"
"ds_read_b64_tr_b16 v[156:157], v163 offset:28672\n"
"v_mfma_f32_16x16x32_bf16 a[56:59], v[80:83], v[48:51], a[56:59]\n"
"ds_read_b64_tr_b16 v[158:159], v163 offset:32768\n"
"v_mfma_f32_16x16x32_bf16 a[60:63], v[68:71], v[28:31], a[60:63]\n"
"v_mfma_f32_16x16x32_bf16 a[60:63], v[84:87], v[48:51], a[60:63]\n"
"s_cmp_eq_u32 s48, 3\n"
"s_cselect_b32 s47, s68, s69\n"
"v_mfma_f32_16x16x32_bf16 a[64:67], v[56:59], v[32:35], a[64:67]\n"
"s_add_u32 s48, s48, 1\n"
"v_mfma_f32_16x16x32_bf16 a[64:67], v[72:75], v[52:55], a[64:67]\n"
"s_cmp_eq_u32 s48, 4\n"
"s_cselect_b32 s48, 0, s48\n"
"v_add_u32 v160, s47, v160\n"
"v_mfma_f32_16x16x32_bf16 a[68:71], v[60:63], v[32:35], a[68:71]\n"
"v_add_u32 v161, s47, v161\n"
"v_mfma_f32_16x16x32_bf16 a[68:71], v[76:79], v[52:55], a[68:71]\n"
"v_add_u32 v162, s47, v162\n"
"v_mfma_f32_16x16x32_bf16 a[72:75], v[64:67], v[32:35], a[72:75]\n"
"v_add_u32 v163, s47, v163\n"
"v_mfma_f32_16x16x32_bf16 a[72:75], v[80:83], v[52:55], a[72:75]\n"
"v_add_u32 v164, s47, v164\n"
"v_mfma_f32_16x16x32_bf16 a[76:79], v[68:71], v[32:35], a[76:79]\n"
"v_mfma_f32_16x16x32_bf16 a[76:79], v[84:87], v[52:55], a[76:79]\n"
"s_waitcnt lgkmcnt(0)\n"
"v_mfma_f32_16x16x32_bf16 a[0:3], v[128:131], v[88:91], a[0:3]\n"
"s_waitcnt vmcnt(9) lgkmcnt(0)\n"
"s_barrier\n"
"v_mfma_f32_16x16x32_bf16 a[0:3], v[144:147], v[108:111], a[0:3]\n"
"ds_read_b128 v[16:19], v164 offset:0\n"
"v_mfma_f32_16x16x32_bf16 a[4:7], v[132:135], v[88:91], a[4:7]\n"
"ds_read_b128 v[20:23], v164 offset:1024\n"
"v_mfma_f32_16x16x32_bf16 a[4:7], v[148:151], v[108:111], a[4:7]\n"
"ds_read_b128 v[24:27], v164 offset:2048\n"
"v_mfma_f32_16x16x32_bf16 a[8:11], v[136:139], v[88:91], a[8:11]\n"
"ds_read_b128 v[28:31], v164 offset:3072\n"
"v_mfma_f32_16x16x32_bf16 a[8:11], v[152:155], v[108:111], a[8:11]\n"
"ds_read_b128 v[32:35], v164 offset:4096\n"
"v_mfma_f32_16x16x32_bf16 a[12:15], v[140:143], v[88:91], a[12:15]\n"
"ds_read_b64_tr_b16 v[56:57], v160 offset:20480\n"
"v_mfma_f32_16x16x32_bf16 a[12:15], v[156:159], v[108:111], a[12:15]\n"
"ds_read_b64_tr_b16 v[58:59], v160 offset:24576\n"
"v_mfma_f32_16x16x32_bf16 a[16:19], v[128:131], v[92:95], a[16:19]\n"
"v_mfma_f32_16x16x32_bf16 a[16:19], v[144:147], v[112:115], a[16:19]\n"
"ds_read_b64_tr_b16 v[60:61], v161 offset:20480\n"
"v_mfma_f32_16x16x32_bf16 a[20:23], v[132:135], v[92:95], a[20:23]\n"
"ds_read_b64_tr_b16 v[62:63], v161 offset:24576\n"
"v_mfma_f32_16x16x32_bf16 a[20:23], v[148:151], v[112:115], a[20:23]\n"
"ds_read_b64_tr_b16 v[64:65], v162 offset:20480\n"
"v_mfma_f32_16x16x32_bf16 a[24:27], v[136:139], v[92:95], a[24:27]\n"
"ds_read_b64_tr_b16 v[66:67], v162 offset:24576\n"
"v_mfma_f32_16x16x32_bf16 a[24:27], v[152:155], v[112:115], a[24:27]\n"
"ds_read_b64_tr_b16 v[68:69], v163 offset:20480\n"
"v_mfma_f32_16x16x32_bf16 a[28:31], v[140:143], v[92:95], a[28:31]\n"
"ds_read_b64_tr_b16 v[70:71], v163 offset:24576\n"
"v_mfma_f32_16x16x32_bf16 a[28:31], v[156:159], v[112:115], a[28:31]\n"
"v_mfma_f32_16x16x32_bf16 a[32:35], v[128:131], v[96:99], a[32:35]\n"
"ds_read_b128 v[36:39], v164 offset:10240\n"
"v_mfma_f32_16x16x32_bf16 a[32:35], v[144:147], v[116:119], a[32:35]\n"
"ds_read_b128 v[40:43], v164 offset:11264\n"
"v_mfma_f32_16x16x32_bf16 a[36:39], v[132:135], v[96:99], a[36:39]\n"
"ds_read_b128 v[44:47], v164 offset:12288\n"
"v_mfma_f32_16x16x32_bf16 a[36:39], v[148:151], v[116:119], a[36:39]\n"
"ds_read_b128 v[48:51], v164 offset:13312\n"
"v_mfma_f32_16x16x32_bf16 a[40:43], v[136:139], v[96:99], a[40:43]\n"
"ds_read_b128 v[52:55], v164 offset:14336\n"
"v_mfma_f32_16x16x32_bf16 a[40:43], v[152:155], v[116:119], a[40:43]\n"
"ds_read_b64_tr_b16 v[72:73], v160 offset:28672\n"
"v_mfma_f32_16x16x32_bf16 a[44:47], v[140:143], v[96:99], a[44:47]\n"
"ds_read_b64_tr_b16 v[74:75], v160 offset:32768\n"
"v_mfma_f32_16x16x32_bf16 a[44:47], v[156:159], v[116:119], a[44:47]\n"
"v_mfma_f32_16x16x32_bf16 a[48:51], v[128:131], v[100:103], a[48:51]\n"
"ds_read_b64_tr_b16 v[76:77], v161 offset:28672\n"
"v_mfma_f32_16x16x32_bf16 a[48:51], v[144:147], v[120:123], a[48:51]\n"
"ds_read_b64_tr_b16 v[78:79], v161 offset:32768\n"
"v_mfma_f32_16x16x32_bf16 a[52:55], v[132:135], v[100:103], a[52:55]\n"
"ds_read_b64_tr_b16 v[80:81], v162 offset:28672\n"
"v_mfma_f32_16x16x32_bf16 a[52:55], v[148:151], v[120:123], a[52:55]\n"
"ds_read_b64_tr_b16 v[82:83], v162 offset:32768\n"
"v_mfma_f32_16x16x32_bf16 a[56:59], v[136:139], v[100:103], a[56:59]\n"
"ds_read_b64_tr_b16 v[84:85], v163 offset:28672\n"
"v_mfma_f32_16x16x32_bf16 a[56:59], v[152:155], v[120:123], a[56:59]\n"
"ds_read_b64_tr_b16 v[86:87], v163 offset:32768\n"
"v_mfma_f32_16x16x32_bf16 a[60:63], v[140:143], v[100:103], a[60:63]\n"
"v_mfma_f32_16x16x32_bf16 a[60:63], v[156:159], v[120:123], a[60:63]\n"
"s_cmp_eq_u32 s48, 3\n"
"s_cselect_b32 s47, s68, s69\n"
"v_mfma_f32_16x16x32_bf16 a[64:67], v[128:131], v[104:107], a[64:67]\n"
"s_add_u32 s48, s48, 1\n"
"v_mfma_f32_16x16x32_bf16 a[64:67], v[144:147], v[124:127], a[64:67]\n"
"s_cmp_eq_u32 s48, 4\n"
"s_cselect_b32 s48, 0, s48\n"
"v_add_u32 v160, s47, v160\n"
"v_mfma_f32_16x16x32_bf16 a[68:71], v[132:135], v[104:107], a[68:71]\n"
"v_add_u32 v161, s47, v161\n"
"v_mfma_f32_16x16x32_bf16 a[68:71], v[148:151], v[124:127], a[68:71]\n"
"v_add_u32 v162, s47, v162\n"
"v_mfma_f32_16x16x32_bf16 a[72:75], v[136:139], v[104:107], a[72:75]\n"
"v_add_u32 v163, s47, v163\n"
"v_mfma_f32_16x16x32_bf16 a[72:75], v[152:155], v[124:127], a[72:75]\n"
"v_add_u32 v164, s47, v164\n"
"v_mfma_f32_16x16x32_bf16 a[76:79], v[140:143], v[104:107], a[76:79]\n"
"v_mfma_f32_16x16x32_bf16 a[76:79], v[156:159], v[124:127], a[76:79]\n"
"s_waitcnt lgkmcnt(0)\n"
"v_mfma_f32_16x16x32_bf16 a[0:3], v[56:59], v[16:19], a[0:3]\n"
"s_waitcnt vmcnt(0) lgkmcnt(0)\n"
"s_barrier\n"
"v_mfma_f32_16x16x32_bf16 a[0:3], v[72:75], v[36:39], a[0:3]\n"
"ds_read_b128 v[88:91], v164 offset:0\n"
"v_mfma_f32_16x16x32_bf16 a[4:7], v[60:63], v[16:19], a[4:7]\n"
"ds_read_b128 v[92:95], v164 offset:1024\n"
"v_mfma_f32_16x16x32_bf16 a[4:7], v[76:79], v[36:39], a[4:7]\n"
"ds_read_b128 v[96:99], v164 offset:2048\n"
"v_mfma_f32_16x16x32_bf16 a[8:11], v[64:67], v[16:19], a[8:11]\n"
"ds_read_b128 v[100:103], v164 offset:3072\n"
"v_mfma_f32_16x16x32_bf16 a[8:11], v[80:83], v[36:39], a[8:11]\n"
"ds_read_b128 v[104:107], v164 offset:4096\n"
"v_mfma_f32_16x16x32_bf16 a[12:15], v[68:71], v[16:19], a[12:15]\n"
"ds_read_b64_tr_b16 v[128:129], v160 offset:20480\n"
"v_mfma_f32_16x16x32_bf16 a[12:15], v[84:87], v[36:39], a[12:15]\n"
"ds_read_b64_tr_b16 v[130:131], v160 offset:24576\n"
"v_mfma_f32_16x16x32_bf16 a[16:19], v[56:59], v[20:23], a[16:19]\n"
"v_mfma_f32_16x16x32_bf16 a[16:19], v[72:75], v[40:43], a[16:19]\n"
"ds_read_b64_tr_b16 v[132:133], v161 offset:20480\n"
"v_mfma_f32_16x16x32_bf16 a[20:23], v[60:63], v[20:23], a[20:23]\n"
"ds_read_b64_tr_b16 v[134:135], v161 offset:24576\n"
"v_mfma_f32_16x16x32_bf16 a[20:23], v[76:79], v[40:43], a[20:23]\n"
"ds_read_b64_tr_b16 v[136:137], v162 offset:20480\n"
"v_mfma_f32_16x16x32_bf16 a[24:27], v[64:67], v[20:23], a[24:27]\n"
"ds_read_b64_tr_b16 v[138:139], v162 offset:24576\n"
"v_mfma_f32_16x16x32_bf16 a[24:27], v[80:83], v[40:43], a[24:27]\n"
"ds_read_b64_tr_b16 v[140:141], v163 offset:20480\n"
"v_mfma_f32_16x16x32_bf16 a[28:31], v[68:71], v[20:23], a[28:31]\n"
"ds_read_b64_tr_b16 v[142:143], v163 offset:24576\n"
"v_mfma_f32_16x16x32_bf16 a[28:31], v[84:87], v[40:43], a[28:31]\n"
"v_mfma_f32_16x16x32_bf16 a[32:35], v[56:59], v[24:27], a[32:35]\n"
"ds_read_b128 v[108:111], v164 offset:10240\n"
"v_mfma_f32_16x16x32_bf16 a[32:35], v[72:75], v[44:47], a[32:35]\n"
"ds_read_b128 v[112:115], v164 offset:11264\n"
"v_mfma_f32_16x16x32_bf16 a[36:39], v[60:63], v[24:27], a[36:39]\n"
"ds_read_b128 v[116:119], v164 offset:12288\n"
"v_mfma_f32_16x16x32_bf16 a[36:39], v[76:79], v[44:47], a[36:39]\n"
"ds_read_b128 v[120:123], v164 offset:13312\n"
"v_mfma_f32_16x16x32_bf16 a[40:43], v[64:67], v[24:27], a[40:43]\n"
"ds_read_b128 v[124:127], v164 offset:14336\n"
"v_mfma_f32_16x16x32_bf16 a[40:43], v[80:83], v[44:47], a[40:43]\n"
"ds_read_b64_tr_b16 v[144:145], v160 offset:28672\n"
"v_mfma_f32_16x16x32_bf16 a[44:47], v[68:71], v[24:27], a[44:47]\n"
"ds_read_b64_tr_b16 v[146:147], v160 offset:32768\n"
"v_mfma_f32_16x16x32_bf16 a[44:47], v[84:87], v[44:47], a[44:47]\n"
"v_mfma_f32_16x16x32_bf16 a[48:51], v[56:59], v[28:31], a[48:51]\n"
"ds_read_b64_tr_b16 v[148:149], v161 offset:28672\n"
"v_mfma_f32_16x16x32_bf16 a[48:51], v[72:75], v[48:51], a[48:51]\n"
"ds_read_b64_tr_b16 v[150:151], v161 offset:32768\n"
"v_mfma_f32_16x16x32_bf16 a[52:55], v[60:63], v[28:31], a[52:55]\n"
"ds_read_b64_tr_b16 v[152:153], v162 offset:28672\n"
"v_mfma_f32_16x16x32_bf16 a[52:55], v[76:79], v[48:51], a[52:55]\n"
"ds_read_b64_tr_b16 v[154:155], v162 offset:32768\n"
"v_mfma_f32_16x16x32_bf16 a[56:59], v[64:67], v[28:31], a[56:59]\n"
"ds_read_b64_tr_b16 v[156:157], v163 offset:28672\n"
"v_mfma_f32_16x16x32_bf16 a[56:59], v[80:83], v[48:51], a[56:59]\n"
"ds_read_b64_tr_b16 v[158:159], v163 offset:32768\n"
"v_mfma_f32_16x16x32_bf16 a[60:63], v[68:71], v[28:31], a[60:63]\n"
"v_mfma_f32_16x16x32_bf16 a[60:63], v[84:87], v[48:51], a[60:63]\n"
"s_cmp_eq_u32 s48, 3\n"
"s_cselect_b32 s47, s68, s69\n"
"v_mfma_f32_16x16x32_bf16 a[64:67], v[56:59], v[32:35], a[64:67]\n"
"s_add_u32 s48, s48, 1\n"
"v_mfma_f32_16x16x32_bf16 a[64:67], v[72:75], v[52:55], a[64:67]\n"
"s_cmp_eq_u32 s48, 4\n"
"s_cselect_b32 s48, 0, s48\n"
"v_add_u32 v160, s47, v160\n"
"v_mfma_f32_16x16x32_bf16 a[68:71], v[60:63], v[32:35], a[68:71]\n"
"v_add_u32 v161, s47, v161\n"
"v_mfma_f32_16x16x32_bf16 a[68:71], v[76:79], v[52:55], a[68:71]\n"
"v_add_u32 v162, s47, v162\n"
"v_mfma_f32_16x16x32_bf16 a[72:75], v[64:67], v[32:35], a[72:75]\n"
"v_add_u32 v163, s47, v163\n"
"v_mfma_f32_16x16x32_bf16 a[72:75], v[80:83], v[52:55], a[72:75]\n"
"v_add_u32 v164, s47, v164\n"
"v_mfma_f32_16x16x32_bf16 a[76:79], v[68:71], v[32:35], a[76:79]\n"
"v_mfma_f32_16x16x32_bf16 a[76:79], v[84:87], v[52:55], a[76:79]\n"
"s_mul_i32 s60, s50, 5\n"
"s_add_u32 s60, s60, s51\n"
"s_mul_i32 s61, s60, 327680\n"
"s_mul_i32 s62, s53, 10240\n"
"s_add_u32 s61, s61, s62\n"
"s_mul_i32 s62, s52, 20480\n"
"s_lshl_b32 s63, s54, 1\n"
"s_add_u32 s63, s63, 0\n"
"s_mul_i32 s80, s63, 81920\n"
"s_add_u32 s80, s80, s61\n"
"s_add_u32 s80, s80, s62\n"
"s_cmp_eq_u32 s63, s52\n"
"s_cselect_b32 s80, 0x8000000, s80\n"
"s_add_u32 s81, s80, 4096\n"
"s_add_u32 s82, s80, 8192\n"
"s_lshl_b32 s63, s54, 1\n"
"s_add_u32 s63, s63, 1\n"
"s_mul_i32 s83, s63, 81920\n"
"s_add_u32 s83, s83, s61\n"
"s_add_u32 s83, s83, s62\n"
"s_cmp_eq_u32 s63, s52\n"
"s_cselect_b32 s83, 0x8000000, s83\n"
"s_add_u32 s84, s83, 4096\n"
"s_add_u32 s85, s83, 8192\n"
"v_lshlrev_b32 v5, 4, v4\n"
"s_waitcnt lgkmcnt(0)\n"
"v_mfma_f32_16x16x32_bf16 a[0:3], v[128:131], v[88:91], a[0:3]\n"
"v_mfma_f32_16x16x32_bf16 a[0:3], v[144:147], v[108:111], a[0:3]\n"
"v_mfma_f32_16x16x32_bf16 a[4:7], v[132:135], v[88:91], a[4:7]\n"
"v_mfma_f32_16x16x32_bf16 a[4:7], v[148:151], v[108:111], a[4:7]\n"
"v_mfma_f32_16x16x32_bf16 a[8:11], v[136:139], v[88:91], a[8:11]\n"
"v_mfma_f32_16x16x32_bf16 a[8:11], v[152:155], v[108:111], a[8:11]\n"
"s_nop 3\n"
"buffer_store_dwordx4 a[0:3], v5, s[24:27], s80 offen offset:0 sc1\n"
"v_mfma_f32_16x16x32_bf16 a[12:15], v[140:143], v[88:91], a[12:15]\n"
"v_mfma_f32_16x16x32_bf16 a[12:15], v[156:159], v[108:111], a[12:15]\n"
"s_nop 3\n"
"buffer_store_dwordx4 a[4:7], v5, s[24:27], s80 offen offset:1024 sc1\n"
"v_mfma_f32_16x16x32_bf16 a[16:19], v[128:131], v[92:95], a[16:19]\n"
"v_mfma_f32_16x16x32_bf16 a[16:19], v[144:147], v[112:115], a[16:19]\n"
"s_nop 3\n"
"buffer_store_dwordx4 a[8:11], v5, s[24:27], s83 offen offset:0 sc1\n"
"v_mfma_f32_16x16x32_bf16 a[20:23], v[132:135], v[92:95], a[20:23]\n"
"v_mfma_f32_16x16x32_bf16 a[20:23], v[148:151], v[112:115], a[20:23]\n"
"s_nop 3\n"
"buffer_store_dwordx4 a[12:15], v5, s[24:27], s83 offen offset:1024 sc1\n"
"v_mfma_f32_16x16x32_bf16 a[24:27], v[136:139], v[92:95], a[24:27]\n"
"v_mfma_f32_16x16x32_bf16 a[24:27], v[152:155], v[112:115], a[24:27]\n"
"s_nop 3\n"
"buffer_store_dwordx4 a[16:19], v5, s[24:27], s80 offen offset:2048 sc1\n"
"v_mfma_f32_16x16x32_bf16 a[28:31], v[140:143], v[92:95], a[28:31]\n"
"v_mfma_f32_16x16x32_bf16 a[28:31], v[156:159], v[112:115], a[28:31]\n"
"s_nop 3\n"
"buffer_store_dwordx4 a[20:23], v5, s[24:27], s80 offen offset:3072 sc1\n"
"v_mfma_f32_16x16x32_bf16 a[32:35], v[128:131], v[96:99], a[32:35]\n"
"v_mfma_f32_16x16x32_bf16 a[32:35], v[144:147], v[116:119], a[32:35]\n"
"s_nop 3\n"
"buffer_store_dwordx4 a[24:27], v5, s[24:27], s83 offen offset:2048 sc1\n"
"v_mfma_f32_16x16x32_bf16 a[36:39], v[132:135], v[96:99], a[36:39]\n"
"v_mfma_f32_16x16x32_bf16 a[36:39], v[148:151], v[116:119], a[36:39]\n"
"s_nop 3\n"
"buffer_store_dwordx4 a[28:31], v5, s[24:27], s83 offen offset:3072 sc1\n"
"v_mfma_f32_16x16x32_bf16 a[40:43], v[136:139], v[96:99], a[40:43]\n"
"v_mfma_f32_16x16x32_bf16 a[40:43], v[152:155], v[116:119], a[40:43]\n"
"s_nop 3\n"
"buffer_store_dwordx4 a[32:35], v5, s[24:27], s81 offen offset:0 sc1\n"
"v_mfma_f32_16x16x32_bf16 a[44:47], v[140:143], v[96:99], a[44:47]\n"
"v_mfma_f32_16x16x32_bf16 a[44:47], v[156:159], v[116:119], a[44:47]\n"
"s_nop 3\n"
"buffer_store_dwordx4 a[36:39], v5, s[24:27], s81 offen offset:1024 sc1\n"
"v_mfma_f32_16x16x32_bf16 a[48:51], v[128:131], v[100:103], a[48:51]\n"
"v_mfma_f32_16x16x32_bf16 a[48:51], v[144:147], v[120:123], a[48:51]\n"
"s_nop 3\n"
"buffer_store_dwordx4 a[40:43], v5, s[24:27], s84 offen offset:0 sc1\n"
"v_mfma_f32_16x16x32_bf16 a[52:55], v[132:135], v[100:103], a[52:55]\n"
"v_mfma_f32_16x16x32_bf16 a[52:55], v[148:151], v[120:123], a[52:55]\n"
"s_nop 3\n"
"buffer_store_dwordx4 a[44:47], v5, s[24:27], s84 offen offset:1024 sc1\n"
"v_mfma_f32_16x16x32_bf16 a[56:59], v[136:139], v[100:103], a[56:59]\n"
"v_mfma_f32_16x16x32_bf16 a[56:59], v[152:155], v[120:123], a[56:59]\n"
"s_nop 3\n"
"buffer_store_dwordx4 a[48:51], v5, s[24:27], s81 offen offset:2048 sc1\n"
"v_mfma_f32_16x16x32_bf16 a[60:63], v[140:143], v[100:103], a[60:63]\n"
"v_mfma_f32_16x16x32_bf16 a[60:63], v[156:159], v[120:123], a[60:63]\n"
"s_nop 3\n"
"buffer_store_dwordx4 a[52:55], v5, s[24:27], s81 offen offset:3072 sc1\n"
"v_mfma_f32_16x16x32_bf16 a[64:67], v[128:131], v[104:107], a[64:67]\n"
"v_mfma_f32_16x16x32_bf16 a[64:67], v[144:147], v[124:127], a[64:67]\n"
"s_nop 3\n"
"buffer_store_dwordx4 a[56:59], v5, s[24:27], s84 offen offset:2048 sc1\n"
"v_mfma_f32_16x16x32_bf16 a[68:71], v[132:135], v[104:107], a[68:71]\n"
"v_mfma_f32_16x16x32_bf16 a[68:71], v[148:151], v[124:127], a[68:71]\n"
"s_nop 3\n"
"buffer_store_dwordx4 a[60:63], v5, s[24:27], s84 offen offset:3072 sc1\n"
"v_mfma_f32_16x16x32_bf16 a[72:75], v[136:139], v[104:107], a[72:75]\n"
"v_mfma_f32_16x16x32_bf16 a[72:75], v[152:155], v[124:127], a[72:75]\n"
"s_nop 3\n"
"buffer_store_dwordx4 a[64:67], v5, s[24:27], s82 offen offset:0 sc1\n"
"v_mfma_f32_16x16x32_bf16 a[76:79], v[140:143], v[104:107], a[76:79]\n"
"v_mfma_f32_16x16x32_bf16 a[76:79], v[156:159], v[124:127], a[76:79]\n"
"s_nop 3\n"
"buffer_store_dwordx4 a[68:71], v5, s[24:27], s82 offen offset:1024 sc1\n"
"s_nop 7\n"
"buffer_store_dwordx4 a[72:75], v5, s[24:27], s85 offen offset:0 sc1\n"
"buffer_store_dwordx4 a[76:79], v5, s[24:27], s85 offen offset:1024 sc1\n"
"s_mul_i32 s64, s52, 81920\n"
"s_add_u32 s64, s64, s61\n"
"s_cmp_ge_u32 0, s52\n"
"s_cselect_b32 s42, 1, 0\n"
"s_mul_i32 s42, s42, 20480\n"
"s_add_u32 s42, s42, s64\n"
"s_cmp_ge_u32 1, s52\n"
"s_cselect_b32 s43, 2, 1\n"
"s_mul_i32 s43, s43, 20480\n"
"s_add_u32 s43, s43, s64\n"
"s_cmp_ge_u32 2, s52\n"
"s_cselect_b32 s44, 3, 2\n"
"s_mul_i32 s44, s44, 20480\n"
"s_add_u32 s44, s44, s64\n"
"v_and_b32 v9, 15, v4\n"
"v_lshrrev_b32 v10, 4, v4\n"
"v_mul_u32_u24 v9, 1280, v9\n"
"v_lshl_add_u32 v9, v10, 3, v9\n"
"s_mul_i32 s45, s50, 204800\n"
"s_mul_i32 s66, s53, 102400\n"
"s_add_u32 s45, s45, s66\n"
"s_lshl_b32 s66, s51, 8\n"
"s_add_u32 s45, s45, s66\n"
"s_lshl_b32 s66, s52, 6\n"
"s_add_u32 s45, s45, s66\n"
"s_and_b32 s65, s52, 1\n"
"s_cmp_eq_u32 s65, 1\n"
"s_cbranch_scc1 L_odd%=\n"
"v_accvgpr_read_b32 v136, a0\n"
"v_accvgpr_read_b32 v137, a1\n"
"v_accvgpr_read_b32 v138, a2\n"
"v_accvgpr_read_b32 v139, a3\n"
"v_accvgpr_read_b32 v140, a4\n"
"v_accvgpr_read_b32 v141, a5\n"
"v_accvgpr_read_b32 v142, a6\n"
"v_accvgpr_read_b32 v143, a7\n"
"v_accvgpr_read_b32 v144, a16\n"
"v_accvgpr_read_b32 v145, a17\n"
"v_accvgpr_read_b32 v146, a18\n"
"v_accvgpr_read_b32 v147, a19\n"
"v_accvgpr_read_b32 v148, a20\n"
"v_accvgpr_read_b32 v149, a21\n"
"v_accvgpr_read_b32 v150, a22\n"
"v_accvgpr_read_b32 v151, a23\n"
"v_accvgpr_read_b32 v152, a32\n"
"v_accvgpr_read_b32 v153, a33\n"
"v_accvgpr_read_b32 v154, a34\n"
"v_accvgpr_read_b32 v155, a35\n"
"v_accvgpr_read_b32 v156, a36\n"
"v_accvgpr_read_b32 v157, a37\n"
"v_accvgpr_read_b32 v158, a38\n"
"v_accvgpr_read_b32 v159, a39\n"
"v_accvgpr_read_b32 v160, a48\n"
"v_accvgpr_read_b32 v161, a49\n"
"v_accvgpr_read_b32 v162, a50\n"
"v_accvgpr_read_b32 v163, a51\n"
"v_accvgpr_read_b32 v164, a52\n"
"v_accvgpr_read_b32 v165, a53\n"
"v_accvgpr_read_b32 v166, a54\n"
"v_accvgpr_read_b32 v167, a55\n"
"v_accvgpr_read_b32 v168, a64\n"
"v_accvgpr_read_b32 v169, a65\n"
"v_accvgpr_read_b32 v170, a66\n"
"v_accvgpr_read_b32 v171, a67\n"
"v_accvgpr_read_b32 v172, a68\n"
"v_accvgpr_read_b32 v173, a69\n"
"v_accvgpr_read_b32 v174, a70\n"
"v_accvgpr_read_b32 v175, a71\n"
"s_branch L_sum%=\n"
"L_odd%=:\n"
"v_accvgpr_read_b32 v136, a8\n"
"v_accvgpr_read_b32 v137, a9\n"
"v_accvgpr_read_b32 v138, a10\n"
"v_accvgpr_read_b32 v139, a11\n"
"v_accvgpr_read_b32 v140, a12\n"
"v_accvgpr_read_b32 v141, a13\n"
"v_accvgpr_read_b32 v142, a14\n"
"v_accvgpr_read_b32 v143, a15\n"
"v_accvgpr_read_b32 v144, a24\n"
"v_accvgpr_read_b32 v145, a25\n"
"v_accvgpr_read_b32 v146, a26\n"
"v_accvgpr_read_b32 v147, a27\n"
"v_accvgpr_read_b32 v148, a28\n"
"v_accvgpr_read_b32 v149, a29\n"
"v_accvgpr_read_b32 v150, a30\n"
"v_accvgpr_read_b32 v151, a31\n"
"v_accvgpr_read_b32 v152, a40\n"
"v_accvgpr_read_b32 v153, a41\n"
"v_accvgpr_read_b32 v154, a42\n"
"v_accvgpr_read_b32 v155, a43\n"
"v_accvgpr_read_b32 v156, a44\n"
"v_accvgpr_read_b32 v157, a45\n"
"v_accvgpr_read_b32 v158, a46\n"
"v_accvgpr_read_b32 v159, a47\n"
"v_accvgpr_read_b32 v160, a56\n"
"v_accvgpr_read_b32 v161, a57\n"
"v_accvgpr_read_b32 v162, a58\n"
"v_accvgpr_read_b32 v163, a59\n"
"v_accvgpr_read_b32 v164, a60\n"
"v_accvgpr_read_b32 v165, a61\n"
"v_accvgpr_read_b32 v166, a62\n"
"v_accvgpr_read_b32 v167, a63\n"
"v_accvgpr_read_b32 v168, a72\n"
"v_accvgpr_read_b32 v169, a73\n"
"v_accvgpr_read_b32 v170, a74\n"
"v_accvgpr_read_b32 v171, a75\n"
"v_accvgpr_read_b32 v172, a76\n"
"v_accvgpr_read_b32 v173, a77\n"
"v_accvgpr_read_b32 v174, a78\n"
"v_accvgpr_read_b32 v175, a79\n"
"L_sum%=:\n"
"s_waitcnt vmcnt(0)\n"
"s_barrier\n"
"s_cmp_lg_u32 s40, 0\n"
"s_cbranch_scc1 L_nosig%=\n"
"s_lshl_b32 s65, %[blk], 7\n"
"s_add_u32 s65, s65, 58785792\n"
"v_mov_b32 v6, s65\n"
"v_mov_b32 v10, s94\n"
"v_mov_b32 v11, s95\n"
"s_mov_b64 exec, 1\n"
"global_store_dwordx2 v6, v[10:11], %[o] sc1\n"
"s_mov_b64 exec, -1\n"
"v_and_b32 v6, 3, v4\n"
"s_andn2_b32 s65, %[blk], 3\n"
"v_or_b32 v6, s65, v6\n"
"v_lshlrev_b32 v6, 7, v6\n"
"v_add_u32 v6, 58785792, v6\n"
"s_mov_b32 s66, 0\n"
"L_poll_x%=:\n"
"global_load_dwordx2 v[12:13], v6, %[o] sc1\n"
"s_waitcnt vmcnt(0)\n"
"v_cmp_ne_u64 vcc, s[94:95], v[12:13]\n"
"s_nop 1\n"
"s_add_u32 s66, s66, 1\n"
"s_cmp_eq_u64 vcc, 0\n"
"s_cbranch_scc1 L_polled_x%=\n"
"s_cmp_lt_u32 s66, 0x80000\n"
"s_cbranch_scc1 L_poll_x%=\n"
"L_polled_x%=:\n"
"L_nosig%=:\n"
"s_barrier\n"
"s_lshr_b32 s63, s52, 1\n"
"s_cmp_lg_u32 s63, s54\n"
"s_cbranch_scc1 L_done%=\n"
"buffer_load_dwordx4 v[16:19], v5, s[24:27], s42 offen offset:0 sc1\n"
"buffer_load_dwordx4 v[20:23], v5, s[24:27], s42 offen offset:1024 sc1\n"
"buffer_load_dwordx4 v[24:27], v5, s[24:27], s42 offen offset:2048 sc1\n"
"buffer_load_dwordx4 v[28:31], v5, s[24:27], s42 offen offset:3072 sc1\n"
"s_add_u32 s42, s42, 4096\n"
"buffer_load_dwordx4 v[32:35], v5, s[24:27], s42 offen offset:0 sc1\n"
"buffer_load_dwordx4 v[36:39], v5, s[24:27], s42 offen offset:1024 sc1\n"
"buffer_load_dwordx4 v[40:43], v5, s[24:27], s42 offen offset:2048 sc1\n"
"buffer_load_dwordx4 v[44:47], v5, s[24:27], s42 offen offset:3072 sc1\n"
"s_add_u32 s42, s42, 4096\n"
"buffer_load_dwordx4 v[48:51], v5, s[24:27], s42 offen offset:0 sc1\n"
"buffer_load_dwordx4 v[52:55], v5, s[24:27], s42 offen offset:1024 sc1\n"
"buffer_load_dwordx4 v[56:59], v5, s[24:27], s43 offen offset:0 sc1\n"
"buffer_load_dwordx4 v[60:63], v5, s[24:27], s43 offen offset:1024 sc1\n"
"buffer_load_dwordx4 v[64:67], v5, s[24:27], s43 offen offset:2048 sc1\n"
"buffer_load_dwordx4 v[68:71], v5, s[24:27], s43 offen offset:3072 sc1\n"
"s_add_u32 s43, s43, 4096\n"
"buffer_load_dwordx4 v[72:75], v5, s[24:27], s43 offen offset:0 sc1\n"
"buffer_load_dwordx4 v[76:79], v5, s[24:27], s43 offen offset:1024 sc1\n"
"buffer_load_dwordx4 v[80:83], v5, s[24:27], s43 offen offset:2048 sc1\n"
"buffer_load_dwordx4 v[84:87], v5, s[24:27], s43 offen offset:3072 sc1\n"
"s_add_u32 s43, s43, 4096\n"
"buffer_load_dwordx4 v[88:91], v5, s[24:27], s43 offen offset:0 sc1\n"
"buffer_load_dwordx4 v[92:95], v5, s[24:27], s43 offen offset:1024 sc1\n"
"buffer_load_dwordx4 v[96:99], v5, s[24:27], s44 offen offset:0 sc1\n"
"buffer_load_dwordx4 v[100:103], v5, s[24:27], s44 offen offset:1024 sc1\n"
"buffer_load_dwordx4 v[104:107], v5, s[24:27], s44 offen offset:2048 sc1\n"
"buffer_load_dwordx4 v[108:111], v5, s[24:27], s44 offen offset:3072 sc1\n"
"s_add_u32 s44, s44, 4096\n"
"buffer_load_dwordx4 v[112:115], v5, s[24:27], s44 offen offset:0 sc1\n"
"buffer_load_dwordx4 v[116:119], v5, s[24:27], s44 offen offset:1024 sc1\n"
"buffer_load_dwordx4 v[120:123], v5, s[24:27], s44 offen offset:2048 sc1\n"
"buffer_load_dwordx4 v[124:127], v5, s[24:27], s44 offen offset:3072 sc1\n"
"s_add_u32 s44, s44, 4096\n"
"buffer_load_dwordx4 v[128:131], v5, s[24:27], s44 offen offset:0 sc1\n"
"buffer_load_dwordx4 v[132:135], v5, s[24:27], s44 offen offset:1024 sc1\n"
"s_waitcnt vmcnt(0)\n"
"v_add_f32 v16, v16, v56\n"
"v_add_f32 v136, v136, v96\n"
"v_add_f32 v17, v17, v57\n"
"v_add_f32 v137, v137, v97\n"
"v_add_f32 v18, v18, v58\n"
"v_add_f32 v138, v138, v98\n"
"v_add_f32 v19, v19, v59\n"
"v_add_f32 v139, v139, v99\n"
"v_add_f32 v20, v20, v60\n"
"v_add_f32 v140, v140, v100\n"
"v_add_f32 v21, v21, v61\n"
"v_add_f32 v141, v141, v101\n"
"v_add_f32 v22, v22, v62\n"
"v_add_f32 v142, v142, v102\n"
"v_add_f32 v23, v23, v63\n"
"v_add_f32 v143, v143, v103\n"
"v_add_f32 v24, v24, v64\n"
"v_add_f32 v144, v144, v104\n"
"v_add_f32 v25, v25, v65\n"
"v_add_f32 v145, v145, v105\n"
"v_add_f32 v26, v26, v66\n"
"v_add_f32 v146, v146, v106\n"
"v_add_f32 v27, v27, v67\n"
"v_add_f32 v147, v147, v107\n"
"v_add_f32 v28, v28, v68\n"
"v_add_f32 v148, v148, v108\n"
"v_add_f32 v29, v29, v69\n"
"v_add_f32 v149, v149, v109\n"
"v_add_f32 v30, v30, v70\n"
"v_add_f32 v150, v150, v110\n"
"v_add_f32 v31, v31, v71\n"
"v_add_f32 v151, v151, v111\n"
"v_add_f32 v32, v32, v72\n"
"v_add_f32 v152, v152, v112\n"
"v_add_f32 v33, v33, v73\n"
"v_add_f32 v153, v153, v113\n"
"v_add_f32 v34, v34, v74\n"
"v_add_f32 v154, v154, v114\n"
"v_add_f32 v35, v35, v75\n"
"v_add_f32 v155, v155, v115\n"
"v_add_f32 v36, v36, v76\n"
"v_add_f32 v156, v156, v116\n"
"v_add_f32 v37, v37, v77\n"
"v_add_f32 v157, v157, v117\n"
"v_add_f32 v38, v38, v78\n"
"v_add_f32 v158, v158, v118\n"
"v_add_f32 v39, v39, v79\n"
"v_add_f32 v159, v159, v119\n"
"v_add_f32 v40, v40, v80\n"
"v_add_f32 v160, v160, v120\n"
"v_add_f32 v41, v41, v81\n"
"v_add_f32 v161, v161, v121\n"
"v_add_f32 v42, v42, v82\n"
"v_add_f32 v162, v162, v122\n"
"v_add_f32 v43, v43, v83\n"
"v_add_f32 v163, v163, v123\n"
"v_add_f32 v44, v44, v84\n"
"v_add_f32 v164, v164, v124\n"
"v_add_f32 v45, v45, v85\n"
"v_add_f32 v165, v165, v125\n"
"v_add_f32 v46, v46, v86\n"
"v_add_f32 v166, v166, v126\n"
"v_add_f32 v47, v47, v87\n"
"v_add_f32 v167, v167, v127\n"
"v_add_f32 v48, v48, v88\n"
"v_add_f32 v168, v168, v128\n"
"v_add_f32 v49, v49, v89\n"
"v_add_f32 v169, v169, v129\n"
"v_add_f32 v50, v50, v90\n"
"v_add_f32 v170, v170, v130\n"
"v_add_f32 v51, v51, v91\n"
"v_add_f32 v171, v171, v131\n"
"v_add_f32 v52, v52, v92\n"
"v_add_f32 v172, v172, v132\n"
"v_add_f32 v53, v53, v93\n"
"v_add_f32 v173, v173, v133\n"
"v_add_f32 v54, v54, v94\n"
"v_add_f32 v174, v174, v134\n"
"v_add_f32 v55, v55, v95\n"
"v_add_f32 v175, v175, v135\n"
"v_add_f32 v16, v16, v136\n"
"v_add_f32 v17, v17, v137\n"
"v_add_f32 v18, v18, v138\n"
"v_add_f32 v19, v19, v139\n"
"v_cvt_pk_bf16_f32 v176, v16, v17\n"
"v_cvt_pk_bf16_f32 v177, v18, v19\n"
"s_add_u32 s66, s45, 0\n"
"buffer_store_dwordx2 v[176:177], v9, s[28:31], s66 offen offset:0\n"
"v_add_f32 v20, v20, v140\n"
"v_add_f32 v21, v21, v141\n"
"v_add_f32 v22, v22, v142\n"
"v_add_f32 v23, v23, v143\n"
"v_cvt_pk_bf16_f32 v178, v20, v21\n"
"v_cvt_pk_bf16_f32 v179, v22, v23\n"
"s_add_u32 s66, s45, 0\n"
"buffer_store_dwordx2 v[178:179], v9, s[28:31], s66 offen offset:32\n"
"v_add_f32 v24, v24, v144\n"
"v_add_f32 v25, v25, v145\n"
"v_add_f32 v26, v26, v146\n"
"v_add_f32 v27, v27, v147\n"
"v_cvt_pk_bf16_f32 v180, v24, v25\n"
"v_cvt_pk_bf16_f32 v181, v26, v27\n"
"s_add_u32 s66, s45, 20480\n"
"buffer_store_dwordx2 v[180:181], v9, s[28:31], s66 offen offset:0\n"
"v_add_f32 v28, v28, v148\n"
"v_add_f32 v29, v29, v149\n"
"v_add_f32 v30, v30, v150\n"
"v_add_f32 v31, v31, v151\n"
"v_cvt_pk_bf16_f32 v182, v28, v29\n"
"v_cvt_pk_bf16_f32 v183, v30, v31\n"
"s_add_u32 s66, s45, 20480\n"
"buffer_store_dwordx2 v[182:183], v9, s[28:31], s66 offen offset:32\n"
"v_add_f32 v32, v32, v152\n"
"v_add_f32 v33, v33, v153\n"
"v_add_f32 v34, v34, v154\n"
"v_add_f32 v35, v35, v155\n"
"v_cvt_pk_bf16_f32 v184, v32, v33\n"
"v_cvt_pk_bf16_f32 v185, v34, v35\n"
"s_add_u32 s66, s45, 40960\n"
"buffer_store_dwordx2 v[184:185], v9, s[28:31], s66 offen offset:0\n"
"v_add_f32 v36, v36, v156\n"
"v_add_f32 v37, v37, v157\n"
"v_add_f32 v38, v38, v158\n"
"v_add_f32 v39, v39, v159\n"
"v_cvt_pk_bf16_f32 v186, v36, v37\n"
"v_cvt_pk_bf16_f32 v187, v38, v39\n"
"s_add_u32 s66, s45, 40960\n"
"buffer_store_dwordx2 v[186:187], v9, s[28:31], s66 offen offset:32\n"
"v_add_f32 v40, v40, v160\n"
"v_add_f32 v41, v41, v161\n"
"v_add_f32 v42, v42, v162\n"
"v_add_f32 v43, v43, v163\n"
"v_cvt_pk_bf16_f32 v188, v40, v41\n"
"v_cvt_pk_bf16_f32 v189, v42, v43\n"
"s_add_u32 s66, s45, 61440\n"
"buffer_store_dwordx2 v[188:189], v9, s[28:31], s66 offen offset:0\n"
"v_add_f32 v44, v44, v164\n"
"v_add_f32 v45, v45, v165\n"
"v_add_f32 v46, v46, v166\n"
"v_add_f32 v47, v47, v167\n"
"v_cvt_pk_bf16_f32 v190, v44, v45\n"
"v_cvt_pk_bf16_f32 v191, v46, v47\n"
"s_add_u32 s66, s45, 61440\n"
"buffer_store_dwordx2 v[190:191], v9, s[28:31], s66 offen offset:32\n"
"v_add_f32 v48, v48, v168\n"
"v_add_f32 v49, v49, v169\n"
"v_add_f32 v50, v50, v170\n"
"v_add_f32 v51, v51, v171\n"
"v_cvt_pk_bf16_f32 v192, v48, v49\n"
"v_cvt_pk_bf16_f32 v193, v50, v51\n"
"s_add_u32 s66, s45, 81920\n"
"buffer_store_dwordx2 v[192:193], v9, s[28:31], s66 offen offset:0\n"
"v_add_f32 v52, v52, v172\n"
"v_add_f32 v53, v53, v173\n"
"v_add_f32 v54, v54, v174\n"
"v_add_f32 v55, v55, v175\n"
"v_cvt_pk_bf16_f32 v194, v52, v53\n"
"v_cvt_pk_bf16_f32 v195, v54, v55\n"
"s_add_u32 s66, s45, 81920\n"
"buffer_store_dwordx2 v[194:195], v9, s[28:31], s66 offen offset:32\n"
"L_done%=:\n"
"L_end%=:\n"
    :
    : [x] "s"(x), [k] "s"(k), [v] "s"(v), [wq] "s"(wq), [wo] "s"(wo), [o] "s"(o), [out] "s"(out), [blk] "s"(blockIdx.x), [tid] "v"(threadIdx.x)
    : "memory", "vcc", "scc", "v4", "v5", "v6", "v7", "v8", "v9", "v10", "v11", "v12", "v13", "v14", "v15", "v16", "v17", "v18", "v19", "v20",
      "v21", "v22", "v23", "v24", "v25", "v26", "v27", "v28", "v29", "v30", "v31", "v32", "v33", "v34", "v35", "v36", "v37", "v38", "v39", "v40",
      "v41", "v42", "v43", "v44", "v45", "v46", "v47", "v48", "v49", "v50", "v51", "v52", "v53", "v54", "v55", "v56", "v57", "v58", "v59", "v60",
      "v61", "v62", "v63", "v64", "v65", "v66", "v67", "v68", "v69", "v70", "v71", "v72", "v73", "v74", "v75", "v76", "v77", "v78", "v79", "v80",
      "v81", "v82", "v83", "v84", "v85", "v86", "v87", "v88", "v89", "v90", "v91", "v92", "v93", "v94", "v95", "v96", "v97", "v98", "v99", "v100",
      "v101", "v102", "v103", "v104", "v105", "v106", "v107", "v108", "v109", "v110", "v111", "v112", "v113", "v114", "v115", "v116", "v117", "v118", "v119", "v120",
      "v121", "v122", "v123", "v124", "v125", "v126", "v127", "v128", "v129", "v130", "v131", "v132", "v133", "v134", "v135", "v136", "v137", "v138", "v139", "v140",
      "v141", "v142", "v143", "v144", "v145", "v146", "v147", "v148", "v149", "v150", "v151", "v152", "v153", "v154", "v155", "v156", "v157", "v158", "v159", "v160",
      "v161", "v162", "v163", "v164", "v165", "v166", "v167", "v168", "v169", "v170", "v171", "v172", "v173", "v174", "v175", "v176", "v177", "v178", "v179", "v180",
      "v181", "v182", "v183", "v184", "v185", "v186", "v187", "v188", "v189", "v190", "v191", "v192", "v193", "v194", "v195", "v196", "v197", "v198", "v199", "v200",
      "v201", "v202", "v203", "v204", "v205", "v206", "v207", "v208", "v209", "v210", "v211", "v212", "v213", "v214", "v215", "v216", "v217", "v218", "v219", "v220",
      "v221", "v222", "v223", "v224", "v225", "v226", "v227", "v228", "v229", "v230", "v231", "v232", "v233", "v234", "v235", "v236", "v237", "v238", "v239", "v240",
      "v241", "v242", "v243", "v244", "v245", "v246", "v247", "v248", "v249", "v250", "v251", "v252", "v253", "v254", "v255", "a0", "a1", "a2", "a3", "a4",
      "a5", "a6", "a7", "a8", "a9", "a10", "a11", "a12", "a13", "a14", "a15", "a16", "a17", "a18", "a19", "a20", "a21", "a22", "a23", "a24",
      "a25", "a26", "a27", "a28", "a29", "a30", "a31", "a32", "a33", "a34", "a35", "a36", "a37", "a38", "a39", "a40", "a41", "a42", "a43", "a44",
      "a45", "a46", "a47", "a48", "a49", "a50", "a51", "a52", "a53", "a54", "a55", "a56", "a57", "a58", "a59", "a60", "a61", "a62", "a63", "a64",
      "a65", "a66", "a67", "a68", "a69", "a70", "a71", "a72", "a73", "a74", "a75", "a76", "a77", "a78", "a79", "a80", "a81", "a82", "a83", "a84",
      "a85", "a86", "a87", "a88", "a89", "a90", "a91", "a92", "a93", "a94", "a95", "a96", "a97", "a98", "a99", "a100", "a101", "a102", "a103", "a104",
      "a105", "a106", "a107", "a108", "a109", "a110", "a111", "a112", "a113", "a114", "a115", "a116", "a117", "a118", "a119", "a120", "a121", "a122", "a123", "a124",
      "a125", "a126", "a127", "a128", "a129", "a130", "a131", "a132", "a133", "a134", "a135", "a136", "a137", "a138", "a139", "a140", "a141", "a142", "a143", "a144",
      "a145", "a146", "a147", "a148", "a149", "a150", "a151", "a152", "a153", "a154", "a155", "a156", "a157", "a158", "a159", "a160", "a161", "a162", "a163", "a164",
      "a165", "a166", "a167", "a168", "a169", "a170", "a171", "a172", "a173", "a174", "a175", "a176", "a177", "a178", "a179", "a180", "a181", "a182", "a183", "a184",
      "a185", "a186", "a187", "a188", "a189", "a190", "a191", "a192", "a193", "a194", "a195", "a196", "a197", "a198", "a199", "a200", "a201", "a202", "a203", "a204",
      "a205", "a206", "a207", "a208", "a209", "a210", "a211", "a212", "a213", "a214", "a215", "a216", "a217", "a218", "a219", "a220", "a221", "a222", "a223", "a224",
      "a225", "a226", "a227", "a228", "a229", "a230", "a231", "a232", "a233", "a234", "a235", "a236", "a237", "a238", "a239", "a240", "a241", "a242", "a243", "a244",
      "a245", "a246", "a247", "a248", "a249", "a250", "a251", "a252", "a253", "a254", "a255", "s16", "s17", "s18", "s19", "s20", "s21", "s22", "s23", "s24",
      "s25", "s26", "s27", "s28", "s29", "s30", "s31", "s32", "s33", "s34", "s35", "s36", "s37", "s38", "s39", "s40", "s41", "s42", "s43", "s44",
      "s45", "s46", "s47", "s48", "s49", "s50", "s51", "s52", "s53", "s54", "s55", "s56", "s57", "s58", "s59", "s60", "s61", "s62", "s63", "s64",
      "s65", "s66", "s67", "s68", "s69", "s70", "s71", "s72", "s73", "s74", "s75", "s76", "s77", "s78", "s79", "s80", "s81", "s82", "s83", "s84",
      "s85", "s86", "s87", "s88", "s89", "s90", "s91", "s92", "s93", "s94", "s95", "s96", "s97", "s98", "s99");
}

extern "C" void kernel_launch(void* const* d_in, const int* in_sizes, int n_in, void* d_out, int out_size, void* d_ws, size_t ws_size, hipStream_t stream) {
  const u16* x = (const u16*)d_in[0]; const u16* k = (const u16*)d_in[1]; const u16* v = (const u16*)d_in[2];
  const u16* wq = (const u16*)d_in[3]; const u16* wo = (const u16*)d_in[4];
  (void)hipFuncSetAttribute((const void*)k_fused, hipFuncAttributeMaxDynamicSharedMemorySize, 163840);
  k_fused<<<256, 256, 163840, stream>>>(x, k, v, wq, wo, (u16*)d_ws, (u16*)d_out);
}
```
